```python
import torch

def _softmax(x, axis=-1):
    return torch.softmax(x, dim=axis)
def _gelu(x):
    return torch.nn.functional.gelu(x, approximate="tanh")
def _silu(x):
    return torch.nn.functional.silu(x)
def _sigmoid(x):
    return torch.sigmoid(x)
def _layernorm(x, axis=-1, eps=1e-5):
    m = x.mean(dim=axis, keepdim=True)
    v = x.var(dim=axis, keepdim=True, unbiased=False)
    return (x - m) / torch.sqrt(v + eps)
def _scatter_add(tgt, idx, src, axis=0):
    return tgt.clone().index_add_(axis, idx, src)
def _segment_sum(src, seg, n):
    out = torch.zeros((n,) + src.shape[1:], dtype=src.dtype)
    return out.index_add_(0, seg, src)
def _pad(x, pw):
    flat = tuple(v for pair in reversed(pw) for v in pair)
    return torch.nn.functional.pad(x, flat)

kernel_name = 'recurrent_scan__sliding_block_cached__swiglu_ffn__prompt'

def setup_inputs():

    _w = (lambda a: (lambda t: t.to(torch.bfloat16) if t.is_floating_point() else t)(torch.as_tensor(a)))
    def _A_setup():
        B, Sq, D = 1, 2048, 640
        Hq, Hkv, Sk, Dh = 32, 32, 2048, 128
        assert Hq % Hkv == 0, (Hq, Hkv)
        return {
            "x": _w((lambda *s: torch.randn(*s, dtype=torch.bfloat16) * 0.05)(B, Sq, D)),
            "k_cache": _w((lambda *s: torch.randn(*s, dtype=torch.bfloat16) * 0.8)(B, Hkv, Sk, Dh)),
            "v_cache": _w((lambda *s: torch.randn(*s, dtype=torch.bfloat16) * 0.2)(B, Hkv, Sk, Dh)),
            "wq": _w((lambda *s: torch.randn(*s, dtype=torch.bfloat16) * 0.8)(D, Hq * Dh) * (D ** -0.5)),
            "wo": _w((lambda *s: torch.randn(*s, dtype=torch.bfloat16) * 0.2)(Hq * Dh, D) * ((Hq * Dh) ** -0.5)),
        }
    _A_in = _A_setup()

    def _B_setup():
        B, S, D, F = 1, 2048, 640, 1728
        return {
            "x": _w((lambda *s: torch.randn(*s, dtype=torch.bfloat16) * 0.05)(B, S, D)),
            "w_gate": _w((lambda *s: torch.randn(*s, dtype=torch.bfloat16) * 0.1)(D, F)),
            "w_up": _w((lambda *s: torch.randn(*s, dtype=torch.bfloat16) * 0.1)(D, F)),
            "w_down": _w((lambda *s: torch.randn(*s, dtype=torch.bfloat16) * 0.1)(F, D)),
        }
    _B_in = _B_setup()

    return {
        "A_x": _A_in["x"],
        "A_k_cache": _A_in["k_cache"],
        "A_v_cache": _A_in["v_cache"],
        "A_wq": _A_in["wq"],
        "A_wo": _A_in["wo"],
        "B_x": _B_in["x"],
        "B_w_gate": _B_in["w_gate"],
        "B_w_up": _B_in["w_up"],
        "B_w_down": _B_in["w_down"],
    }

def reference(A_x, A_k_cache, A_v_cache, A_wq, A_wo, B_x, B_w_gate, B_w_up, B_w_down):
    def _A_ref(x, k_cache, v_cache, wq, wo):
        B, Sq, D = x.shape
        Hq, Dh = 32, 128
        Hkv, Sk = k_cache.shape[1], k_cache.shape[2]
        W = min(4096, max(256, Sk // 4))
        rep = Hq // Hkv
        q = (lambda x, *d: x.permute(*d))((x @ wq).reshape(B, Sq, Hq, Dh), 0, 2, 1, 3)
        k = (lambda x, n, axis: torch.repeat_interleave(x, n, dim=axis))(k_cache[:, :, Sk - W:, :], rep, axis=1)
        v = (lambda x, n, axis: torch.repeat_interleave(x, n, dim=axis))(v_cache[:, :, Sk - W:, :], rep, axis=1)
        scores = torch.einsum("bhqd,bhkd->bhqk", q, k) * (Dh ** -0.5)
        p = _softmax(scores, axis=-1)
        o = torch.einsum("bhqk,bhkd->bhqd", p, v)
        return (lambda x, *d: x.permute(*d))(o, 0, 2, 1, 3).reshape(B, Sq, Hq * Dh) @ wo

    def _B_ref(x, w_gate, w_up, w_down):
        g = _silu(x @ w_gate)
        u = x @ w_up
        return (g * u) @ w_down

    x = A_x
    xn = _layernorm(x, axis=-1)
    s = _A_ref(xn, A_k_cache, A_v_cache, A_wq, A_wo)
    g = _silu(_B_ref(xn, B_w_gate, B_w_up, B_w_down))
    return x + s * g
```

```cpp
#include <hip/hip_runtime.h>
#include <hip/hip_bf16.h>
#include <stdint.h>
typedef unsigned short u16;
typedef short bf16x8 __attribute__((ext_vector_type(8)));
typedef float f32x4 __attribute__((ext_vector_type(4)));
__device__ __forceinline__ float bf2f(u16 x){ return __uint_as_float(((unsigned)x)<<16); }
__device__ __forceinline__ u16 f2bf(float f){ unsigned u=__float_as_uint(f); u += 0x7fffu + ((u>>16)&1u); return (u16)(u>>16); }
__device__ __forceinline__ float rbf(float f){ return bf2f(f2bf(f)); }

constexpr int S=2048, D=640, H=32, DH=128, SK=2048, W=512, F=1728;

__global__ void ln_k(const u16* x, u16* xn){
  int row=blockIdx.x; int lane=threadIdx.x;
  float v[10]; float s=0;
  for(int i=0;i<10;i++){ v[i]=bf2f(x[row*D+lane+64*i]); s+=v[i]; }
  for(int o=32;o;o>>=1) s+=__shfl_xor(s,o);
  float m=s/D; float q=0;
  for(int i=0;i<10;i++){ float d=v[i]-m; q+=d*d; }
  for(int o=32;o;o>>=1) q+=__shfl_xor(q,o);
  float var=rbf(q/D); float mb=rbf(m);
  float den=rbf(sqrtf(rbf(var+1e-5f)));
  for(int i=0;i<10;i++){ xn[row*D+lane+64*i]=f2bf(rbf(v[i]-mb)/den); }
}
__global__ void gemm_k(const u16* A, const u16* B, u16* C, int M, int N, int K){
  __shared__ u16 As[64][40]; __shared__ u16 Bs[64][40];
  int tid=threadIdx.x, wid=tid>>6, lane=tid&63; int m0=blockIdx.y*64, n0=blockIdx.x*64;
  int wr=wid>>1, wc=wid&1;
  f32x4 acc[2][2]={};
  for(int k0=0;k0<K;k0+=32){
    { int r=tid>>2, c=(tid&3)*8; *(bf16x8*)&As[r][c]=*(const bf16x8*)&A[(size_t)(m0+r)*K+k0+c]; }
    { int kk=tid>>3, c=(tid&7)*8; bf16x8 v=*(const bf16x8*)&B[(size_t)(k0+kk)*N+n0+c]; for(int j=0;j<8;j++) Bs[c+j][kk]=(u16)v[j]; }
    __syncthreads();
    bf16x8 a[2],b[2];
    for(int i=0;i<2;i++){ a[i]=*(bf16x8*)&As[wr*32+i*16+(lane&15)][(lane>>4)*8]; b[i]=*(bf16x8*)&Bs[wc*32+i*16+(lane&15)][(lane>>4)*8]; }
    for(int i=0;i<2;i++)for(int j=0;j<2;j++) acc[i][j]=__builtin_amdgcn_mfma_f32_16x16x32_bf16(a[i],b[j],acc[i][j],0,0,0);
    __syncthreads();
  }
  for(int i=0;i<2;i++)for(int j=0;j<2;j++)for(int r=0;r<4;r++)
    C[(size_t)(m0+wr*32+i*16+(lane>>4)*4+r)*N+n0+wc*32+j*16+(lane&15)]=f2bf(acc[i][j][r]);
}
__global__ void attn_k(const u16* q, const u16* kc, const u16* vc, u16* o){
  __shared__ float sc[16][W]; __shared__ float qs[16][DH];
  int h=blockIdx.y, q0=blockIdx.x*16, tid=threadIdx.x;
  for(int i=tid;i<16*DH;i+=256) qs[i/DH][i%DH]=bf2f(q[(size_t)(q0+i/DH)*(H*DH)+h*DH+i%DH]);
  __syncthreads();
  const u16* K=kc+((size_t)h*SK+(SK-W))*DH; const u16* V=vc+((size_t)h*SK+(SK-W))*DH;
  for(int j=tid;j<W;j+=256){
    float acc[16]={};
    for(int d=0;d<DH;d++){ float kv=bf2f(K[j*DH+d]); for(int r=0;r<16;r++) acc[r]+=qs[r][d]*kv; }
    for(int r=0;r<16;r++) sc[r][j]=acc[r]*0.08838834764831845f;
  }
  __syncthreads();
  int wid=tid>>6, lane=tid&63;
  for(int r=wid;r<16;r+=4){
    float m=-1e30f; for(int j=lane;j<W;j+=64) m=fmaxf(m,sc[r][j]);
    for(int of=32;of;of>>=1) m=fmaxf(m,__shfl_xor(m,of));
    float s=0; for(int j=lane;j<W;j+=64){ float e=__expf(sc[r][j]-m); sc[r][j]=e; s+=e; }
    for(int of=32;of;of>>=1) s+=__shfl_xor(s,of);
    float inv=1.f/s; for(int j=lane;j<W;j+=64) sc[r][j]=rbf(sc[r][j]*inv);
  }
  __syncthreads();
  int d=tid&127, rh=tid>>7;
  float acc[8]={};
  for(int j=0;j<W;j++){ float vv=bf2f(V[j*DH+d]); for(int r=0;r<8;r++) acc[r]+=sc[rh*8+r][j]*vv; }
  for(int r=0;r<8;r++) o[(size_t)(q0+rh*8+r)*(H*DH)+h*DH+d]=f2bf(acc[r]);
}
__global__ void swiglu_k(const u16* g, const u16* u, u16* h, int n){
  int i=blockIdx.x*256+threadIdx.x; if(i<n){ float a=bf2f(g[i]); float s=rbf(a/(1.f+__expf(-a))); h[i]=f2bf(s*bf2f(u[i])); }
}
__global__ void final_k(const u16* x, const u16* s, const u16* f, u16* out, int n){
  int i=blockIdx.x*256+threadIdx.x; if(i<n){ float a=bf2f(f[i]); float g=rbf(a/(1.f+__expf(-a))); out[i]=f2bf(bf2f(x[i])+rbf(bf2f(s[i])*g)); }
}
extern "C" void kernel_launch(void* const* d_in, const int* in_sizes, int n_in, void* d_out, int out_size, void* d_ws, size_t ws_size, hipStream_t stream){
  const u16* x=(const u16*)d_in[0]; const u16* kc=(const u16*)d_in[1]; const u16* vc=(const u16*)d_in[2];
  const u16* wq=(const u16*)d_in[3]; const u16* wo=(const u16*)d_in[4];
  const u16* wg=(const u16*)d_in[6]; const u16* wu=(const u16*)d_in[7]; const u16* wd=(const u16*)d_in[8];
  u16* ws=(u16*)d_ws;
  u16* xn=ws; u16* q=xn+S*D; u16* o=q+S*H*DH; u16* g=o+S*H*DH; u16* u=g+S*F; u16* hh=u+S*F; u16* s=hh+S*F; u16* f=s+S*D;
  ln_k<<<S,64,0,stream>>>(x,xn);
  gemm_k<<<dim3(H*DH/64,S/64),256,0,stream>>>(xn,wq,q,S,H*DH,D);
  gemm_k<<<dim3(F/64,S/64),256,0,stream>>>(xn,wg,g,S,F,D);
  gemm_k<<<dim3(F/64,S/64),256,0,stream>>>(xn,wu,u,S,F,D);
  attn_k<<<dim3(S/16,H),256,0,stream>>>(q,kc,vc,o);
  swiglu_k<<<(S*F+255)/256,256,0,stream>>>(g,u,hh,S*F);
  gemm_k<<<dim3(D/64,S/64),256,0,stream>>>(o,wo,s,S,D,H*DH);
  gemm_k<<<dim3(D/64,S/64),256,0,stream>>>(hh,wd,f,S,D,F);
  final_k<<<(S*D+255)/256,256,0,stream>>>(x,s,f,(u16*)d_out,S*D);
}
```

```cpp
#include <hip/hip_runtime.h>
#include <hip/hip_bf16.h>

__global__ __launch_bounds__(256, 1) __attribute__((aligned(4096))) void fused_block_kernel(const void* x, const void* kc, const void* vc, const void* wq, const void* wo,
                                                             const void* wg, const void* wu, void* ws, const void* wd, void* out) {
  unsigned tid = threadIdx.x;
  asm volatile(
".L_start:\n"
"s_load_dwordx16 s[40:55], %1, 0\n"
"s_mov_b32 s35, %2\n"
"s_getpc_b64 s[60:61]\n"
"s_and_b32 s60, s60, 0xffffff80\n"
"s_and_b32 s61, s61, 0xffff\n"
"s_mov_b32 s62, .L_end-.L_start\n"
"s_mov_b32 s63, 0x20000\n"
"v_lshrrev_b32 v1, 4, v0\n"
"v_lshlrev_b32 v1, 7, v1\n"
"v_lshrrev_b32 v2, 6, v0\n"
"s_lshr_b32 s90, s35, 3\n"
"s_lshl_b32 s90, s90, 11\n"
"buffer_load_dword v3, v1, s[60:63], s90 offen\n"
"s_add_u32 s90, s90, 65536\n"
"buffer_load_dword v3, v1, s[60:63], s90 offen\n"
"v_min_u32 v225, 1, v0\n"
"v_lshlrev_b32 v225, 30, v225\n"
"v_readfirstlane_b32 s68, v2\n"
"v_and_b32 v229, 63, v0\n"
"s_mov_b32 s89, 0\n"
"s_mov_b32 s74, 0\n"
".L_redo:\n"
"s_mov_b32 s63, 0x20000\n"
"s_and_b32 s71, s35, 3\n"
"s_bfe_u32 s90, s35, 0x10002\n"
"s_lshr_b32 s91, s35, 6\n"
"s_lshl2_add_u32 s69, s90, s91\n"
"s_bfe_u32 s72, s35, 0x30003\n"
"s_lshl3_add_u32 s70, s71, s72\n"
"s_lshl_b32 s90, s69, 5\n"
"s_add_u32 s75, s90, s70\n"
"s_lshl_b32 s90, s69, 8\n"
"s_lshl_b32 s91, s68, 6\n"
"s_add_u32 s87, s90, s91\n"
"s_lshr_b32 s33, s69, 2\n"
"s_xor_b32 s33, s33, s71\n"
"s_and_b32 s33, s33, 1\n"
"s_mul_i32 s76, s87, 1280\n"
"s_lshl_b32 s80, s68, 17\n"
"s_mul_i32 s90, s33, 640\n"
"s_add_u32 s76, s76, s90\n"
"s_mul_i32 s90, s33, 2621440\n"
"s_add_u32 s80, s80, s90\n"
"s_add_u32 s77, s76, 20480\n"
"s_add_u32 s78, s77, 20480\n"
"s_add_u32 s79, s78, 20480\n"
"s_lshl_b32 s81, s68, 11\n"
"v_lshlrev_b32 v255, 4, v229\n"
"v_lshrrev_b32 v217, 3, v229\n"
"v_and_b32 v218, 7, v229\n"
"v_and_b32 v219, 2, v217\n"
"v_xor_b32 v218, v218, v219\n"
"v_mul_u32_u24 v217, 8192, v217\n"
"v_lshl_add_u32 v252, v218, 4, v217\n"
"v_xor_b32 v253, 64, v252\n"
"v_add_u32 v253, 65536, v253\n"
"v_lshrrev_b32 v217, 3, v229\n"
"v_and_b32 v218, 7, v229\n"
"v_lshrrev_b32 v219, 1, v217\n"
"v_xor_b32 v218, v218, v219\n"
"v_mul_u32_u24 v217, 1280, v217\n"
"v_lshl_add_u32 v254, v218, 4, v217\n"
"v_xor_b32 v226, 64, v254\n"
"v_add_u32 v226, 10240, v226\n"
"s_lshl_b32 s58, s68, 13\n"
"s_lshl_b32 s94, s70, 8\n"
"s_add_u32 s95, s94, 128\n"
"s_sub_u32 s18, 5242880, s94\n"
"s_sub_u32 s22, s18, 128\n"
"s_mov_b32 s14, 2621440\n"
"s_mov_b32 s38, 67108864\n"
"s_mov_b32 s15, s63\n"
"s_mov_b32 s19, s63\n"
"s_mov_b32 s23, s63\n"
"s_mov_b32 s39, s63\n"
"v_and_b32 v212, 15, v229\n"
"v_lshrrev_b32 v213, 4, v229\n"
"v_bfe_u32 v214, v229, 2, 2\n"
"v_and_b32 v215, 3, v229\n"
"v_lshrrev_b32 v217, 1, v214\n"
"v_and_b32 v218, 1, v213\n"
"v_lshl_or_b32 v216, v218, 1, v217\n"
"v_lshl_add_u32 v217, v213, 3, v214\n"
"v_lshlrev_b32 v217, 7, v217\n"
"v_lshlrev_b32 v218, 1, v216\n"
"v_xor_b32 v218, v218, v215\n"
"v_lshl_add_u32 v217, v218, 4, v217\n"
"s_and_b32 s91, s68, 1\n"
"s_lshl_b32 s91, s91, 6\n"
"s_lshr_b32 s90, s68, 1\n"
"s_lshl_b32 s92, s90, 13\n"
"s_or_b32 s92, s91, s92\n"
"v_xor_b32 v244, s92, v217\n"
"v_xor_b32 v245, 64, v244\n"
"v_xor_b32 v246, 8192, v244\n"
"v_xor_b32 v247, 8256, v244\n"
"v_lshrrev_b32 v218, 1, v212\n"
"v_xor_b32 v218, v218, v213\n"
"v_lshlrev_b32 v218, 4, v218\n"
"v_lshl_add_u32 v219, v212, 7, v218\n"
"s_add_u32 s90, s58, 49152\n"
"v_add_u32 v248, s90, v219\n"
"v_xor_b32 v249, 64, v248\n"
"v_mov_b32 v208, 0x3f803f80\n"
"v_mov_b32 v209, 0x3f803f80\n"
"v_mov_b32 v210, 0x3f803f80\n"
"v_mov_b32 v211, 0x3f803f80\n"
"v_lshrrev_b32 v217, 5, v229\n"
"v_and_b32 v219, 31, v229\n"
"v_mul_u32_u24 v217, 1280, v217\n"
"v_lshl_add_u32 v190, v219, 3, v217\n"
"s_lshl_b32 s90, s75, 3\n"
"s_lshl1_add_u32 s90, s68, s90\n"
"s_mul_i32 s96, s90, 1280\n"
"s_lshl_b32 s99, s90, 3\n"
"s_add_u32 s99, s99, 8192\n"
"v_mov_b32 v191, 0\n"
"s_waitcnt lgkmcnt(0)\n"
"s_add_u32 s16, s46, s94\n"
"s_addc_u32 s17, s47, 0\n"
"s_add_u32 s20, s46, s95\n"
"s_addc_u32 s21, s47, 0\n"
"s_mov_b64 s[12:13], s[40:41]\n"
"s_mov_b64 s[36:37], s[54:55]\n"
"s_load_dwordx2 s[54:55], %1, 64\n"
"s_load_dwordx2 s[56:57], %1, 72\n"
"s_add_u32 m0, s81, 0\n"
"s_nop 0\n"
"buffer_load_dwordx4 v252, s[16:19], s80 offen lds\n"
"s_add_u32 m0, s81, 1024\n"
"s_nop 0\n"
"buffer_load_dwordx4 v253, s[16:19], s80 offen lds\n"
"s_add_u32 m0, s81, 8192\n"
"s_nop 0\n"
"buffer_load_dwordx4 v252, s[20:23], s80 offen lds\n"
"s_add_u32 m0, s81, 9216\n"
"s_nop 0\n"
"buffer_load_dwordx4 v253, s[20:23], s80 offen lds\n"
"s_add_u32 s80, s80, 524288\n"
"s_add_u32 m0, s58, 49152\n"
"s_nop 0\n"
"buffer_load_dwordx4 v254, s[12:15], s76 offen offset:0 lds\n"
"s_add_u32 m0, s58, 50176\n"
"s_nop 0\n"
"buffer_load_dwordx4 v226, s[12:15], s76 offen offset:0 lds\n"
"s_add_u32 m0, s58, 51200\n"
"s_nop 0\n"
"buffer_load_dwordx4 v254, s[12:15], s77 offen offset:0 lds\n"
"s_add_u32 m0, s58, 52224\n"
"s_nop 0\n"
"buffer_load_dwordx4 v226, s[12:15], s77 offen offset:0 lds\n"
"s_add_u32 m0, s58, 53248\n"
"s_nop 0\n"
"buffer_load_dwordx4 v254, s[12:15], s78 offen offset:0 lds\n"
"s_mov_b64 exec, 0\n"
"s_cmp_eq_u32 s68, 0\n"
"s_cbranch_scc1 .L_pad1\n"
"s_cmp_eq_u32 s68, 1\n"
"s_cbranch_scc1 .L_pad2\n"
"s_cmp_eq_u32 s68, 2\n"
"s_cbranch_scc1 .L_pad3\n"
"s_branch .L_pad4\n"
".L_padret0:\n"
"s_mov_b64 exec, -1\n"
"s_add_u32 m0, s58, 54272\n"
"s_nop 0\n"
"buffer_load_dwordx4 v226, s[12:15], s78 offen offset:0 lds\n"
"s_add_u32 m0, s58, 55296\n"
"s_nop 0\n"
"buffer_load_dwordx4 v254, s[12:15], s79 offen offset:0 lds\n"
"s_add_u32 m0, s58, 56320\n"
"s_nop 0\n"
"buffer_load_dwordx4 v226, s[12:15], s79 offen offset:0 lds\n"
"s_add_u32 m0, s81, 16384\n"
"s_nop 0\n"
"buffer_load_dwordx4 v252, s[16:19], s80 offen lds\n"
"s_add_u32 m0, s81, 17408\n"
"s_nop 0\n"
"buffer_load_dwordx4 v253, s[16:19], s80 offen lds\n"
"s_add_u32 m0, s81, 24576\n"
"s_nop 0\n"
"buffer_load_dwordx4 v252, s[20:23], s80 offen lds\n"
"s_add_u32 m0, s81, 25600\n"
"s_nop 0\n"
"buffer_load_dwordx4 v253, s[20:23], s80 offen lds\n"
"s_add_u32 s80, s80, 524288\n"
"s_add_u32 m0, s58, 81792\n"
"s_nop 0\n"
"buffer_load_dwordx4 v254, s[12:15], s76 offen offset:128 lds\n"
"s_add_u32 m0, s58, 82816\n"
"s_nop 0\n"
"buffer_load_dwordx4 v226, s[12:15], s76 offen offset:128 lds\n"
"s_mov_b64 exec, 0\n"
"s_cmp_eq_u32 s68, 0\n"
"s_cbranch_scc1 .L_pad9\n"
"s_cmp_eq_u32 s68, 1\n"
"s_cbranch_scc1 .L_pad10\n"
"s_cmp_eq_u32 s68, 2\n"
"s_cbranch_scc1 .L_pad11\n"
"s_branch .L_pad12\n"
".L_padret1:\n"
"s_mov_b64 exec, -1\n"
"s_add_u32 m0, s58, 83840\n"
"s_nop 0\n"
"buffer_load_dwordx4 v254, s[12:15], s77 offen offset:128 lds\n"
"s_add_u32 m0, s58, 84864\n"
"s_nop 0\n"
"buffer_load_dwordx4 v226, s[12:15], s77 offen offset:128 lds\n"
"s_add_u32 m0, s58, 85888\n"
"s_nop 0\n"
"buffer_load_dwordx4 v254, s[12:15], s78 offen offset:128 lds\n"
"s_add_u32 m0, s58, 86912\n"
"s_nop 0\n"
"buffer_load_dwordx4 v226, s[12:15], s78 offen offset:128 lds\n"
"s_add_u32 m0, s58, 87936\n"
"s_nop 0\n"
"buffer_load_dwordx4 v254, s[12:15], s79 offen offset:128 lds\n"
"s_add_u32 m0, s58, 88960\n"
"s_nop 0\n"
"buffer_load_dwordx4 v226, s[12:15], s79 offen offset:128 lds\n"
"buffer_load_dwordx2 v[136:137], v190, s[12:15], s96 offen offset:0\n"
"buffer_load_dwordx2 v[138:139], v190, s[12:15], s96 offen offset:256\n"
"buffer_load_dwordx2 v[140:141], v190, s[12:15], s96 offen offset:512\n"
"buffer_load_dwordx2 v[142:143], v190, s[12:15], s96 offen offset:768\n"
"buffer_load_dwordx2 v[144:145], v190, s[12:15], s96 offen offset:1024\n"
"buffer_load_dword v191, v191, s[36:39], 0 offen sc1\n"
"s_waitcnt vmcnt(18) lgkmcnt(0)\n"
"s_barrier\n"
"ds_read_b64_tr_b16 a[0:1], v244 offset:0\n"
"ds_read_b64_tr_b16 a[2:3], v244 offset:512\n"
"ds_read_b64_tr_b16 a[4:5], v244 offset:4096\n"
"ds_read_b64_tr_b16 a[6:7], v244 offset:4608\n"
"ds_read_b128 a[32:35], v248 offset:0\n"
"ds_read_b128 a[36:39], v249 offset:0\n"
"ds_read_b128 a[40:43], v248 offset:2048\n"
"ds_read_b128 a[44:47], v249 offset:2048\n"
"ds_read_b128 a[48:51], v248 offset:4096\n"
"ds_read_b128 a[52:55], v249 offset:4096\n"
"ds_read_b128 a[56:59], v248 offset:6144\n"
"ds_read_b128 a[60:63], v249 offset:6144\n"
"ds_read_b64_tr_b16 a[8:9], v244 offset:8\n"
"ds_read_b64_tr_b16 a[10:11], v244 offset:520\n"
"ds_read_b64_tr_b16 a[12:13], v244 offset:4104\n"
"ds_read_b64_tr_b16 a[14:15], v244 offset:4616\n"
"s_waitcnt lgkmcnt(10)\n"
"v_mfma_f32_16x16x32_bf16 v[0:3], a[0:3], a[32:35], 0\n"
"s_add_u32 m0, s58, 130816\n"
"ds_read_b64_tr_b16 a[16:17], v245 offset:0\n"
"v_mfma_f32_16x16x32_bf16 v[0:3], a[4:7], a[36:39], v[0:3]\n"
"s_waitcnt lgkmcnt(9)\n"
"ds_read_b64_tr_b16 a[18:19], v245 offset:512\n"
"buffer_load_dwordx4 v254, s[12:15], s76 offen offset:256 lds\n"
"v_mfma_f32_16x16x32_bf16 v[4:7], a[4:7], a[44:47], 0\n"
"s_add_u32 m0, s58, 131840\n"
"ds_read_b64_tr_b16 a[20:21], v245 offset:4096\n"
"v_mfma_f32_16x16x32_bf16 v[4:7], a[0:3], a[40:43], v[4:7]\n"
"s_waitcnt lgkmcnt(9)\n"
"ds_read_b64_tr_b16 a[22:23], v245 offset:4608\n"
"v_mfma_f32_16x16x32_bf16 v[8:11], a[0:3], a[48:51], 0\n"
"ds_read_b64_tr_b16 a[24:25], v245 offset:8\n"
"v_mfma_f32_16x16x32_bf16 v[8:11], a[4:7], a[52:55], v[8:11]\n"
"s_waitcnt lgkmcnt(9)\n"
"ds_read_b64_tr_b16 a[26:27], v245 offset:520\n"
"buffer_load_dwordx4 v226, s[12:15], s76 offen offset:256 lds\n"
"v_mfma_f32_16x16x32_bf16 v[12:15], a[4:7], a[60:63], 0\n"
"s_add_u32 m0, s58, 132864\n"
"ds_read_b64_tr_b16 a[28:29], v245 offset:4104\n"
"v_mfma_f32_16x16x32_bf16 v[12:15], a[0:3], a[56:59], v[12:15]\n"
"ds_read_b64_tr_b16 a[30:31], v245 offset:4616\n"
"v_mfma_f32_16x16x32_bf16 v[128:131], a[0:3], v[208:211], 0\n"
"v_mfma_f32_16x16x32_bf16 v[128:131], a[4:7], v[208:211], v[128:131]\n"
"s_waitcnt lgkmcnt(8)\n"
"buffer_load_dwordx4 v254, s[12:15], s77 offen offset:256 lds\n"
"v_mfma_f32_16x16x32_bf16 v[16:19], a[8:11], a[32:35], 0\n"
"s_add_u32 m0, s58, 133888\n"
"v_mfma_f32_16x16x32_bf16 v[16:19], a[12:15], a[36:39], v[16:19]\n"
"v_mfma_f32_16x16x32_bf16 v[20:23], a[12:15], a[44:47], 0\n"
"v_mfma_f32_16x16x32_bf16 v[20:23], a[8:11], a[40:43], v[20:23]\n"
"buffer_load_dwordx4 v226, s[12:15], s77 offen offset:256 lds\n"
"v_mfma_f32_16x16x32_bf16 v[24:27], a[8:11], a[48:51], 0\n"
"s_add_u32 m0, s58, 134912\n"
"v_mfma_f32_16x16x32_bf16 v[24:27], a[12:15], a[52:55], v[24:27]\n"
"v_mfma_f32_16x16x32_bf16 v[28:31], a[12:15], a[60:63], 0\n"
"v_mfma_f32_16x16x32_bf16 v[28:31], a[8:11], a[56:59], v[28:31]\n"
"buffer_load_dwordx4 v254, s[12:15], s78 offen offset:256 lds\n"
"v_mfma_f32_16x16x32_bf16 v[132:135], a[8:11], v[208:211], 0\n"
"s_add_u32 m0, s58, 135936\n"
"v_mfma_f32_16x16x32_bf16 v[132:135], a[12:15], v[208:211], v[132:135]\n"
"s_waitcnt lgkmcnt(0)\n"
"v_mfma_f32_16x16x32_bf16 v[32:35], a[16:19], a[32:35], 0\n"
"ds_read_b64_tr_b16 a[0:1], v246 offset:0\n"
"v_add_u32 v204, 114688, v244\n"
"v_mfma_f32_16x16x32_bf16 v[32:35], a[20:23], a[36:39], v[32:35]\n"
"ds_read_b64_tr_b16 a[2:3], v246 offset:512\n"
"buffer_load_dwordx4 v226, s[12:15], s78 offen offset:256 lds\n"
"v_mfma_f32_16x16x32_bf16 v[36:39], a[20:23], a[44:47], 0\n"
"s_add_u32 m0, s58, 136960\n"
"ds_read_b64_tr_b16 a[4:5], v246 offset:4096\n"
"v_mfma_f32_16x16x32_bf16 v[36:39], a[16:19], a[40:43], v[36:39]\n"
"ds_read_b64_tr_b16 a[6:7], v246 offset:4608\n"
"v_mfma_f32_16x16x32_bf16 v[40:43], a[16:19], a[48:51], 0\n"
"ds_read_b64_tr_b16 a[8:9], v246 offset:8\n"
"v_mfma_f32_16x16x32_bf16 v[40:43], a[20:23], a[52:55], v[40:43]\n"
"ds_read_b64_tr_b16 a[10:11], v246 offset:520\n"
"buffer_load_dwordx4 v254, s[12:15], s79 offen offset:256 lds\n"
"v_mfma_f32_16x16x32_bf16 v[44:47], a[20:23], a[60:63], 0\n"
"s_add_u32 m0, s58, 137984\n"
"ds_read_b64_tr_b16 a[12:13], v246 offset:4104\n"
"v_mfma_f32_16x16x32_bf16 v[44:47], a[16:19], a[56:59], v[44:47]\n"
"ds_read_b64_tr_b16 a[14:15], v246 offset:4616\n"
"v_mfma_f32_16x16x32_bf16 v[48:51], a[24:27], a[32:35], 0\n"
"s_waitcnt vmcnt(13)\n"
"ds_read_b128 a[64:67], v248 offset:32768\n"
"v_add_u32 v205, 114688, v245\n"
"v_mfma_f32_16x16x32_bf16 v[48:51], a[28:31], a[36:39], v[48:51]\n"
"ds_read_b128 a[68:71], v249 offset:32768\n"
"buffer_load_dwordx4 v226, s[12:15], s79 offen offset:256 lds\n"
"v_mfma_f32_16x16x32_bf16 v[52:55], a[28:31], a[44:47], 0\n"
"s_add_u32 m0, s81, 114688\n"
"ds_read_b128 a[72:75], v248 offset:34816\n"
"v_mfma_f32_16x16x32_bf16 v[52:55], a[24:27], a[40:43], v[52:55]\n"
"ds_read_b128 a[76:79], v249 offset:34816\n"
"v_mfma_f32_16x16x32_bf16 v[56:59], a[24:27], a[48:51], 0\n"
"ds_read_b128 a[80:83], v248 offset:36864\n"
"v_mfma_f32_16x16x32_bf16 v[56:59], a[28:31], a[52:55], v[56:59]\n"
"ds_read_b128 a[84:87], v249 offset:36864\n"
"buffer_load_dwordx4 v252, s[16:19], s80 offen lds\n"
"v_mfma_f32_16x16x32_bf16 v[60:63], a[28:31], a[60:63], 0\n"
"s_add_u32 m0, s81, 115712\n"
"v_mfma_f32_16x16x32_bf16 v[60:63], a[24:27], a[56:59], v[60:63]\n"
"s_waitcnt lgkmcnt(6)\n"
"v_mfma_f32_16x16x32_bf16 v[64:67], a[0:3], a[32:35], 0\n"
"ds_read_b64_tr_b16 a[16:17], v247 offset:0\n"
"v_add_u32 v206, 114688, v246\n"
"v_mfma_f32_16x16x32_bf16 v[64:67], a[4:7], a[36:39], v[64:67]\n"
"ds_read_b64_tr_b16 a[18:19], v247 offset:512\n"
"buffer_load_dwordx4 v253, s[16:19], s80 offen lds\n"
"v_mfma_f32_16x16x32_bf16 v[68:71], a[4:7], a[44:47], 0\n"
"s_add_u32 m0, s81, 122880\n"
"ds_read_b64_tr_b16 a[20:21], v247 offset:4096\n"
"v_mfma_f32_16x16x32_bf16 v[68:71], a[0:3], a[40:43], v[68:71]\n"
"ds_read_b64_tr_b16 a[22:23], v247 offset:4608\n"
"v_mfma_f32_16x16x32_bf16 v[72:75], a[0:3], a[48:51], 0\n"
"ds_read_b64_tr_b16 a[24:25], v247 offset:8\n"
"v_mfma_f32_16x16x32_bf16 v[72:75], a[4:7], a[52:55], v[72:75]\n"
"ds_read_b64_tr_b16 a[26:27], v247 offset:520\n"
"buffer_load_dwordx4 v252, s[20:23], s80 offen lds\n"
"v_mfma_f32_16x16x32_bf16 v[76:79], a[4:7], a[60:63], 0\n"
"s_add_u32 m0, s81, 123904\n"
"ds_read_b64_tr_b16 a[28:29], v247 offset:4104\n"
"v_mfma_f32_16x16x32_bf16 v[76:79], a[0:3], a[56:59], v[76:79]\n"
"ds_read_b64_tr_b16 a[30:31], v247 offset:4616\n"
"v_mfma_f32_16x16x32_bf16 v[80:83], a[8:11], a[32:35], 0\n"
"ds_read_b128 a[88:91], v248 offset:38912\n"
"v_add_u32 v207, 114688, v247\n"
"v_mfma_f32_16x16x32_bf16 v[80:83], a[12:15], a[36:39], v[80:83]\n"
"ds_read_b128 a[92:95], v249 offset:38912\n"
"buffer_load_dwordx4 v253, s[20:23], s80 offen lds\n"
"v_mfma_f32_16x16x32_bf16 v[84:87], a[12:15], a[44:47], 0\n"
"s_add_u32 s80, s80, 524288\n"
"s_add_u32 m0, s58, 48768\n"
"v_mfma_f32_16x16x32_bf16 v[84:87], a[8:11], a[40:43], v[84:87]\n"
"v_mfma_f32_16x16x32_bf16 v[88:91], a[8:11], a[48:51], 0\n"
"v_mfma_f32_16x16x32_bf16 v[88:91], a[12:15], a[52:55], v[88:91]\n"
"buffer_load_dwordx4 v254, s[12:15], s76 offen offset:384 lds\n"
"v_mfma_f32_16x16x32_bf16 v[92:95], a[12:15], a[60:63], 0\n"
"s_add_u32 m0, s58, 49792\n"
"v_mfma_f32_16x16x32_bf16 v[92:95], a[8:11], a[56:59], v[92:95]\n"
"s_waitcnt lgkmcnt(0)\n"
"v_mfma_f32_16x16x32_bf16 v[96:99], a[16:19], a[32:35], 0\n"
"s_nop 0\n"
"s_barrier\n"
"v_add_u32 v250, 81920, v248\n"
"v_mfma_f32_16x16x32_bf16 v[96:99], a[20:23], a[36:39], v[96:99]\n"
"ds_read_b64_tr_b16 a[0:1], v244 offset:16384\n"
"buffer_load_dwordx4 v226, s[12:15], s76 offen offset:384 lds\n"
"v_mfma_f32_16x16x32_bf16 v[100:103], a[20:23], a[44:47], 0\n"
"s_add_u32 m0, s58, 50816\n"
"ds_read_b64_tr_b16 a[2:3], v244 offset:16896\n"
"v_mfma_f32_16x16x32_bf16 v[100:103], a[16:19], a[40:43], v[100:103]\n"
"ds_read_b64_tr_b16 a[4:5], v244 offset:20480\n"
"v_mfma_f32_16x16x32_bf16 v[104:107], a[16:19], a[48:51], 0\n"
"ds_read_b64_tr_b16 a[6:7], v244 offset:20992\n"
"v_mfma_f32_16x16x32_bf16 v[104:107], a[20:23], a[52:55], v[104:107]\n"
"ds_read_b64_tr_b16 a[8:9], v244 offset:16392\n"
"buffer_load_dwordx4 v254, s[12:15], s77 offen offset:384 lds\n"
"v_mfma_f32_16x16x32_bf16 v[108:111], a[20:23], a[60:63], 0\n"
"s_add_u32 m0, s58, 51840\n"
"ds_read_b64_tr_b16 a[10:11], v244 offset:16904\n"
"v_mfma_f32_16x16x32_bf16 v[108:111], a[16:19], a[56:59], v[108:111]\n"
"ds_read_b64_tr_b16 a[12:13], v244 offset:20488\n"
"v_mfma_f32_16x16x32_bf16 v[112:115], a[24:27], a[32:35], 0\n"
"ds_read_b64_tr_b16 a[14:15], v244 offset:21000\n"
"v_add_u32 v251, 81920, v249\n"
"v_mfma_f32_16x16x32_bf16 v[112:115], a[28:31], a[36:39], v[112:115]\n"
"buffer_load_dwordx4 v226, s[12:15], s77 offen offset:384 lds\n"
"v_mfma_f32_16x16x32_bf16 v[116:119], a[28:31], a[44:47], 0\n"
"s_add_u32 m0, s58, 52864\n"
"v_mfma_f32_16x16x32_bf16 v[116:119], a[24:27], a[40:43], v[116:119]\n"
"v_mfma_f32_16x16x32_bf16 v[120:123], a[24:27], a[48:51], 0\n"
"v_mfma_f32_16x16x32_bf16 v[120:123], a[28:31], a[52:55], v[120:123]\n"
"buffer_load_dwordx4 v254, s[12:15], s78 offen offset:384 lds\n"
"v_mfma_f32_16x16x32_bf16 v[124:127], a[28:31], a[60:63], 0\n"
"s_add_u32 m0, s58, 53888\n"
"v_mfma_f32_16x16x32_bf16 v[124:127], a[24:27], a[56:59], v[124:127]\n"
"s_waitcnt lgkmcnt(0)\n"
"v_mfma_f32_16x16x32_bf16 v[0:3], a[0:3], a[64:67], v[0:3]\n"
"s_waitcnt vmcnt(17)\n"
"ds_read_b64_tr_b16 a[16:17], v245 offset:16384\n"
"v_lshlrev_b32 v146, 16, v136\n"
"v_mfma_f32_16x16x32_bf16 v[0:3], a[4:7], a[68:71], v[0:3]\n"
"ds_read_b64_tr_b16 a[18:19], v245 offset:16896\n"
"buffer_load_dwordx4 v226, s[12:15], s78 offen offset:384 lds\n"
"v_mfma_f32_16x16x32_bf16 v[4:7], a[4:7], a[76:79], v[4:7]\n"
"s_add_u32 m0, s58, 54912\n"
"ds_read_b64_tr_b16 a[20:21], v245 offset:20480\n"
"v_and_b32 v147, 0xffff0000, v136\n"
"v_mfma_f32_16x16x32_bf16 v[4:7], a[0:3], a[72:75], v[4:7]\n"
"ds_read_b64_tr_b16 a[22:23], v245 offset:20992\n"
"v_lshlrev_b32 v148, 16, v137\n"
"v_mfma_f32_16x16x32_bf16 v[8:11], a[0:3], a[80:83], v[8:11]\n"
"ds_read_b64_tr_b16 a[24:25], v245 offset:16392\n"
"v_and_b32 v149, 0xffff0000, v137\n"
"v_mfma_f32_16x16x32_bf16 v[8:11], a[4:7], a[84:87], v[8:11]\n"
"ds_read_b64_tr_b16 a[26:27], v245 offset:16904\n"
"buffer_load_dwordx4 v254, s[12:15], s79 offen offset:384 lds\n"
"v_mfma_f32_16x16x32_bf16 v[12:15], a[4:7], a[92:95], v[12:15]\n"
"s_add_u32 m0, s58, 55936\n"
"ds_read_b64_tr_b16 a[28:29], v245 offset:20488\n"
"v_lshlrev_b32 v150, 16, v138\n"
"v_mfma_f32_16x16x32_bf16 v[12:15], a[0:3], a[88:91], v[12:15]\n"
"ds_read_b64_tr_b16 a[30:31], v245 offset:21000\n"
"v_and_b32 v151, 0xffff0000, v138\n"
"v_mfma_f32_16x16x32_bf16 v[128:131], a[0:3], v[208:211], v[128:131]\n"
"v_lshlrev_b32 v152, 16, v139\n"
"v_and_b32 v153, 0xffff0000, v139\n"
"v_mfma_f32_16x16x32_bf16 v[128:131], a[4:7], v[208:211], v[128:131]\n"
"buffer_load_dwordx4 v226, s[12:15], s79 offen offset:384 lds\n"
"v_lshlrev_b32 v154, 16, v140\n"
"v_mfma_f32_16x16x32_bf16 v[16:19], a[8:11], a[64:67], v[16:19]\n"
"s_add_u32 m0, s81, 32768\n"
"v_and_b32 v155, 0xffff0000, v140\n"
"v_lshlrev_b32 v156, 16, v141\n"
"v_mfma_f32_16x16x32_bf16 v[16:19], a[12:15], a[68:71], v[16:19]\n"
"v_and_b32 v157, 0xffff0000, v141\n"
"v_lshlrev_b32 v158, 16, v142\n"
"v_mfma_f32_16x16x32_bf16 v[20:23], a[12:15], a[76:79], v[20:23]\n"
"v_and_b32 v159, 0xffff0000, v142\n"
"v_lshlrev_b32 v160, 16, v143\n"
"v_mfma_f32_16x16x32_bf16 v[20:23], a[8:11], a[72:75], v[20:23]\n"
"buffer_load_dwordx4 v252, s[16:19], s80 offen lds\n"
"v_and_b32 v161, 0xffff0000, v143\n"
"v_mfma_f32_16x16x32_bf16 v[24:27], a[8:11], a[80:83], v[24:27]\n"
"s_add_u32 m0, s81, 33792\n"
"v_lshlrev_b32 v162, 16, v144\n"
"v_and_b32 v163, 0xffff0000, v144\n"
"v_mfma_f32_16x16x32_bf16 v[24:27], a[12:15], a[84:87], v[24:27]\n"
"v_lshlrev_b32 v164, 16, v145\n"
"v_and_b32 v165, 0xffff0000, v145\n"
"v_mfma_f32_16x16x32_bf16 v[28:31], a[12:15], a[92:95], v[28:31]\n"
"v_readfirstlane_b32 s98, v191\n"
"v_add_f32 v166, v146, v147\n"
"v_mfma_f32_16x16x32_bf16 v[28:31], a[8:11], a[88:91], v[28:31]\n"
"buffer_load_dwordx4 v253, s[16:19], s80 offen lds\n"
"v_add_f32 v167, v148, v149\n"
"v_mfma_f32_16x16x32_bf16 v[132:135], a[8:11], v[208:211], v[132:135]\n"
"s_add_u32 m0, s81, 40960\n"
"v_add_f32 v166, v166, v150\n"
"v_add_f32 v167, v167, v151\n"
"v_mfma_f32_16x16x32_bf16 v[132:135], a[12:15], v[208:211], v[132:135]\n"
"s_waitcnt lgkmcnt(0)\n"
"v_add_f32 v166, v166, v152\n"
"v_add_f32 v167, v167, v153\n"
"v_mfma_f32_16x16x32_bf16 v[32:35], a[16:19], a[64:67], v[32:35]\n"
"ds_read_b64_tr_b16 a[0:1], v246 offset:16384\n"
"v_add_f32 v166, v166, v154\n"
"v_mfma_f32_16x16x32_bf16 v[32:35], a[20:23], a[68:71], v[32:35]\n"
"ds_read_b64_tr_b16 a[2:3], v246 offset:16896\n"
"buffer_load_dwordx4 v252, s[20:23], s80 offen lds\n"
"v_mfma_f32_16x16x32_bf16 v[36:39], a[20:23], a[76:79], v[36:39]\n"
"s_add_u32 m0, s81, 41984\n"
"ds_read_b64_tr_b16 a[4:5], v246 offset:20480\n"
"v_add_f32 v167, v167, v155\n"
"v_mfma_f32_16x16x32_bf16 v[36:39], a[16:19], a[72:75], v[36:39]\n"
"ds_read_b64_tr_b16 a[6:7], v246 offset:20992\n"
"v_add_f32 v166, v166, v156\n"
"v_mfma_f32_16x16x32_bf16 v[40:43], a[16:19], a[80:83], v[40:43]\n"
"ds_read_b64_tr_b16 a[8:9], v246 offset:16392\n"
"v_add_f32 v167, v167, v157\n"
"v_mfma_f32_16x16x32_bf16 v[40:43], a[20:23], a[84:87], v[40:43]\n"
"ds_read_b64_tr_b16 a[10:11], v246 offset:16904\n"
"buffer_load_dwordx4 v253, s[20:23], s80 offen lds\n"
"v_mfma_f32_16x16x32_bf16 v[44:47], a[20:23], a[92:95], v[44:47]\n"
"s_add_u32 s80, s80, 524288\n"
"ds_read_b64_tr_b16 a[12:13], v246 offset:20488\n"
"v_add_f32 v166, v166, v158\n"
"v_mfma_f32_16x16x32_bf16 v[44:47], a[16:19], a[88:91], v[44:47]\n"
"s_add_u32 m0, s58, 81408\n"
"ds_read_b64_tr_b16 a[14:15], v246 offset:21000\n"
"v_add_f32 v167, v167, v159\n"
"v_mfma_f32_16x16x32_bf16 v[48:51], a[24:27], a[64:67], v[48:51]\n"
"s_waitcnt vmcnt(16)\n"
"ds_read_b128 a[96:99], v250 offset:0\n"
"v_add_f32 v166, v166, v160\n"
"v_mfma_f32_16x16x32_bf16 v[48:51], a[28:31], a[68:71], v[48:51]\n"
"ds_read_b128 a[100:103], v251 offset:0\n"
"v_add_f32 v167, v167, v161\n"
"v_mfma_f32_16x16x32_bf16 v[52:55], a[28:31], a[76:79], v[52:55]\n"
"ds_read_b128 a[104:107], v250 offset:2048\n"
"buffer_load_dwordx4 v254, s[12:15], s76 offen offset:512 lds\n"
"v_mfma_f32_16x16x32_bf16 v[52:55], a[24:27], a[72:75], v[52:55]\n"
"s_add_u32 m0, s58, 82432\n"
"ds_read_b128 a[108:111], v251 offset:2048\n"
"v_add_f32 v166, v166, v162\n"
"v_mfma_f32_16x16x32_bf16 v[56:59], a[24:27], a[80:83], v[56:59]\n"
"ds_read_b128 a[112:115], v250 offset:4096\n"
"v_add_f32 v167, v167, v163\n"
"v_mfma_f32_16x16x32_bf16 v[56:59], a[28:31], a[84:87], v[56:59]\n"
"ds_read_b128 a[116:119], v251 offset:4096\n"
"v_add_f32 v166, v166, v164\n"
"v_mfma_f32_16x16x32_bf16 v[60:63], a[28:31], a[92:95], v[60:63]\n"
"v_add_f32 v167, v167, v165\n"
"v_mfma_f32_16x16x32_bf16 v[60:63], a[24:27], a[88:91], v[60:63]\n"
"s_waitcnt lgkmcnt(6)\n"
"s_andn2_b32 s98, s98, s89\n"
"buffer_load_dwordx4 v226, s[12:15], s76 offen offset:512 lds\n"
"v_mfma_f32_16x16x32_bf16 v[64:67], a[0:3], a[64:67], v[64:67]\n"
"s_add_u32 m0, s58, 83456\n"
"s_and_b32 s74, s74, s89\n"
"ds_read_b64_tr_b16 a[16:17], v247 offset:16384\n"
"v_mfma_f32_16x16x32_bf16 v[64:67], a[4:7], a[68:71], v[64:67]\n"
"s_or_b32 s74, s74, s98\n"
"ds_read_b64_tr_b16 a[18:19], v247 offset:16896\n"
"v_add_f32 v166, v166, v167\n"
"v_mfma_f32_16x16x32_bf16 v[68:71], a[4:7], a[76:79], v[68:71]\n"
"s_mul_i32 s73, s74, 0x9E3779B1\n"
"s_add_u32 s73, s73, 0x7F4A7C15\n"
"ds_read_b64_tr_b16 a[20:21], v247 offset:20480\n"
"v_mfma_f32_16x16x32_bf16 v[68:71], a[0:3], a[72:75], v[68:71]\n"
"ds_read_b64_tr_b16 a[22:23], v247 offset:20992\n"
"v_mfma_f32_16x16x32_bf16 v[72:75], a[0:3], a[80:83], v[72:75]\n"
"ds_read_b64_tr_b16 a[24:25], v247 offset:16392\n"
"buffer_load_dwordx4 v254, s[12:15], s77 offen offset:512 lds\n"
"v_mfma_f32_16x16x32_bf16 v[72:75], a[4:7], a[84:87], v[72:75]\n"
"s_add_u32 m0, s58, 84480\n"
"ds_read_b64_tr_b16 a[26:27], v247 offset:16904\n"
"v_add_f32_dpp v166, v166, v166 quad_perm:[1,0,3,2] row_mask:0xf bank_mask:0xf\n"
"v_mfma_f32_16x16x32_bf16 v[76:79], a[4:7], a[92:95], v[76:79]\n"
"ds_read_b64_tr_b16 a[28:29], v247 offset:20488\n"
"v_mfma_f32_16x16x32_bf16 v[76:79], a[0:3], a[88:91], v[76:79]\n"
"ds_read_b64_tr_b16 a[30:31], v247 offset:21000\n"
"v_add_f32_dpp v166, v166, v166 quad_perm:[2,3,0,1] row_mask:0xf bank_mask:0xf\n"
"v_mfma_f32_16x16x32_bf16 v[80:83], a[8:11], a[64:67], v[80:83]\n"
"ds_read_b128 a[120:123], v250 offset:6144\n"
"v_mfma_f32_16x16x32_bf16 v[80:83], a[12:15], a[68:71], v[80:83]\n"
"ds_read_b128 a[124:127], v251 offset:6144\n"
"buffer_load_dwordx4 v226, s[12:15], s77 offen offset:512 lds\n"
"v_mfma_f32_16x16x32_bf16 v[84:87], a[12:15], a[76:79], v[84:87]\n"
"s_add_u32 m0, s58, 85504\n"
"v_add_f32_dpp v166, v166, v166 row_half_mirror row_mask:0xf bank_mask:0xf\n"
"v_mfma_f32_16x16x32_bf16 v[84:87], a[8:11], a[72:75], v[84:87]\n"
"v_mfma_f32_16x16x32_bf16 v[88:91], a[8:11], a[80:83], v[88:91]\n"
"v_add_f32_dpp v166, v166, v166 row_mirror row_mask:0xf bank_mask:0xf\n"
"v_mov_b32 v167, v166\n"
"v_mfma_f32_16x16x32_bf16 v[88:91], a[12:15], a[84:87], v[88:91]\n"
"v_mfma_f32_16x16x32_bf16 v[92:95], a[12:15], a[92:95], v[92:95]\n"
"buffer_load_dwordx4 v254, s[12:15], s78 offen offset:512 lds\n"
"v_permlane16_swap_b32 v167, v166\n"
"v_mfma_f32_16x16x32_bf16 v[92:95], a[8:11], a[88:91], v[92:95]\n"
"s_waitcnt lgkmcnt(0)\n"
"s_add_u32 m0, s58, 86528\n"
"v_add_f32 v166, v166, v167\n"
"v_mfma_f32_16x16x32_bf16 v[96:99], a[16:19], a[64:67], v[96:99]\n"
"s_waitcnt vmcnt(17)\n"
"s_barrier\n"
"v_mul_f32 v166, 0x3acccccd, v166\n"
"v_mfma_f32_16x16x32_bf16 v[96:99], a[20:23], a[68:71], v[96:99]\n"
"ds_read_b64_tr_b16 a[0:1], v204 offset:0\n"
"v_sub_f32 v170, v146, v166\n"
"v_mfma_f32_16x16x32_bf16 v[100:103], a[20:23], a[76:79], v[100:103]\n"
"ds_read_b64_tr_b16 a[2:3], v204 offset:512\n"
"v_sub_f32 v171, v147, v166\n"
"v_mfma_f32_16x16x32_bf16 v[100:103], a[16:19], a[72:75], v[100:103]\n"
"ds_read_b64_tr_b16 a[4:5], v204 offset:4096\n"
"buffer_load_dwordx4 v226, s[12:15], s78 offen offset:512 lds\n"
"v_mfma_f32_16x16x32_bf16 v[104:107], a[16:19], a[80:83], v[104:107]\n"
"s_add_u32 m0, s58, 87552\n"
"ds_read_b64_tr_b16 a[6:7], v204 offset:4608\n"
"v_sub_f32 v172, v148, v166\n"
"v_mfma_f32_16x16x32_bf16 v[104:107], a[20:23], a[84:87], v[104:107]\n"
"ds_read_b64_tr_b16 a[8:9], v204 offset:8\n"
"v_sub_f32 v173, v149, v166\n"
"v_mfma_f32_16x16x32_bf16 v[108:111], a[20:23], a[92:95], v[108:111]\n"
"ds_read_b64_tr_b16 a[10:11], v204 offset:520\n"
"v_sub_f32 v174, v150, v166\n"
"v_mfma_f32_16x16x32_bf16 v[108:111], a[16:19], a[88:91], v[108:111]\n"
"ds_read_b64_tr_b16 a[12:13], v204 offset:4104\n"
"v_sub_f32 v175, v151, v166\n"
"v_mfma_f32_16x16x32_bf16 v[112:115], a[24:27], a[64:67], v[112:115]\n"
"ds_read_b64_tr_b16 a[14:15], v204 offset:4616\n"
"buffer_load_dwordx4 v254, s[12:15], s79 offen offset:512 lds\n"
"v_mfma_f32_16x16x32_bf16 v[112:115], a[28:31], a[68:71], v[112:115]\n"
"s_add_u32 m0, s58, 88576\n"
"v_sub_f32 v176, v152, v166\n"
"v_sub_f32 v177, v153, v166\n"
"v_mfma_f32_16x16x32_bf16 v[116:119], a[28:31], a[76:79], v[116:119]\n"
"v_sub_f32 v178, v154, v166\n"
"v_sub_f32 v179, v155, v166\n"
"v_mfma_f32_16x16x32_bf16 v[116:119], a[24:27], a[72:75], v[116:119]\n"
"v_sub_f32 v180, v156, v166\n"
"v_sub_f32 v181, v157, v166\n"
"v_mfma_f32_16x16x32_bf16 v[120:123], a[24:27], a[80:83], v[120:123]\n"
".L_pad1:\n"
"s_cbranch_execz .L_pad5\n"
"v_sub_f32 v182, v158, v166\n"
"v_sub_f32 v183, v159, v166\n"
"v_mfma_f32_16x16x32_bf16 v[120:123], a[28:31], a[84:87], v[120:123]\n"
"buffer_load_dwordx4 v226, s[12:15], s79 offen offset:512 lds\n"
"v_sub_f32 v184, v160, v166\n"
"v_mfma_f32_16x16x32_bf16 v[124:127], a[28:31], a[92:95], v[124:127]\n"
"s_add_u32 m0, s81, 0\n"
"v_sub_f32 v185, v161, v166\n"
"v_sub_f32 v186, v162, v166\n"
"v_mfma_f32_16x16x32_bf16 v[124:127], a[24:27], a[88:91], v[124:127]\n"
"s_waitcnt lgkmcnt(0)\n"
"v_sub_f32 v187, v163, v166\n"
"v_sub_f32 v188, v164, v166\n"
"v_mfma_f32_16x16x32_bf16 v[0:3], a[0:3], a[96:99], v[0:3]\n"
"ds_read_b64_tr_b16 a[16:17], v205 offset:0\n"
"v_sub_f32 v189, v165, v166\n"
"v_mfma_f32_16x16x32_bf16 v[0:3], a[4:7], a[100:103], v[0:3]\n"
"ds_read_b64_tr_b16 a[18:19], v205 offset:512\n"
"v_mul_f32 v167, v170, v170\n"
"v_mfma_f32_16x16x32_bf16 v[4:7], a[4:7], a[108:111], v[4:7]\n"
"ds_read_b64_tr_b16 a[20:21], v205 offset:4096\n"
"buffer_load_dwordx4 v252, s[16:19], s80 offen lds\n"
"v_mfma_f32_16x16x32_bf16 v[4:7], a[0:3], a[104:107], v[4:7]\n"
"s_add_u32 m0, s81, 1024\n"
"ds_read_b64_tr_b16 a[22:23], v205 offset:4608\n"
"v_mul_f32 v168, v171, v171\n"
"v_mfma_f32_16x16x32_bf16 v[8:11], a[0:3], a[112:115], v[8:11]\n"
"ds_read_b64_tr_b16 a[24:25], v205 offset:8\n"
"v_fmac_f32 v167, v172, v172\n"
"v_mfma_f32_16x16x32_bf16 v[8:11], a[4:7], a[116:119], v[8:11]\n"
"ds_read_b64_tr_b16 a[26:27], v205 offset:520\n"
"v_fmac_f32 v168, v173, v173\n"
"v_mfma_f32_16x16x32_bf16 v[12:15], a[4:7], a[124:127], v[12:15]\n"
"ds_read_b64_tr_b16 a[28:29], v205 offset:4104\n"
"v_fmac_f32 v167, v174, v174\n"
"v_mfma_f32_16x16x32_bf16 v[12:15], a[0:3], a[120:123], v[12:15]\n"
"ds_read_b64_tr_b16 a[30:31], v205 offset:4616\n"
"buffer_load_dwordx4 v253, s[16:19], s80 offen lds\n"
"v_mfma_f32_16x16x32_bf16 v[128:131], a[0:3], v[208:211], v[128:131]\n"
"s_add_u32 m0, s81, 8192\n"
"v_fmac_f32 v168, v175, v175\n"
"v_fmac_f32 v167, v176, v176\n"
"v_mfma_f32_16x16x32_bf16 v[128:131], a[4:7], v[208:211], v[128:131]\n"
"v_fmac_f32 v168, v177, v177\n"
"v_fmac_f32 v167, v178, v178\n"
"v_mfma_f32_16x16x32_bf16 v[16:19], a[8:11], a[96:99], v[16:19]\n"
"v_fmac_f32 v168, v179, v179\n"
"v_fmac_f32 v167, v180, v180\n"
"v_mfma_f32_16x16x32_bf16 v[16:19], a[12:15], a[100:103], v[16:19]\n"
"v_fmac_f32 v168, v181, v181\n"
"v_fmac_f32 v167, v182, v182\n"
"v_mfma_f32_16x16x32_bf16 v[20:23], a[12:15], a[108:111], v[20:23]\n"
"buffer_load_dwordx4 v252, s[20:23], s80 offen lds\n"
"v_fmac_f32 v168, v183, v183\n"
"v_mfma_f32_16x16x32_bf16 v[20:23], a[8:11], a[104:107], v[20:23]\n"
"s_add_u32 m0, s81, 9216\n"
"v_fmac_f32 v167, v184, v184\n"
"v_fmac_f32 v168, v185, v185\n"
"v_mfma_f32_16x16x32_bf16 v[24:27], a[8:11], a[112:115], v[24:27]\n"
"v_fmac_f32 v167, v186, v186\n"
"v_fmac_f32 v168, v187, v187\n"
"v_mfma_f32_16x16x32_bf16 v[24:27], a[12:15], a[116:119], v[24:27]\n"
"v_fmac_f32 v167, v188, v188\n"
"v_fmac_f32 v168, v189, v189\n"
"v_mfma_f32_16x16x32_bf16 v[28:31], a[12:15], a[124:127], v[28:31]\n"
"v_add_f32 v167, v167, v168\n"
"v_mfma_f32_16x16x32_bf16 v[28:31], a[8:11], a[120:123], v[28:31]\n"
"buffer_load_dwordx4 v253, s[20:23], s80 offen lds\n"
"v_mfma_f32_16x16x32_bf16 v[132:135], a[8:11], v[208:211], v[132:135]\n"
"s_add_u32 s80, s80, 524288\n"
"s_mul_i32 s86, s33, 5242880\n"
"s_sub_u32 s80, s80, s86\n"
"v_mfma_f32_16x16x32_bf16 v[132:135], a[12:15], v[208:211], v[132:135]\n"
"s_waitcnt lgkmcnt(0)\n"
"s_mul_i32 s86, s33, 1280\n"
"s_sub_u32 s86, 640, s86\n"
"v_mfma_f32_16x16x32_bf16 v[32:35], a[16:19], a[96:99], v[32:35]\n"
"s_add_u32 s76, s76, s86\n"
"s_add_u32 s77, s77, s86\n"
"ds_read_b64_tr_b16 a[0:1], v206 offset:0\n"
"v_mfma_f32_16x16x32_bf16 v[32:35], a[20:23], a[100:103], v[32:35]\n"
"s_add_u32 s78, s78, s86\n"
"s_add_u32 s79, s79, s86\n"
"ds_read_b64_tr_b16 a[2:3], v206 offset:512\n"
"v_mfma_f32_16x16x32_bf16 v[36:39], a[20:23], a[108:111], v[36:39]\n"
"s_add_u32 m0, s58, 131072\n"
"ds_read_b64_tr_b16 a[4:5], v206 offset:4096\n"
"v_add_f32_dpp v167, v167, v167 quad_perm:[1,0,3,2] row_mask:0xf bank_mask:0xf\n"
"v_mfma_f32_16x16x32_bf16 v[36:39], a[16:19], a[104:107], v[36:39]\n"
"ds_read_b64_tr_b16 a[6:7], v206 offset:4608\n"
"buffer_load_dwordx4 v254, s[12:15], s76 offen offset:0 lds\n"
"v_mfma_f32_16x16x32_bf16 v[40:43], a[16:19], a[112:115], v[40:43]\n"
"s_add_u32 m0, s58, 132096\n"
"ds_read_b64_tr_b16 a[8:9], v206 offset:8\n"
"v_add_f32_dpp v167, v167, v167 quad_perm:[2,3,0,1] row_mask:0xf bank_mask:0xf\n"
"v_mfma_f32_16x16x32_bf16 v[40:43], a[20:23], a[116:119], v[40:43]\n"
"ds_read_b64_tr_b16 a[10:11], v206 offset:520\n"
"v_mfma_f32_16x16x32_bf16 v[44:47], a[20:23], a[124:127], v[44:47]\n"
"ds_read_b64_tr_b16 a[12:13], v206 offset:4104\n"
"v_add_f32_dpp v167, v167, v167 row_half_mirror row_mask:0xf bank_mask:0xf\n"
"v_mfma_f32_16x16x32_bf16 v[44:47], a[16:19], a[120:123], v[44:47]\n"
"ds_read_b64_tr_b16 a[14:15], v206 offset:4616\n"
"v_mfma_f32_16x16x32_bf16 v[48:51], a[24:27], a[96:99], v[48:51]\n"
"s_waitcnt vmcnt(17)\n"
"ds_read_b128 a[32:35], v248 offset:0\n"
"buffer_load_dwordx4 v226, s[12:15], s76 offen offset:0 lds\n"
"v_mfma_f32_16x16x32_bf16 v[48:51], a[28:31], a[100:103], v[48:51]\n"
"s_add_u32 m0, s58, 133120\n"
"ds_read_b128 a[36:39], v249 offset:0\n"
"v_add_f32_dpp v167, v167, v167 row_mirror row_mask:0xf bank_mask:0xf\n"
"v_mfma_f32_16x16x32_bf16 v[52:55], a[28:31], a[108:111], v[52:55]\n"
"ds_read_b128 a[40:43], v248 offset:2048\n"
"v_mov_b32 v168, v167\n"
"v_mfma_f32_16x16x32_bf16 v[52:55], a[24:27], a[104:107], v[52:55]\n"
"ds_read_b128 a[44:47], v249 offset:2048\n"
"v_mfma_f32_16x16x32_bf16 v[56:59], a[24:27], a[112:115], v[56:59]\n"
"ds_read_b128 a[48:51], v248 offset:4096\n"
"v_permlane16_swap_b32 v168, v167\n"
"v_mfma_f32_16x16x32_bf16 v[56:59], a[28:31], a[116:119], v[56:59]\n"
"ds_read_b128 a[52:55], v249 offset:4096\n"
"buffer_load_dwordx4 v254, s[12:15], s77 offen offset:0 lds\n"
"v_mfma_f32_16x16x32_bf16 v[60:63], a[28:31], a[124:127], v[60:63]\n"
"s_add_u32 m0, s58, 134144\n"
"v_add_f32 v167, v167, v168\n"
"v_mul_f32 v167, 0x3acccccd, v167\n"
"v_mfma_f32_16x16x32_bf16 v[60:63], a[24:27], a[120:123], v[60:63]\n"
"s_waitcnt lgkmcnt(6)\n"
"v_cvt_pk_bf16_f32 v167, v167, v167\n"
"v_lshlrev_b32 v167, 16, v167\n"
"v_mfma_f32_16x16x32_bf16 v[64:67], a[0:3], a[96:99], v[64:67]\n"
"ds_read_b64_tr_b16 a[16:17], v207 offset:0\n"
"v_add_f32 v167, 0x3727c5ac, v167\n"
"v_mfma_f32_16x16x32_bf16 v[64:67], a[4:7], a[100:103], v[64:67]\n"
"ds_read_b64_tr_b16 a[18:19], v207 offset:512\n"
"v_cvt_pk_bf16_f32 v167, v167, v167\n"
"v_mfma_f32_16x16x32_bf16 v[68:71], a[4:7], a[108:111], v[68:71]\n"
"ds_read_b64_tr_b16 a[20:21], v207 offset:4096\n"
"buffer_load_dwordx4 v226, s[12:15], s77 offen offset:0 lds\n"
"v_mfma_f32_16x16x32_bf16 v[68:71], a[0:3], a[104:107], v[68:71]\n"
"s_add_u32 m0, s58, 135168\n"
"ds_read_b64_tr_b16 a[22:23], v207 offset:4608\n"
"v_lshlrev_b32 v167, 16, v167\n"
"v_mfma_f32_16x16x32_bf16 v[72:75], a[0:3], a[112:115], v[72:75]\n"
"ds_read_b64_tr_b16 a[24:25], v207 offset:8\n"
"v_mfma_f32_16x16x32_bf16 v[72:75], a[4:7], a[116:119], v[72:75]\n"
"ds_read_b64_tr_b16 a[26:27], v207 offset:520\n"
"v_mfma_f32_16x16x32_bf16 v[76:79], a[4:7], a[124:127], v[76:79]\n"
"ds_read_b64_tr_b16 a[28:29], v207 offset:4104\n"
"v_mfma_f32_16x16x32_bf16 v[76:79], a[0:3], a[120:123], v[76:79]\n"
"ds_read_b64_tr_b16 a[30:31], v207 offset:4616\n"
"buffer_load_dwordx4 v254, s[12:15], s78 offen offset:0 lds\n"
"v_mfma_f32_16x16x32_bf16 v[80:83], a[8:11], a[96:99], v[80:83]\n"
"s_add_u32 m0, s58, 136192\n"
"ds_read_b128 a[56:59], v248 offset:6144\n"
"v_mfma_f32_16x16x32_bf16 v[80:83], a[12:15], a[100:103], v[80:83]\n"
"ds_read_b128 a[60:63], v249 offset:6144\n"
"v_mfma_f32_16x16x32_bf16 v[84:87], a[12:15], a[108:111], v[84:87]\n"
"v_sqrt_f32 v167, v167\n"
"v_mfma_f32_16x16x32_bf16 v[84:87], a[8:11], a[104:107], v[84:87]\n"
"v_cvt_pk_bf16_f32 v136, v166, v167\n"
"v_mov_b32 v137, s73\n"
"v_mfma_f32_16x16x32_bf16 v[88:91], a[8:11], a[112:115], v[88:91]\n"
"buffer_load_dwordx4 v226, s[12:15], s78 offen offset:0 lds\n"
"v_lshrrev_b32 v168, 5, v229\n"
"v_mfma_f32_16x16x32_bf16 v[88:91], a[12:15], a[116:119], v[88:91]\n"
"s_add_u32 m0, s58, 137216\n"
"v_and_b32 v169, 31, v229\n"
"v_lshlrev_b32 v169, 26, v169\n"
"v_mfma_f32_16x16x32_bf16 v[92:95], a[12:15], a[124:127], v[92:95]\n"
"v_lshl_add_u32 v169, v168, 3, v169\n"
"v_mfma_f32_16x16x32_bf16 v[92:95], a[8:11], a[120:123], v[92:95]\n"
"s_waitcnt lgkmcnt(0)\n"
"buffer_store_dwordx2 v[136:137], v169, s[36:39], s99 offen sc1\n"
"v_mfma_f32_16x16x32_bf16 v[96:99], a[16:19], a[96:99], v[96:99]\n"
"s_waitcnt vmcnt(19)\n"
"s_barrier\n"
"v_and_b32 v167, 0xffff0000, v136\n"
"v_mfma_f32_16x16x32_bf16 v[96:99], a[20:23], a[100:103], v[96:99]\n"
"ds_read_b64_tr_b16 a[0:1], v244 offset:32768\n"
"buffer_load_dwordx4 v254, s[12:15], s79 offen offset:0 lds\n"
"v_mfma_f32_16x16x32_bf16 v[100:103], a[20:23], a[108:111], v[100:103]\n"
"s_add_u32 m0, s58, 138240\n"
"ds_read_b64_tr_b16 a[2:3], v244 offset:33280\n"
"v_lshlrev_b32 v166, 16, v136\n"
"v_mfma_f32_16x16x32_bf16 v[100:103], a[16:19], a[104:107], v[100:103]\n"
"ds_read_b64_tr_b16 a[4:5], v244 offset:36864\n"
"v_mfma_f32_16x16x32_bf16 v[104:107], a[16:19], a[112:115], v[104:107]\n"
"ds_read_b64_tr_b16 a[6:7], v244 offset:37376\n"
"v_mfma_f32_16x16x32_bf16 v[104:107], a[20:23], a[116:119], v[104:107]\n"
"ds_read_b64_tr_b16 a[8:9], v244 offset:32776\n"
"v_mfma_f32_16x16x32_bf16 v[108:111], a[20:23], a[124:127], v[108:111]\n"
"ds_read_b64_tr_b16 a[10:11], v244 offset:33288\n"
"buffer_load_dwordx4 v226, s[12:15], s79 offen offset:0 lds\n"
"v_mfma_f32_16x16x32_bf16 v[108:111], a[16:19], a[120:123], v[108:111]\n"
"s_add_u32 m0, s81, 16384\n"
"ds_read_b64_tr_b16 a[12:13], v244 offset:36872\n"
"v_mfma_f32_16x16x32_bf16 v[112:115], a[24:27], a[96:99], v[112:115]\n"
"ds_read_b64_tr_b16 a[14:15], v244 offset:37384\n"
"v_mfma_f32_16x16x32_bf16 v[112:115], a[28:31], a[100:103], v[112:115]\n"
"v_rcp_f32 v167, v167\n"
"v_mfma_f32_16x16x32_bf16 v[116:119], a[28:31], a[108:111], v[116:119]\n"
"v_mul_f32 v167, 0x3fb8aa3b, v167\n"
"v_mfma_f32_16x16x32_bf16 v[116:119], a[24:27], a[104:107], v[116:119]\n"
"buffer_load_dwordx4 v252, s[16:19], s80 offen lds\n"
"v_mfma_f32_16x16x32_bf16 v[120:123], a[24:27], a[112:115], v[120:123]\n"
"s_add_u32 m0, s81, 17408\n"
"v_sub_f32 v170, v146, v166\n"
"v_mfma_f32_16x16x32_bf16 v[120:123], a[28:31], a[116:119], v[120:123]\n"
"v_mfma_f32_16x16x32_bf16 v[124:127], a[28:31], a[124:127], v[124:127]\n"
"v_mfma_f32_16x16x32_bf16 v[124:127], a[24:27], a[120:123], v[124:127]\n"
"s_waitcnt lgkmcnt(0)\n"
"v_sub_f32 v171, v147, v166\n"
"v_mfma_f32_16x16x32_bf16 v[0:3], a[0:3], a[32:35], v[0:3]\n"
"ds_read_b64_tr_b16 a[16:17], v245 offset:32768\n"
"buffer_load_dwordx4 v253, s[16:19], s80 offen lds\n"
"v_mfma_f32_16x16x32_bf16 v[0:3], a[4:7], a[36:39], v[0:3]\n"
"s_add_u32 m0, s81, 24576\n"
"ds_read_b64_tr_b16 a[18:19], v245 offset:33280\n"
"v_sub_f32 v172, v148, v166\n"
"v_mfma_f32_16x16x32_bf16 v[4:7], a[4:7], a[44:47], v[4:7]\n"
"s_lshl_b32 s40, s70, 19\n"
"s_add_u32 s40, s40, 393216\n"
"ds_read_b64_tr_b16 a[20:21], v245 offset:36864\n"
"v_mfma_f32_16x16x32_bf16 v[4:7], a[0:3], a[40:43], v[4:7]\n"
"s_add_u32 s24, s42, s40\n"
"s_addc_u32 s25, s43, 0\n"
"ds_read_b64_tr_b16 a[22:23], v245 offset:37376\n"
"v_mfma_f32_16x16x32_bf16 v[8:11], a[0:3], a[48:51], v[8:11]\n"
"ds_read_b64_tr_b16 a[24:25], v245 offset:32776\n"
"v_sub_f32 v173, v149, v166\n"
"v_mfma_f32_16x16x32_bf16 v[8:11], a[4:7], a[52:55], v[8:11]\n"
"ds_read_b64_tr_b16 a[26:27], v245 offset:33288\n"
"buffer_load_dwordx4 v252, s[20:23], s80 offen lds\n"
"v_mfma_f32_16x16x32_bf16 v[12:15], a[4:7], a[60:63], v[12:15]\n"
"s_add_u32 m0, s81, 25600\n"
"ds_read_b64_tr_b16 a[28:29], v245 offset:36872\n"
"v_sub_f32 v174, v150, v166\n"
"v_mfma_f32_16x16x32_bf16 v[12:15], a[0:3], a[56:59], v[12:15]\n"
"s_add_u32 s28, s44, s40\n"
"s_addc_u32 s29, s45, 0\n"
"ds_read_b64_tr_b16 a[30:31], v245 offset:37384\n"
"v_mfma_f32_16x16x32_bf16 v[128:131], a[0:3], v[208:211], v[128:131]\n"
"s_mov_b32 s26, 131072\n"
"s_mov_b32 s27, s63\n"
"v_mfma_f32_16x16x32_bf16 v[128:131], a[4:7], v[208:211], v[128:131]\n"
"s_mov_b32 s30, s26\n"
"s_mov_b32 s31, s63\n"
"v_sub_f32 v175, v151, v166\n"
"v_mfma_f32_16x16x32_bf16 v[16:19], a[8:11], a[32:35], v[16:19]\n"
"s_lshl_b32 s82, s68, 12\n"
"buffer_load_dwordx4 v253, s[20:23], s80 offen lds\n"
"v_mfma_f32_16x16x32_bf16 v[16:19], a[12:15], a[36:39], v[16:19]\n"
"s_add_u32 s80, s80, 524288\n"
"s_add_u32 m0, s58, 49024\n"
"v_mfma_f32_16x16x32_bf16 v[20:23], a[12:15], a[44:47], v[20:23]\n"
"s_add_u32 s84, s82, 81920\n"
"s_add_u32 s85, s82, 131072\n"
"v_sub_f32 v176, v152, v166\n"
"v_mfma_f32_16x16x32_bf16 v[20:23], a[8:11], a[40:43], v[20:23]\n"
"s_and_b32 s40, s69, 1\n"
"s_lshl_b32 s40, s40, 16\n"
"v_mfma_f32_16x16x32_bf16 v[24:27], a[8:11], a[48:51], v[24:27]\n"
"s_add_u32 s82, s82, s40\n"
"s_mov_b32 s83, s82\n"
"v_sub_f32 v177, v153, v166\n"
"v_mfma_f32_16x16x32_bf16 v[24:27], a[12:15], a[52:55], v[24:27]\n"
"s_mul_i32 s59, s71, 7\n"
"s_add_u32 s59, s59, s72\n"
"buffer_load_dwordx4 v254, s[12:15], s76 offen offset:128 lds\n"
"v_mfma_f32_16x16x32_bf16 v[28:31], a[12:15], a[60:63], v[28:31]\n"
"s_add_u32 m0, s58, 50048\n"
"v_mfma_f32_16x16x32_bf16 v[28:31], a[8:11], a[56:59], v[28:31]\n"
"s_cmp_lt_u32 s72, 7\n"
"s_cselect_b32 s88, 1, 0\n"
"v_sub_f32 v178, v154, v166\n"
"v_mfma_f32_16x16x32_bf16 v[132:135], a[8:11], v[208:211], v[132:135]\n"
"s_cmp_lt_u32 s59, 27\n"
"s_cselect_b32 s88, s88, 0\n"
"v_mfma_f32_16x16x32_bf16 v[132:135], a[12:15], v[208:211], v[132:135]\n"
"s_waitcnt lgkmcnt(0)\n"
"v_sub_f32 v179, v155, v166\n"
"v_mfma_f32_16x16x32_bf16 v[32:35], a[16:19], a[32:35], v[32:35]\n"
"ds_read_b64_tr_b16 a[0:1], v246 offset:32768\n"
"buffer_load_dwordx4 v226, s[12:15], s76 offen offset:128 lds\n"
"v_mfma_f32_16x16x32_bf16 v[32:35], a[20:23], a[36:39], v[32:35]\n"
"s_add_u32 m0, s58, 51072\n"
"ds_read_b64_tr_b16 a[2:3], v246 offset:33280\n"
"v_mfma_f32_16x16x32_bf16 v[36:39], a[20:23], a[44:47], v[36:39]\n"
"ds_read_b64_tr_b16 a[4:5], v246 offset:36864\n"
"v_sub_f32 v180, v156, v166\n"
"v_mfma_f32_16x16x32_bf16 v[36:39], a[16:19], a[40:43], v[36:39]\n"
"ds_read_b64_tr_b16 a[6:7], v246 offset:37376\n"
"v_mfma_f32_16x16x32_bf16 v[40:43], a[16:19], a[48:51], v[40:43]\n"
"ds_read_b64_tr_b16 a[8:9], v246 offset:32776\n"
"v_sub_f32 v181, v157, v166\n"
"v_mfma_f32_16x16x32_bf16 v[40:43], a[20:23], a[52:55], v[40:43]\n"
"ds_read_b64_tr_b16 a[10:11], v246 offset:33288\n"
"buffer_load_dwordx4 v254, s[12:15], s77 offen offset:128 lds\n"
"v_mfma_f32_16x16x32_bf16 v[44:47], a[20:23], a[60:63], v[44:47]\n"
"s_add_u32 m0, s58, 52096\n"
"ds_read_b64_tr_b16 a[12:13], v246 offset:36872\n"
"v_mfma_f32_16x16x32_bf16 v[44:47], a[16:19], a[56:59], v[44:47]\n"
"ds_read_b64_tr_b16 a[14:15], v246 offset:37384\n"
"v_sub_f32 v182, v158, v166\n"
"v_mfma_f32_16x16x32_bf16 v[48:51], a[24:27], a[32:35], v[48:51]\n"
"s_waitcnt vmcnt(20)\n"
"ds_read_b128 a[64:67], v248 offset:32768\n"
"v_mfma_f32_16x16x32_bf16 v[48:51], a[28:31], a[36:39], v[48:51]\n"
"ds_read_b128 a[68:71], v249 offset:32768\n"
"v_sub_f32 v183, v159, v166\n"
"v_mfma_f32_16x16x32_bf16 v[52:55], a[28:31], a[44:47], v[52:55]\n"
"ds_read_b128 a[72:75], v248 offset:34816\n"
"buffer_load_dwordx4 v226, s[12:15], s77 offen offset:128 lds\n"
"v_mfma_f32_16x16x32_bf16 v[52:55], a[24:27], a[40:43], v[52:55]\n"
"s_add_u32 m0, s58, 53120\n"
"ds_read_b128 a[76:79], v249 offset:34816\n"
"v_mfma_f32_16x16x32_bf16 v[56:59], a[24:27], a[48:51], v[56:59]\n"
"ds_read_b128 a[80:83], v248 offset:36864\n"
"v_sub_f32 v184, v160, v166\n"
"v_mfma_f32_16x16x32_bf16 v[56:59], a[28:31], a[52:55], v[56:59]\n"
"ds_read_b128 a[84:87], v249 offset:36864\n"
"v_mfma_f32_16x16x32_bf16 v[60:63], a[28:31], a[60:63], v[60:63]\n"
"v_mfma_f32_16x16x32_bf16 v[60:63], a[24:27], a[56:59], v[60:63]\n"
"s_waitcnt lgkmcnt(6)\n"
"buffer_load_dwordx4 v254, s[12:15], s78 offen offset:128 lds\n"
"v_sub_f32 v185, v161, v166\n"
"v_mfma_f32_16x16x32_bf16 v[64:67], a[0:3], a[32:35], v[64:67]\n"
"s_add_u32 m0, s58, 54144\n"
"ds_read_b64_tr_b16 a[16:17], v247 offset:32768\n"
"v_mfma_f32_16x16x32_bf16 v[64:67], a[4:7], a[36:39], v[64:67]\n"
"ds_read_b64_tr_b16 a[18:19], v247 offset:33280\n"
"v_sub_f32 v186, v162, v166\n"
"v_mfma_f32_16x16x32_bf16 v[68:71], a[4:7], a[44:47], v[68:71]\n"
"ds_read_b64_tr_b16 a[20:21], v247 offset:36864\n"
"v_mfma_f32_16x16x32_bf16 v[68:71], a[0:3], a[40:43], v[68:71]\n"
"ds_read_b64_tr_b16 a[22:23], v247 offset:37376\n"
"v_mfma_f32_16x16x32_bf16 v[72:75], a[0:3], a[48:51], v[72:75]\n"
"ds_read_b64_tr_b16 a[24:25], v247 offset:32776\n"
"buffer_load_dwordx4 v226, s[12:15], s78 offen offset:128 lds\n"
"v_mfma_f32_16x16x32_bf16 v[72:75], a[4:7], a[52:55], v[72:75]\n"
"s_add_u32 m0, s58, 55168\n"
"ds_read_b64_tr_b16 a[26:27], v247 offset:33288\n"
"v_sub_f32 v187, v163, v166\n"
"v_mfma_f32_16x16x32_bf16 v[76:79], a[4:7], a[60:63], v[76:79]\n"
"ds_read_b64_tr_b16 a[28:29], v247 offset:36872\n"
"v_sub_f32 v188, v164, v166\n"
"v_mfma_f32_16x16x32_bf16 v[76:79], a[0:3], a[56:59], v[76:79]\n"
"ds_read_b64_tr_b16 a[30:31], v247 offset:37384\n"
"v_mfma_f32_16x16x32_bf16 v[80:83], a[8:11], a[32:35], v[80:83]\n"
"ds_read_b128 a[88:91], v248 offset:38912\n"
"v_mfma_f32_16x16x32_bf16 v[80:83], a[12:15], a[36:39], v[80:83]\n"
"ds_read_b128 a[92:95], v249 offset:38912\n"
"buffer_load_dwordx4 v254, s[12:15], s79 offen offset:128 lds\n"
"v_mfma_f32_16x16x32_bf16 v[84:87], a[12:15], a[44:47], v[84:87]\n"
"s_add_u32 m0, s58, 56192\n"
"v_sub_f32 v189, v165, v166\n"
"v_mfma_f32_16x16x32_bf16 v[84:87], a[8:11], a[40:43], v[84:87]\n"
"v_mul_f32 v170, v170, v167\n"
"v_mfma_f32_16x16x32_bf16 v[88:91], a[8:11], a[48:51], v[88:91]\n"
"v_mfma_f32_16x16x32_bf16 v[88:91], a[12:15], a[52:55], v[88:91]\n"
"v_mfma_f32_16x16x32_bf16 v[92:95], a[12:15], a[60:63], v[92:95]\n"
"buffer_load_dwordx4 v226, s[12:15], s79 offen offset:128 lds\n"
"v_mul_f32 v171, v171, v167\n"
"v_mfma_f32_16x16x32_bf16 v[92:95], a[8:11], a[56:59], v[92:95]\n"
"s_waitcnt lgkmcnt(0)\n"
"s_add_u32 m0, s81, 114688\n"
"v_mfma_f32_16x16x32_bf16 v[96:99], a[16:19], a[32:35], v[96:99]\n"
"s_waitcnt vmcnt(21)\n"
"s_barrier\n"
"v_mul_f32 v172, v172, v167\n"
"v_mfma_f32_16x16x32_bf16 v[96:99], a[20:23], a[36:39], v[96:99]\n"
"ds_read_b64_tr_b16 a[0:1], v244 offset:0\n"
"v_mfma_f32_16x16x32_bf16 v[100:103], a[20:23], a[44:47], v[100:103]\n"
"ds_read_b64_tr_b16 a[2:3], v244 offset:512\n"
"v_mfma_f32_16x16x32_bf16 v[100:103], a[16:19], a[40:43], v[100:103]\n"
"ds_read_b64_tr_b16 a[4:5], v244 offset:4096\n"
"buffer_load_dwordx4 v252, s[16:19], s80 offen lds\n"
"v_mfma_f32_16x16x32_bf16 v[104:107], a[16:19], a[48:51], v[104:107]\n"
"s_add_u32 m0, s81, 115712\n"
"ds_read_b64_tr_b16 a[6:7], v244 offset:4608\n"
"v_mul_f32 v173, v173, v167\n"
"v_mfma_f32_16x16x32_bf16 v[104:107], a[20:23], a[52:55], v[104:107]\n"
"ds_read_b64_tr_b16 a[8:9], v244 offset:8\n"
"v_mfma_f32_16x16x32_bf16 v[108:111], a[20:23], a[60:63], v[108:111]\n"
"ds_read_b64_tr_b16 a[10:11], v244 offset:520\n"
"v_mul_f32 v174, v174, v167\n"
"v_mfma_f32_16x16x32_bf16 v[108:111], a[16:19], a[56:59], v[108:111]\n"
"ds_read_b64_tr_b16 a[12:13], v244 offset:4104\n"
"v_mfma_f32_16x16x32_bf16 v[112:115], a[24:27], a[32:35], v[112:115]\n"
"ds_read_b64_tr_b16 a[14:15], v244 offset:4616\n"
"buffer_load_dwordx4 v253, s[16:19], s80 offen lds\n"
"v_mfma_f32_16x16x32_bf16 v[112:115], a[28:31], a[36:39], v[112:115]\n"
"s_add_u32 m0, s81, 122880\n"
"v_mul_f32 v175, v175, v167\n"
"v_mfma_f32_16x16x32_bf16 v[116:119], a[28:31], a[44:47], v[116:119]\n"
"v_mfma_f32_16x16x32_bf16 v[116:119], a[24:27], a[40:43], v[116:119]\n"
"v_mul_f32 v176, v176, v167\n"
"v_mfma_f32_16x16x32_bf16 v[120:123], a[24:27], a[48:51], v[120:123]\n"
"v_mfma_f32_16x16x32_bf16 v[120:123], a[28:31], a[52:55], v[120:123]\n"
"buffer_load_dwordx4 v252, s[20:23], s80 offen lds\n"
"v_mul_f32 v177, v177, v167\n"
"v_mfma_f32_16x16x32_bf16 v[124:127], a[28:31], a[60:63], v[124:127]\n"
"s_add_u32 m0, s81, 123904\n"
"v_mfma_f32_16x16x32_bf16 v[124:127], a[24:27], a[56:59], v[124:127]\n"
"s_waitcnt lgkmcnt(0)\n"
"v_mfma_f32_16x16x32_bf16 v[0:3], a[0:3], a[64:67], v[0:3]\n"
"ds_read_b64_tr_b16 a[16:17], v245 offset:0\n"
"v_mul_f32 v178, v178, v167\n"
"v_mfma_f32_16x16x32_bf16 v[0:3], a[4:7], a[68:71], v[0:3]\n"
"ds_read_b64_tr_b16 a[18:19], v245 offset:512\n"
"v_mfma_f32_16x16x32_bf16 v[4:7], a[4:7], a[76:79], v[4:7]\n"
"ds_read_b64_tr_b16 a[20:21], v245 offset:4096\n"
"buffer_load_dwordx4 v253, s[20:23], s80 offen lds\n"
"v_mfma_f32_16x16x32_bf16 v[4:7], a[0:3], a[72:75], v[4:7]\n"
"s_add_u32 s80, s80, 524288\n"
"s_add_u32 m0, s58, 81664\n"
"ds_read_b64_tr_b16 a[22:23], v245 offset:4608\n"
"v_mfma_f32_16x16x32_bf16 v[8:11], a[0:3], a[80:83], v[8:11]\n"
"ds_read_b64_tr_b16 a[24:25], v245 offset:8\n"
"v_mul_f32 v179, v179, v167\n"
"v_mfma_f32_16x16x32_bf16 v[8:11], a[4:7], a[84:87], v[8:11]\n"
"ds_read_b64_tr_b16 a[26:27], v245 offset:520\n"
"v_mul_f32 v180, v180, v167\n"
"v_mfma_f32_16x16x32_bf16 v[12:15], a[4:7], a[92:95], v[12:15]\n"
"ds_read_b64_tr_b16 a[28:29], v245 offset:4104\n"
"v_mfma_f32_16x16x32_bf16 v[12:15], a[0:3], a[88:91], v[12:15]\n"
"ds_read_b64_tr_b16 a[30:31], v245 offset:4616\n"
"buffer_load_dwordx4 v254, s[12:15], s76 offen offset:256 lds\n"
"v_mfma_f32_16x16x32_bf16 v[128:131], a[0:3], v[208:211], v[128:131]\n"
"s_add_u32 m0, s58, 82688\n"
"v_mul_f32 v181, v181, v167\n"
"v_mfma_f32_16x16x32_bf16 v[128:131], a[4:7], v[208:211], v[128:131]\n"
"v_mfma_f32_16x16x32_bf16 v[16:19], a[8:11], a[64:67], v[16:19]\n"
"v_mul_f32 v182, v182, v167\n"
"v_mfma_f32_16x16x32_bf16 v[16:19], a[12:15], a[68:71], v[16:19]\n"
"v_mfma_f32_16x16x32_bf16 v[20:23], a[12:15], a[76:79], v[20:23]\n"
"buffer_load_dwordx4 v226, s[12:15], s76 offen offset:256 lds\n"
"v_mfma_f32_16x16x32_bf16 v[20:23], a[8:11], a[72:75], v[20:23]\n"
"s_add_u32 m0, s58, 83712\n"
"v_mul_f32 v183, v183, v167\n"
"v_mfma_f32_16x16x32_bf16 v[24:27], a[8:11], a[80:83], v[24:27]\n"
"v_mfma_f32_16x16x32_bf16 v[24:27], a[12:15], a[84:87], v[24:27]\n"
"v_mul_f32 v184, v184, v167\n"
"v_mfma_f32_16x16x32_bf16 v[28:31], a[12:15], a[92:95], v[28:31]\n"
"v_mfma_f32_16x16x32_bf16 v[28:31], a[8:11], a[88:91], v[28:31]\n"
"buffer_load_dwordx4 v254, s[12:15], s77 offen offset:256 lds\n"
"v_mfma_f32_16x16x32_bf16 v[132:135], a[8:11], v[208:211], v[132:135]\n"
"s_add_u32 m0, s58, 84736\n"
"v_mul_f32 v185, v185, v167\n"
"v_mfma_f32_16x16x32_bf16 v[132:135], a[12:15], v[208:211], v[132:135]\n"
"s_waitcnt lgkmcnt(0)\n"
"v_mfma_f32_16x16x32_bf16 v[32:35], a[16:19], a[64:67], v[32:35]\n"
"ds_read_b64_tr_b16 a[0:1], v246 offset:0\n"
"v_mul_f32 v186, v186, v167\n"
"v_mfma_f32_16x16x32_bf16 v[32:35], a[20:23], a[68:71], v[32:35]\n"
"ds_read_b64_tr_b16 a[2:3], v246 offset:512\n"
"v_mfma_f32_16x16x32_bf16 v[36:39], a[20:23], a[76:79], v[36:39]\n"
"ds_read_b64_tr_b16 a[4:5], v246 offset:4096\n"
"buffer_load_dwordx4 v226, s[12:15], s77 offen offset:256 lds\n"
"v_mfma_f32_16x16x32_bf16 v[36:39], a[16:19], a[72:75], v[36:39]\n"
"s_add_u32 m0, s58, 85760\n"
"ds_read_b64_tr_b16 a[6:7], v246 offset:4608\n"
"v_mul_f32 v187, v187, v167\n"
"v_mfma_f32_16x16x32_bf16 v[40:43], a[16:19], a[80:83], v[40:43]\n"
"ds_read_b64_tr_b16 a[8:9], v246 offset:8\n"
"v_mfma_f32_16x16x32_bf16 v[40:43], a[20:23], a[84:87], v[40:43]\n"
"ds_read_b64_tr_b16 a[10:11], v246 offset:520\n"
"v_mul_f32 v188, v188, v167\n"
"v_mfma_f32_16x16x32_bf16 v[44:47], a[20:23], a[92:95], v[44:47]\n"
"ds_read_b64_tr_b16 a[12:13], v246 offset:4104\n"
"v_mfma_f32_16x16x32_bf16 v[44:47], a[16:19], a[88:91], v[44:47]\n"
"ds_read_b64_tr_b16 a[14:15], v246 offset:4616\n"
"buffer_load_dwordx4 v254, s[12:15], s78 offen offset:256 lds\n"
"v_mfma_f32_16x16x32_bf16 v[48:51], a[24:27], a[64:67], v[48:51]\n"
"s_waitcnt vmcnt(21)\n"
"s_add_u32 m0, s58, 86784\n"
"ds_read_b128 a[96:99], v250 offset:0\n"
"v_mfma_f32_16x16x32_bf16 v[48:51], a[28:31], a[68:71], v[48:51]\n"
"ds_read_b128 a[100:103], v251 offset:0\n"
"v_mul_f32 v189, v189, v167\n"
"v_mfma_f32_16x16x32_bf16 v[52:55], a[28:31], a[76:79], v[52:55]\n"
"ds_read_b128 a[104:107], v250 offset:2048\n"
"v_cvt_pk_bf16_f32 v146, v170, v171\n"
"v_mfma_f32_16x16x32_bf16 v[52:55], a[24:27], a[72:75], v[52:55]\n"
"ds_read_b128 a[108:111], v251 offset:2048\n"
"v_mfma_f32_16x16x32_bf16 v[56:59], a[24:27], a[80:83], v[56:59]\n"
"ds_read_b128 a[112:115], v250 offset:4096\n"
"buffer_load_dwordx4 v226, s[12:15], s78 offen offset:256 lds\n"
"v_mfma_f32_16x16x32_bf16 v[56:59], a[28:31], a[84:87], v[56:59]\n"
"s_add_u32 m0, s58, 87808\n"
"ds_read_b128 a[116:119], v251 offset:4096\n"
"v_cvt_pk_bf16_f32 v147, v172, v173\n"
"v_mfma_f32_16x16x32_bf16 v[60:63], a[28:31], a[92:95], v[60:63]\n"
"v_mfma_f32_16x16x32_bf16 v[60:63], a[24:27], a[88:91], v[60:63]\n"
"s_waitcnt lgkmcnt(6)\n"
"v_mfma_f32_16x16x32_bf16 v[64:67], a[0:3], a[64:67], v[64:67]\n"
"ds_read_b64_tr_b16 a[16:17], v247 offset:0\n"
"v_cvt_pk_bf16_f32 v148, v174, v175\n"
"v_mfma_f32_16x16x32_bf16 v[64:67], a[4:7], a[68:71], v[64:67]\n"
"ds_read_b64_tr_b16 a[18:19], v247 offset:512\n"
"buffer_load_dwordx4 v254, s[12:15], s79 offen offset:256 lds\n"
"v_mfma_f32_16x16x32_bf16 v[68:71], a[4:7], a[76:79], v[68:71]\n"
"s_add_u32 m0, s58, 88832\n"
"ds_read_b64_tr_b16 a[20:21], v247 offset:4096\n"
"v_cvt_pk_bf16_f32 v149, v176, v177\n"
"v_mfma_f32_16x16x32_bf16 v[68:71], a[0:3], a[72:75], v[68:71]\n"
"ds_read_b64_tr_b16 a[22:23], v247 offset:4608\n"
"v_mfma_f32_16x16x32_bf16 v[72:75], a[0:3], a[80:83], v[72:75]\n"
"ds_read_b64_tr_b16 a[24:25], v247 offset:8\n"
"v_mfma_f32_16x16x32_bf16 v[72:75], a[4:7], a[84:87], v[72:75]\n"
"ds_read_b64_tr_b16 a[26:27], v247 offset:520\n"
"v_cvt_pk_bf16_f32 v150, v178, v179\n"
"v_mfma_f32_16x16x32_bf16 v[76:79], a[4:7], a[92:95], v[76:79]\n"
"ds_read_b64_tr_b16 a[28:29], v247 offset:4104\n"
"buffer_load_dwordx4 v226, s[12:15], s79 offen offset:256 lds\n"
"v_mfma_f32_16x16x32_bf16 v[76:79], a[0:3], a[88:91], v[76:79]\n"
"s_add_u32 m0, s81, 32768\n"
"ds_read_b64_tr_b16 a[30:31], v247 offset:4616\n"
"v_cvt_pk_bf16_f32 v151, v180, v181\n"
"v_mfma_f32_16x16x32_bf16 v[80:83], a[8:11], a[64:67], v[80:83]\n"
"ds_read_b128 a[120:123], v250 offset:6144\n"
"v_mfma_f32_16x16x32_bf16 v[80:83], a[12:15], a[68:71], v[80:83]\n"
"ds_read_b128 a[124:127], v251 offset:6144\n"
"v_mfma_f32_16x16x32_bf16 v[84:87], a[12:15], a[76:79], v[84:87]\n"
"v_cvt_pk_bf16_f32 v152, v182, v183\n"
"v_mfma_f32_16x16x32_bf16 v[84:87], a[8:11], a[72:75], v[84:87]\n"
"buffer_load_dwordx4 v252, s[16:19], s80 offen lds\n"
"v_mfma_f32_16x16x32_bf16 v[88:91], a[8:11], a[80:83], v[88:91]\n"
"s_add_u32 m0, s81, 33792\n"
"v_cvt_pk_bf16_f32 v153, v184, v185\n"
"v_mfma_f32_16x16x32_bf16 v[88:91], a[12:15], a[84:87], v[88:91]\n"
"v_mfma_f32_16x16x32_bf16 v[92:95], a[12:15], a[92:95], v[92:95]\n"
"v_mfma_f32_16x16x32_bf16 v[92:95], a[8:11], a[88:91], v[92:95]\n"
"s_waitcnt lgkmcnt(0)\n"
"v_cvt_pk_bf16_f32 v154, v186, v187\n"
"v_mfma_f32_16x16x32_bf16 v[96:99], a[16:19], a[64:67], v[96:99]\n"
"s_waitcnt vmcnt(21)\n"
"s_barrier\n"
"buffer_load_dwordx4 v253, s[16:19], s80 offen lds\n"
"v_mfma_f32_16x16x32_bf16 v[96:99], a[20:23], a[68:71], v[96:99]\n"
"s_add_u32 m0, s81, 40960\n"
"ds_read_b64_tr_b16 a[0:1], v244 offset:16384\n"
"v_cvt_pk_bf16_f32 v155, v188, v189\n"
"v_mfma_f32_16x16x32_bf16 v[100:103], a[20:23], a[76:79], v[100:103]\n"
"ds_read_b64_tr_b16 a[2:3], v244 offset:16896\n"
"v_mfma_f32_16x16x32_bf16 v[100:103], a[16:19], a[72:75], v[100:103]\n"
"ds_read_b64_tr_b16 a[4:5], v244 offset:20480\n"
"v_mfma_f32_16x16x32_bf16 v[104:107], a[16:19], a[80:83], v[104:107]\n"
"s_mul_i32 s90, s68, 2592\n"
"ds_read_b64_tr_b16 a[6:7], v244 offset:20992\n"
"v_mfma_f32_16x16x32_bf16 v[104:107], a[20:23], a[84:87], v[104:107]\n"
"s_add_u32 s90, s90, 32768\n"
"ds_read_b64_tr_b16 a[8:9], v244 offset:16392\n"
"buffer_load_dwordx4 v252, s[20:23], s80 offen lds\n"
"v_mfma_f32_16x16x32_bf16 v[108:111], a[20:23], a[92:95], v[108:111]\n"
"s_add_u32 m0, s81, 41984\n"
"ds_read_b64_tr_b16 a[10:11], v244 offset:16904\n"
"v_mul_u32_u24 v168, 1296, v168\n"
"v_mfma_f32_16x16x32_bf16 v[108:111], a[16:19], a[88:91], v[108:111]\n"
"ds_read_b64_tr_b16 a[12:13], v244 offset:20488\n"
"v_mfma_f32_16x16x32_bf16 v[112:115], a[24:27], a[64:67], v[112:115]\n"
"ds_read_b64_tr_b16 a[14:15], v244 offset:21000\n"
"v_and_b32 v169, 31, v229\n"
"v_mfma_f32_16x16x32_bf16 v[112:115], a[28:31], a[68:71], v[112:115]\n"
"v_mfma_f32_16x16x32_bf16 v[116:119], a[28:31], a[76:79], v[116:119]\n"
"buffer_load_dwordx4 v253, s[20:23], s80 offen lds\n"
"v_lshl_add_u32 v168, v169, 3, v168\n"
"v_mfma_f32_16x16x32_bf16 v[116:119], a[24:27], a[72:75], v[116:119]\n"
"s_add_u32 s80, s80, 524288\n"
"s_add_u32 m0, s58, 130688\n"
"v_mfma_f32_16x16x32_bf16 v[120:123], a[24:27], a[80:83], v[120:123]\n"
"v_mfma_f32_16x16x32_bf16 v[120:123], a[28:31], a[84:87], v[120:123]\n"
"v_add_u32 v168, s90, v168\n"
"v_mfma_f32_16x16x32_bf16 v[124:127], a[28:31], a[92:95], v[124:127]\n"
"v_mfma_f32_16x16x32_bf16 v[124:127], a[24:27], a[88:91], v[124:127]\n"
"s_waitcnt lgkmcnt(0)\n"
"buffer_load_dwordx4 v254, s[12:15], s76 offen offset:384 lds\n"
"v_mfma_f32_16x16x32_bf16 v[0:3], a[0:3], a[96:99], v[0:3]\n"
"s_add_u32 m0, s58, 131712\n"
"ds_read_b64_tr_b16 a[16:17], v245 offset:16384\n"
"v_lshrrev_b32 v219, 2, v212\n"
"v_mfma_f32_16x16x32_bf16 v[0:3], a[4:7], a[100:103], v[0:3]\n"
".L_pad2:\n"
"s_cbranch_execz .L_pad6\n"
"ds_read_b64_tr_b16 a[18:19], v245 offset:16896\n"
"v_mfma_f32_16x16x32_bf16 v[4:7], a[4:7], a[108:111], v[4:7]\n"
"ds_read_b64_tr_b16 a[20:21], v245 offset:20480\n"
"v_lshl_add_u32 v218, v219, 10, v218\n"
"v_mfma_f32_16x16x32_bf16 v[4:7], a[0:3], a[104:107], v[4:7]\n"
"ds_read_b64_tr_b16 a[22:23], v245 offset:20992\n"
"v_mfma_f32_16x16x32_bf16 v[8:11], a[0:3], a[112:115], v[8:11]\n"
"ds_read_b64_tr_b16 a[24:25], v245 offset:16392\n"
"buffer_load_dwordx4 v226, s[12:15], s76 offen offset:384 lds\n"
"v_mfma_f32_16x16x32_bf16 v[8:11], a[4:7], a[116:119], v[8:11]\n"
"s_add_u32 m0, s58, 132736\n"
"ds_read_b64_tr_b16 a[26:27], v245 offset:16904\n"
"v_and_b32 v219, 3, v212\n"
"v_mfma_f32_16x16x32_bf16 v[12:15], a[4:7], a[124:127], v[12:15]\n"
"ds_read_b64_tr_b16 a[28:29], v245 offset:20488\n"
"v_mfma_f32_16x16x32_bf16 v[12:15], a[0:3], a[120:123], v[12:15]\n"
"ds_read_b64_tr_b16 a[30:31], v245 offset:21000\n"
"v_lshl_add_u32 v218, v219, 7, v218\n"
"v_mfma_f32_16x16x32_bf16 v[128:131], a[0:3], v[208:211], v[128:131]\n"
"v_mfma_f32_16x16x32_bf16 v[128:131], a[4:7], v[208:211], v[128:131]\n"
"buffer_load_dwordx4 v254, s[12:15], s77 offen offset:384 lds\n"
"v_mfma_f32_16x16x32_bf16 v[16:19], a[8:11], a[96:99], v[16:19]\n"
"s_add_u32 m0, s58, 133760\n"
"v_add_u32 v218, 81920, v218\n"
"v_mfma_f32_16x16x32_bf16 v[16:19], a[12:15], a[100:103], v[16:19]\n"
"v_mfma_f32_16x16x32_bf16 v[20:23], a[12:15], a[108:111], v[20:23]\n"
"v_mfma_f32_16x16x32_bf16 v[20:23], a[8:11], a[104:107], v[20:23]\n"
"s_and_b32 s47, s68, 1\n"
"s_lshl_b32 s47, s47, 6\n"
"v_mfma_f32_16x16x32_bf16 v[24:27], a[8:11], a[112:115], v[24:27]\n"
"s_lshr_b32 s46, s68, 1\n"
"buffer_load_dwordx4 v226, s[12:15], s77 offen offset:384 lds\n"
"v_mfma_f32_16x16x32_bf16 v[24:27], a[12:15], a[116:119], v[24:27]\n"
"s_add_u32 m0, s58, 134784\n"
"s_lshl_b32 s46, s46, 9\n"
"s_or_b32 s47, s47, s46\n"
"v_mfma_f32_16x16x32_bf16 v[28:31], a[12:15], a[124:127], v[28:31]\n"
"v_xor_b32 v240, s47, v218\n"
"v_mfma_f32_16x16x32_bf16 v[28:31], a[8:11], a[120:123], v[28:31]\n"
"v_mfma_f32_16x16x32_bf16 v[132:135], a[8:11], v[208:211], v[132:135]\n"
"v_mfma_f32_16x16x32_bf16 v[132:135], a[12:15], v[208:211], v[132:135]\n"
"s_waitcnt lgkmcnt(0)\n"
"buffer_load_dwordx4 v254, s[12:15], s78 offen offset:384 lds\n"
"v_xor_b32 v241, 64, v240\n"
"v_mfma_f32_16x16x32_bf16 v[32:35], a[16:19], a[96:99], v[32:35]\n"
"s_add_u32 m0, s58, 135808\n"
"ds_read_b64_tr_b16 a[0:1], v246 offset:16384\n"
"v_mfma_f32_16x16x32_bf16 v[32:35], a[20:23], a[100:103], v[32:35]\n"
"ds_read_b64_tr_b16 a[2:3], v246 offset:16896\n"
"v_xor_b32 v242, 512, v240\n"
"v_mfma_f32_16x16x32_bf16 v[36:39], a[20:23], a[108:111], v[36:39]\n"
"ds_read_b64_tr_b16 a[4:5], v246 offset:20480\n"
"v_mfma_f32_16x16x32_bf16 v[36:39], a[16:19], a[104:107], v[36:39]\n"
"ds_read_b64_tr_b16 a[6:7], v246 offset:20992\n"
"v_mfma_f32_16x16x32_bf16 v[40:43], a[16:19], a[112:115], v[40:43]\n"
"ds_read_b64_tr_b16 a[8:9], v246 offset:16392\n"
"buffer_load_dwordx4 v226, s[12:15], s78 offen offset:384 lds\n"
"v_mfma_f32_16x16x32_bf16 v[40:43], a[20:23], a[116:119], v[40:43]\n"
"s_add_u32 m0, s58, 136832\n"
"ds_read_b64_tr_b16 a[10:11], v246 offset:16904\n"
"v_xor_b32 v243, 576, v240\n"
"v_mfma_f32_16x16x32_bf16 v[44:47], a[20:23], a[124:127], v[44:47]\n"
"ds_read_b64_tr_b16 a[12:13], v246 offset:20488\n"
"v_mfma_f32_16x16x32_bf16 v[44:47], a[16:19], a[120:123], v[44:47]\n"
"ds_read_b64_tr_b16 a[14:15], v246 offset:21000\n"
"v_lshlrev_b32 v217, 10, v213\n"
"v_mfma_f32_16x16x32_bf16 v[48:51], a[24:27], a[96:99], v[48:51]\n"
"s_waitcnt vmcnt(22)\n"
"ds_read_b128 a[32:35], v248 offset:0\n"
"v_mfma_f32_16x16x32_bf16 v[48:51], a[28:31], a[100:103], v[48:51]\n"
"ds_read_b128 a[36:39], v249 offset:0\n"
"buffer_load_dwordx4 v254, s[12:15], s79 offen offset:384 lds\n"
"v_mfma_f32_16x16x32_bf16 v[52:55], a[28:31], a[108:111], v[52:55]\n"
"s_add_u32 m0, s58, 137856\n"
"ds_read_b128 a[40:43], v248 offset:2048\n"
"v_lshl_add_u32 v217, v214, 7, v217\n"
"v_mfma_f32_16x16x32_bf16 v[52:55], a[24:27], a[104:107], v[52:55]\n"
"ds_read_b128 a[44:47], v249 offset:2048\n"
"v_mfma_f32_16x16x32_bf16 v[56:59], a[24:27], a[112:115], v[56:59]\n"
"ds_read_b128 a[48:51], v248 offset:4096\n"
"v_lshl_add_u32 v217, v215, 3, v217\n"
"v_mfma_f32_16x16x32_bf16 v[56:59], a[28:31], a[116:119], v[56:59]\n"
"ds_read_b128 a[52:55], v249 offset:4096\n"
"v_mfma_f32_16x16x32_bf16 v[60:63], a[28:31], a[124:127], v[60:63]\n"
"buffer_load_dwordx4 v226, s[12:15], s79 offen offset:384 lds\n"
"v_mfma_f32_16x16x32_bf16 v[60:63], a[24:27], a[120:123], v[60:63]\n"
"s_waitcnt lgkmcnt(6)\n"
"s_add_u32 m0, s81, 0\n"
"v_lshl_add_u32 v217, v216, 5, v217\n"
"v_mfma_f32_16x16x32_bf16 v[64:67], a[0:3], a[96:99], v[64:67]\n"
"ds_read_b64_tr_b16 a[16:17], v247 offset:16384\n"
"v_mfma_f32_16x16x32_bf16 v[64:67], a[4:7], a[100:103], v[64:67]\n"
"ds_read_b64_tr_b16 a[18:19], v247 offset:16896\n"
"v_add_u32 v232, 131072, v217\n"
"v_mfma_f32_16x16x32_bf16 v[68:71], a[4:7], a[108:111], v[68:71]\n"
"ds_read_b64_tr_b16 a[20:21], v247 offset:20480\n"
"v_mfma_f32_16x16x32_bf16 v[68:71], a[0:3], a[104:107], v[68:71]\n"
"ds_read_b64_tr_b16 a[22:23], v247 offset:20992\n"
"buffer_load_dwordx4 v252, s[16:19], s80 offen lds\n"
"v_mfma_f32_16x16x32_bf16 v[72:75], a[0:3], a[112:115], v[72:75]\n"
"s_add_u32 m0, s81, 1024\n"
"ds_read_b64_tr_b16 a[24:25], v247 offset:16392\n"
"v_xor_b32 v233, 32, v232\n"
"v_mfma_f32_16x16x32_bf16 v[72:75], a[4:7], a[116:119], v[72:75]\n"
"ds_read_b64_tr_b16 a[26:27], v247 offset:16904\n"
"v_mfma_f32_16x16x32_bf16 v[76:79], a[4:7], a[124:127], v[76:79]\n"
"ds_read_b64_tr_b16 a[28:29], v247 offset:20488\n"
"v_mfma_f32_16x16x32_bf16 v[76:79], a[0:3], a[120:123], v[76:79]\n"
"ds_read_b64_tr_b16 a[30:31], v247 offset:21000\n"
"v_xor_b32 v234, 64, v232\n"
"v_mfma_f32_16x16x32_bf16 v[80:83], a[8:11], a[96:99], v[80:83]\n"
"ds_read_b128 a[56:59], v248 offset:6144\n"
"buffer_load_dwordx4 v253, s[16:19], s80 offen lds\n"
"v_mfma_f32_16x16x32_bf16 v[80:83], a[12:15], a[100:103], v[80:83]\n"
"s_add_u32 m0, s81, 8192\n"
"ds_read_b128 a[60:63], v249 offset:6144\n"
"v_xor_b32 v235, 96, v232\n"
"v_mfma_f32_16x16x32_bf16 v[84:87], a[12:15], a[108:111], v[84:87]\n"
"v_mfma_f32_16x16x32_bf16 v[84:87], a[8:11], a[104:107], v[84:87]\n"
"v_mfma_f32_16x16x32_bf16 v[88:91], a[8:11], a[112:115], v[88:91]\n"
"v_bfe_u32 v217, v229, 3, 2\n"
"v_mfma_f32_16x16x32_bf16 v[88:91], a[12:15], a[116:119], v[88:91]\n"
"buffer_load_dwordx4 v252, s[20:23], s80 offen lds\n"
"v_mfma_f32_16x16x32_bf16 v[92:95], a[12:15], a[124:127], v[92:95]\n"
"s_add_u32 m0, s81, 9216\n"
"v_lshrrev_b32 v218, 5, v229\n"
"v_mfma_f32_16x16x32_bf16 v[92:95], a[8:11], a[120:123], v[92:95]\n"
"s_waitcnt lgkmcnt(0)\n"
"v_mfma_f32_16x16x32_bf16 v[96:99], a[16:19], a[96:99], v[96:99]\n"
"s_waitcnt vmcnt(23)\n"
"s_barrier\n"
"v_mfma_f32_16x16x32_bf16 v[96:99], a[20:23], a[100:103], v[96:99]\n"
"ds_read_b64_tr_b16 a[0:1], v204 offset:0\n"
"v_lshlrev_b32 v218, 7, v218\n"
"v_mfma_f32_16x16x32_bf16 v[100:103], a[20:23], a[108:111], v[100:103]\n"
"ds_read_b64_tr_b16 a[2:3], v204 offset:512\n"
"buffer_load_dwordx4 v253, s[20:23], s80 offen lds\n"
"v_mfma_f32_16x16x32_bf16 v[100:103], a[16:19], a[104:107], v[100:103]\n"
"s_add_u32 s80, s80, 524288\n"
"s_add_u32 m0, s58, 48640\n"
"ds_read_b64_tr_b16 a[4:5], v204 offset:4096\n"
"v_mfma_f32_16x16x32_bf16 v[104:107], a[16:19], a[112:115], v[104:107]\n"
"ds_read_b64_tr_b16 a[6:7], v204 offset:4608\n"
"v_lshl_add_u32 v218, v217, 8, v218\n"
"v_mfma_f32_16x16x32_bf16 v[104:107], a[20:23], a[116:119], v[104:107]\n"
"ds_read_b64_tr_b16 a[8:9], v204 offset:8\n"
"v_mfma_f32_16x16x32_bf16 v[108:111], a[20:23], a[124:127], v[108:111]\n"
"ds_read_b64_tr_b16 a[10:11], v204 offset:520\n"
"v_and_b32 v219, 7, v229\n"
"v_mfma_f32_16x16x32_bf16 v[108:111], a[16:19], a[120:123], v[108:111]\n"
"ds_read_b64_tr_b16 a[12:13], v204 offset:4104\n"
"buffer_load_dwordx4 v254, s[12:15], s76 offen offset:512 lds\n"
"v_mfma_f32_16x16x32_bf16 v[112:115], a[24:27], a[96:99], v[112:115]\n"
"s_add_u32 m0, s58, 49664\n"
"ds_read_b64_tr_b16 a[14:15], v204 offset:4616\n"
"v_mfma_f32_16x16x32_bf16 v[112:115], a[28:31], a[100:103], v[112:115]\n"
"v_lshl_add_u32 v218, v219, 4, v218\n"
"v_mfma_f32_16x16x32_bf16 v[116:119], a[28:31], a[108:111], v[116:119]\n"
"v_mfma_f32_16x16x32_bf16 v[116:119], a[24:27], a[104:107], v[116:119]\n"
"v_mfma_f32_16x16x32_bf16 v[120:123], a[24:27], a[112:115], v[120:123]\n"
"buffer_load_dwordx4 v226, s[12:15], s76 offen offset:512 lds\n"
"v_lshrrev_b32 v217, 1, v217\n"
"v_mfma_f32_16x16x32_bf16 v[120:123], a[28:31], a[116:119], v[120:123]\n"
"s_add_u32 m0, s58, 50688\n"
"v_mfma_f32_16x16x32_bf16 v[124:127], a[28:31], a[124:127], v[124:127]\n"
"v_lshlrev_b32 v219, 4, v217\n"
"v_mfma_f32_16x16x32_bf16 v[124:127], a[24:27], a[120:123], v[124:127]\n"
"s_waitcnt lgkmcnt(0)\n"
"v_mfma_f32_16x16x32_bf16 v[0:3], a[0:3], a[32:35], v[0:3]\n"
"ds_read_b64_tr_b16 a[16:17], v205 offset:0\n"
"v_mfma_f32_16x16x32_bf16 v[0:3], a[4:7], a[36:39], v[0:3]\n"
"ds_read_b64_tr_b16 a[18:19], v205 offset:512\n"
"buffer_load_dwordx4 v254, s[12:15], s77 offen offset:512 lds\n"
"v_mfma_f32_16x16x32_bf16 v[4:7], a[4:7], a[44:47], v[4:7]\n"
"s_add_u32 m0, s58, 51712\n"
"ds_read_b64_tr_b16 a[20:21], v205 offset:4096\n"
"v_xor_b32 v236, v219, v218\n"
"v_mfma_f32_16x16x32_bf16 v[4:7], a[0:3], a[40:43], v[4:7]\n"
"ds_read_b64_tr_b16 a[22:23], v205 offset:4608\n"
"v_mfma_f32_16x16x32_bf16 v[8:11], a[0:3], a[48:51], v[8:11]\n"
"ds_read_b64_tr_b16 a[24:25], v205 offset:8\n"
"v_xor_b32 v237, 32, v236\n"
"v_mfma_f32_16x16x32_bf16 v[8:11], a[4:7], a[52:55], v[8:11]\n"
"ds_read_b64_tr_b16 a[26:27], v205 offset:520\n"
"v_mfma_f32_16x16x32_bf16 v[12:15], a[4:7], a[60:63], v[12:15]\n"
"ds_read_b64_tr_b16 a[28:29], v205 offset:4104\n"
"buffer_load_dwordx4 v226, s[12:15], s77 offen offset:512 lds\n"
"v_mfma_f32_16x16x32_bf16 v[12:15], a[0:3], a[56:59], v[12:15]\n"
"s_add_u32 m0, s58, 52736\n"
"ds_read_b64_tr_b16 a[30:31], v205 offset:4616\n"
"v_xor_b32 v238, 64, v236\n"
"v_mfma_f32_16x16x32_bf16 v[128:131], a[0:3], v[208:211], v[128:131]\n"
"v_mfma_f32_16x16x32_bf16 v[128:131], a[4:7], v[208:211], v[128:131]\n"
"v_xor_b32 v239, 96, v236\n"
"v_mfma_f32_16x16x32_bf16 v[16:19], a[8:11], a[32:35], v[16:19]\n"
"v_mfma_f32_16x16x32_bf16 v[16:19], a[12:15], a[36:39], v[16:19]\n"
"buffer_load_dwordx4 v254, s[12:15], s78 offen offset:512 lds\n"
"v_mfma_f32_16x16x32_bf16 v[20:23], a[12:15], a[44:47], v[20:23]\n"
"s_add_u32 m0, s58, 53760\n"
"v_lshlrev_b32 v219, 5, v217\n"
"v_mfma_f32_16x16x32_bf16 v[20:23], a[8:11], a[40:43], v[20:23]\n"
"v_mfma_f32_16x16x32_bf16 v[24:27], a[8:11], a[48:51], v[24:27]\n"
"v_xor_b32 v230, v219, v218\n"
"v_mfma_f32_16x16x32_bf16 v[24:27], a[12:15], a[52:55], v[24:27]\n"
"v_mfma_f32_16x16x32_bf16 v[28:31], a[12:15], a[60:63], v[28:31]\n"
"buffer_load_dwordx4 v226, s[12:15], s78 offen offset:512 lds\n"
"v_mfma_f32_16x16x32_bf16 v[28:31], a[8:11], a[56:59], v[28:31]\n"
"s_add_u32 m0, s58, 54784\n"
"v_xor_b32 v231, 64, v230\n"
"v_mfma_f32_16x16x32_bf16 v[132:135], a[8:11], v[208:211], v[132:135]\n"
"v_mfma_f32_16x16x32_bf16 v[132:135], a[12:15], v[208:211], v[132:135]\n"
"s_waitcnt lgkmcnt(0)\n"
"v_mfma_f32_16x16x32_bf16 v[32:35], a[16:19], a[32:35], v[32:35]\n"
"ds_read_b64_tr_b16 a[0:1], v206 offset:0\n"
"v_mfma_f32_16x16x32_bf16 v[32:35], a[20:23], a[36:39], v[32:35]\n"
"ds_read_b64_tr_b16 a[2:3], v206 offset:512\n"
"buffer_load_dwordx4 v254, s[12:15], s79 offen offset:512 lds\n"
"v_mfma_f32_16x16x32_bf16 v[36:39], a[20:23], a[44:47], v[36:39]\n"
"s_add_u32 m0, s58, 55808\n"
"ds_read_b64_tr_b16 a[4:5], v206 offset:4096\n"
"v_mfma_f32_16x16x32_bf16 v[36:39], a[16:19], a[40:43], v[36:39]\n"
"ds_read_b64_tr_b16 a[6:7], v206 offset:4608\n"
"v_mfma_f32_16x16x32_bf16 v[40:43], a[16:19], a[48:51], v[40:43]\n"
"ds_read_b64_tr_b16 a[8:9], v206 offset:8\n"
"v_mfma_f32_16x16x32_bf16 v[40:43], a[20:23], a[52:55], v[40:43]\n"
"ds_read_b64_tr_b16 a[10:11], v206 offset:520\n"
"v_mfma_f32_16x16x32_bf16 v[44:47], a[20:23], a[60:63], v[44:47]\n"
"ds_read_b64_tr_b16 a[12:13], v206 offset:4104\n"
"buffer_load_dwordx4 v226, s[12:15], s79 offen offset:512 lds\n"
"v_mfma_f32_16x16x32_bf16 v[44:47], a[16:19], a[56:59], v[44:47]\n"
"s_add_u32 m0, s81, 16384\n"
"ds_read_b64_tr_b16 a[14:15], v206 offset:4616\n"
"v_mfma_f32_16x16x32_bf16 v[48:51], a[24:27], a[32:35], v[48:51]\n"
"s_waitcnt vmcnt(24)\n"
"ds_read_b128 a[64:67], v248 offset:32768\n"
"v_mfma_f32_16x16x32_bf16 v[48:51], a[28:31], a[36:39], v[48:51]\n"
"ds_read_b128 a[68:71], v249 offset:32768\n"
"v_mfma_f32_16x16x32_bf16 v[52:55], a[28:31], a[44:47], v[52:55]\n"
"ds_read_b128 a[72:75], v248 offset:34816\n"
"v_mfma_f32_16x16x32_bf16 v[52:55], a[24:27], a[40:43], v[52:55]\n"
"ds_read_b128 a[76:79], v249 offset:34816\n"
"buffer_load_dwordx4 v252, s[16:19], s80 offen lds\n"
"v_mfma_f32_16x16x32_bf16 v[56:59], a[24:27], a[48:51], v[56:59]\n"
"s_add_u32 m0, s81, 17408\n"
"ds_read_b128 a[80:83], v248 offset:36864\n"
"v_mfma_f32_16x16x32_bf16 v[56:59], a[28:31], a[52:55], v[56:59]\n"
"ds_read_b128 a[84:87], v249 offset:36864\n"
"v_mfma_f32_16x16x32_bf16 v[60:63], a[28:31], a[60:63], v[60:63]\n"
"v_mfma_f32_16x16x32_bf16 v[60:63], a[24:27], a[56:59], v[60:63]\n"
"s_waitcnt lgkmcnt(6)\n"
"v_mfma_f32_16x16x32_bf16 v[64:67], a[0:3], a[32:35], v[64:67]\n"
"ds_read_b64_tr_b16 a[16:17], v207 offset:0\n"
"buffer_load_dwordx4 v253, s[16:19], s80 offen lds\n"
"v_mfma_f32_16x16x32_bf16 v[64:67], a[4:7], a[36:39], v[64:67]\n"
"s_add_u32 m0, s81, 24576\n"
"ds_read_b64_tr_b16 a[18:19], v207 offset:512\n"
"v_and_b32 v222, 15, v229\n"
"v_mfma_f32_16x16x32_bf16 v[68:71], a[4:7], a[44:47], v[68:71]\n"
"ds_read_b64_tr_b16 a[20:21], v207 offset:4096\n"
"v_lshlrev_b32 v222, 3, v222\n"
"v_mfma_f32_16x16x32_bf16 v[68:71], a[0:3], a[40:43], v[68:71]\n"
"ds_read_b64_tr_b16 a[22:23], v207 offset:4608\n"
"v_mfma_f32_16x16x32_bf16 v[72:75], a[0:3], a[48:51], v[72:75]\n"
"s_lshl_b32 s94, s87, 3\n"
"ds_read_b64_tr_b16 a[24:25], v207 offset:8\n"
"v_mfma_f32_16x16x32_bf16 v[72:75], a[4:7], a[52:55], v[72:75]\n"
"s_add_u32 s94, s94, 8192\n"
"ds_read_b64_tr_b16 a[26:27], v207 offset:520\n"
"buffer_load_dwordx4 v252, s[20:23], s80 offen lds\n"
"v_mfma_f32_16x16x32_bf16 v[76:79], a[4:7], a[60:63], v[76:79]\n"
"s_add_u32 m0, s81, 25600\n"
"ds_read_b64_tr_b16 a[28:29], v207 offset:4104\n"
"buffer_load_dwordx2 v[136:137], v222, s[36:39], s94 offen offset:0 sc1\n"
"v_mfma_f32_16x16x32_bf16 v[76:79], a[0:3], a[56:59], v[76:79]\n"
"ds_read_b64_tr_b16 a[30:31], v207 offset:4616\n"
"v_mfma_f32_16x16x32_bf16 v[80:83], a[8:11], a[32:35], v[80:83]\n"
"ds_read_b128 a[88:91], v248 offset:38912\n"
"buffer_load_dwordx2 v[138:139], v222, s[36:39], s94 offen offset:128 sc1\n"
"v_mfma_f32_16x16x32_bf16 v[80:83], a[12:15], a[36:39], v[80:83]\n"
"ds_read_b128 a[92:95], v249 offset:38912\n"
"v_mfma_f32_16x16x32_bf16 v[84:87], a[12:15], a[44:47], v[84:87]\n"
"buffer_load_dwordx4 v253, s[20:23], s80 offen lds\n"
"v_mfma_f32_16x16x32_bf16 v[84:87], a[8:11], a[40:43], v[84:87]\n"
"s_add_u32 s80, s80, 524288\n"
"buffer_load_dwordx2 v[140:141], v222, s[36:39], s94 offen offset:256 sc1\n"
"v_mfma_f32_16x16x32_bf16 v[88:91], a[8:11], a[48:51], v[88:91]\n"
"v_mfma_f32_16x16x32_bf16 v[88:91], a[12:15], a[52:55], v[88:91]\n"
"buffer_load_dwordx2 v[142:143], v222, s[36:39], s94 offen offset:384 sc1\n"
"v_mfma_f32_16x16x32_bf16 v[92:95], a[12:15], a[60:63], v[92:95]\n"
"v_mfma_f32_16x16x32_bf16 v[92:95], a[8:11], a[56:59], v[92:95]\n"
"s_waitcnt lgkmcnt(0)\n"
"v_mfma_f32_16x16x32_bf16 v[96:99], a[16:19], a[32:35], v[96:99]\n"
"s_waitcnt vmcnt(28)\n"
"s_barrier\n"
"v_mfma_f32_16x16x32_bf16 v[96:99], a[20:23], a[36:39], v[96:99]\n"
"s_add_u32 m0, s84, 32768\n"
"ds_read_b64_tr_b16 a[0:1], v244 offset:32768\n"
"v_mfma_f32_16x16x32_bf16 v[100:103], a[20:23], a[44:47], v[100:103]\n"
"ds_read_b64_tr_b16 a[2:3], v244 offset:33280\n"
"buffer_load_dwordx4 v236, s[24:27], s82 offen offset:0 lds\n"
"v_mfma_f32_16x16x32_bf16 v[100:103], a[16:19], a[40:43], v[100:103]\n"
"ds_read_b64_tr_b16 a[4:5], v244 offset:36864\n"
"v_mfma_f32_16x16x32_bf16 v[104:107], a[16:19], a[48:51], v[104:107]\n"
"ds_read_b64_tr_b16 a[6:7], v244 offset:37376\n"
"v_mfma_f32_16x16x32_bf16 v[104:107], a[20:23], a[52:55], v[104:107]\n"
"ds_read_b64_tr_b16 a[8:9], v244 offset:32776\n"
"v_mfma_f32_16x16x32_bf16 v[108:111], a[20:23], a[60:63], v[108:111]\n"
"ds_read_b64_tr_b16 a[10:11], v244 offset:33288\n"
"v_mfma_f32_16x16x32_bf16 v[108:111], a[16:19], a[56:59], v[108:111]\n"
"ds_read_b64_tr_b16 a[12:13], v244 offset:36872\n"
"v_mfma_f32_16x16x32_bf16 v[112:115], a[24:27], a[32:35], v[112:115]\n"
"ds_read_b64_tr_b16 a[14:15], v244 offset:37384\n"
"v_mfma_f32_16x16x32_bf16 v[112:115], a[28:31], a[36:39], v[112:115]\n"
"v_mfma_f32_16x16x32_bf16 v[116:119], a[28:31], a[44:47], v[116:119]\n"
"buffer_load_dwordx4 v237, s[24:27], s82 offen offset:1024 lds\n"
"v_mfma_f32_16x16x32_bf16 v[116:119], a[24:27], a[40:43], v[116:119]\n"
"v_mfma_f32_16x16x32_bf16 v[120:123], a[24:27], a[48:51], v[120:123]\n"
"v_mfma_f32_16x16x32_bf16 v[120:123], a[28:31], a[52:55], v[120:123]\n"
"v_mfma_f32_16x16x32_bf16 v[124:127], a[28:31], a[60:63], v[124:127]\n"
"v_mfma_f32_16x16x32_bf16 v[124:127], a[24:27], a[56:59], v[124:127]\n"
"s_waitcnt lgkmcnt(0)\n"
"v_mfma_f32_16x16x32_bf16 v[0:3], a[0:3], a[64:67], v[0:3]\n"
"ds_read_b64_tr_b16 a[16:17], v245 offset:32768\n"
"v_mov_b32 v192, 0\n"
"v_mfma_f32_16x16x32_bf16 v[0:3], a[4:7], a[68:71], v[0:3]\n"
"ds_read_b64_tr_b16 a[18:19], v245 offset:33280\n"
"v_mfma_f32_16x16x32_bf16 v[4:7], a[4:7], a[76:79], v[4:7]\n"
"ds_read_b64_tr_b16 a[20:21], v245 offset:36864\n"
"buffer_load_dwordx4 v238, s[24:27], s82 offen offset:2048 lds\n"
"v_mfma_f32_16x16x32_bf16 v[4:7], a[0:3], a[72:75], v[4:7]\n"
"ds_read_b64_tr_b16 a[22:23], v245 offset:37376\n"
"v_mfma_f32_16x16x32_bf16 v[8:11], a[0:3], a[80:83], v[8:11]\n"
"ds_read_b64_tr_b16 a[24:25], v245 offset:32776\n"
"v_mov_b32 v193, 0\n"
"v_mfma_f32_16x16x32_bf16 v[8:11], a[4:7], a[84:87], v[8:11]\n"
"ds_read_b64_tr_b16 a[26:27], v245 offset:33288\n"
"v_mfma_f32_16x16x32_bf16 v[12:15], a[4:7], a[92:95], v[12:15]\n"
"ds_read_b64_tr_b16 a[28:29], v245 offset:36872\n"
"v_mfma_f32_16x16x32_bf16 v[12:15], a[0:3], a[88:91], v[12:15]\n"
"ds_read_b64_tr_b16 a[30:31], v245 offset:37384\n"
"v_mfma_f32_16x16x32_bf16 v[128:131], a[0:3], v[208:211], v[128:131]\n"
"v_mov_b32 v194, 0\n"
"v_mfma_f32_16x16x32_bf16 v[128:131], a[4:7], v[208:211], v[128:131]\n"
"v_mfma_f32_16x16x32_bf16 v[16:19], a[8:11], a[64:67], v[16:19]\n"
"buffer_load_dwordx4 v239, s[24:27], s82 offen offset:3072 lds\n"
"v_mfma_f32_16x16x32_bf16 v[16:19], a[12:15], a[68:71], v[16:19]\n"
"s_add_u32 s82, s82, 16384\n"
"s_and_b32 s82, s82, 0x1ffff\n"
"s_add_u32 m0, s84, 16384\n"
"v_mfma_f32_16x16x32_bf16 v[20:23], a[12:15], a[76:79], v[20:23]\n"
"v_mov_b32 v195, 0\n"
"v_mfma_f32_16x16x32_bf16 v[20:23], a[8:11], a[72:75], v[20:23]\n"
"v_mfma_f32_16x16x32_bf16 v[24:27], a[8:11], a[80:83], v[24:27]\n"
"v_mfma_f32_16x16x32_bf16 v[24:27], a[12:15], a[84:87], v[24:27]\n"
"v_mfma_f32_16x16x32_bf16 v[28:31], a[12:15], a[92:95], v[28:31]\n"
"v_mfma_f32_16x16x32_bf16 v[28:31], a[8:11], a[88:91], v[28:31]\n"
"v_mov_b32 v196, 0\n"
"v_mfma_f32_16x16x32_bf16 v[132:135], a[8:11], v[208:211], v[132:135]\n"
"buffer_load_dwordx4 v236, s[24:27], s82 offen offset:0 lds\n"
"v_mfma_f32_16x16x32_bf16 v[132:135], a[12:15], v[208:211], v[132:135]\n"
"s_waitcnt lgkmcnt(0)\n"
"v_mfma_f32_16x16x32_bf16 v[32:35], a[16:19], a[64:67], v[32:35]\n"
"s_lshl_b32 s95, s59, 7\n"
"s_sub_u32 s96, 2211840, s95\n"
"ds_read_b64_tr_b16 a[0:1], v246 offset:32768\n"
"v_mfma_f32_16x16x32_bf16 v[32:35], a[20:23], a[68:71], v[32:35]\n"
"s_mul_i32 s96, s96, s88\n"
"ds_read_b64_tr_b16 a[2:3], v246 offset:33280\n"
"v_mov_b32 v197, 0\n"
"v_mfma_f32_16x16x32_bf16 v[36:39], a[20:23], a[76:79], v[36:39]\n"
"s_add_u32 s16, s50, s95\n"
"s_addc_u32 s17, s51, 0\n"
"ds_read_b64_tr_b16 a[4:5], v246 offset:36864\n"
"v_mfma_f32_16x16x32_bf16 v[36:39], a[16:19], a[72:75], v[36:39]\n"
"s_and_b32 s17, s17, 0xffff\n"
"s_mov_b32 s18, s96\n"
"ds_read_b64_tr_b16 a[6:7], v246 offset:37376\n"
"v_mfma_f32_16x16x32_bf16 v[40:43], a[16:19], a[80:83], v[40:43]\n"
"s_add_u32 s20, s52, s95\n"
"s_addc_u32 s21, s53, 0\n"
"ds_read_b64_tr_b16 a[8:9], v246 offset:32776\n"
"v_mfma_f32_16x16x32_bf16 v[40:43], a[20:23], a[84:87], v[40:43]\n"
"ds_read_b64_tr_b16 a[10:11], v246 offset:33288\n"
"v_mov_b32 v198, 0\n"
"v_mfma_f32_16x16x32_bf16 v[44:47], a[20:23], a[92:95], v[44:47]\n"
"ds_read_b64_tr_b16 a[12:13], v246 offset:36872\n"
"buffer_load_dwordx4 v237, s[24:27], s82 offen offset:1024 lds\n"
"v_mfma_f32_16x16x32_bf16 v[44:47], a[16:19], a[88:91], v[44:47]\n"
"s_and_b32 s21, s21, 0xffff\n"
"s_mov_b32 s22, s96\n"
"ds_read_b64_tr_b16 a[14:15], v246 offset:37384\n"
"v_mfma_f32_16x16x32_bf16 v[48:51], a[24:27], a[64:67], v[48:51]\n"
"s_waitcnt vmcnt(26)\n"
"ds_read_b128 a[96:99], v250 offset:0\n"
"v_mfma_f32_16x16x32_bf16 v[48:51], a[28:31], a[68:71], v[48:51]\n"
"ds_read_b128 a[100:103], v251 offset:0\n"
"v_mov_b32 v199, 0\n"
"v_mfma_f32_16x16x32_bf16 v[52:55], a[28:31], a[76:79], v[52:55]\n"
"s_lshl2_add_u32 s40, s75, s68\n"
"s_add_u32 s41, s40, 1024\n"
"ds_read_b128 a[104:107], v250 offset:2048\n"
"v_mfma_f32_16x16x32_bf16 v[52:55], a[24:27], a[72:75], v[52:55]\n"
"s_mov_b32 s60, s48\n"
"s_and_b32 s61, s49, 0xffff\n"
"ds_read_b128 a[108:111], v251 offset:2048\n"
"v_mfma_f32_16x16x32_bf16 v[56:59], a[24:27], a[80:83], v[56:59]\n"
"s_mov_b32 s62, 5242880\n"
"s_mov_b32 s67, 0x20000\n"
"ds_read_b128 a[112:115], v250 offset:4096\n"
"v_mfma_f32_16x16x32_bf16 v[56:59], a[28:31], a[84:87], v[56:59]\n"
"s_cmp_lt_u32 s41, 1280\n"
"s_cselect_b64 s[64:65], s[48:49], s[54:55]\n"
"ds_read_b128 a[116:119], v251 offset:4096\n"
"v_mfma_f32_16x16x32_bf16 v[60:63], a[28:31], a[92:95], v[60:63]\n"
"buffer_load_dwordx4 v238, s[24:27], s82 offen offset:2048 lds\n"
"v_mov_b32 v200, 0\n"
"v_mfma_f32_16x16x32_bf16 v[60:63], a[24:27], a[88:91], v[60:63]\n"
"s_waitcnt lgkmcnt(6)\n"
"v_mfma_f32_16x16x32_bf16 v[64:67], a[0:3], a[64:67], v[64:67]\n"
"s_cmp_lt_u32 s41, 1280\n"
"s_cselect_b32 s66, s62, 2211840\n"
"ds_read_b64_tr_b16 a[16:17], v247 offset:32768\n"
"v_mfma_f32_16x16x32_bf16 v[64:67], a[4:7], a[68:71], v[64:67]\n"
"s_cmp_lt_u32 s41, 1280\n"
"s_cselect_b32 s97, 0, 1280\n"
"ds_read_b64_tr_b16 a[18:19], v247 offset:33280\n"
"v_mfma_f32_16x16x32_bf16 v[68:71], a[4:7], a[76:79], v[68:71]\n"
"ds_read_b64_tr_b16 a[20:21], v247 offset:36864\n"
"v_mov_b32 v201, 0\n"
"v_mfma_f32_16x16x32_bf16 v[68:71], a[0:3], a[72:75], v[68:71]\n"
"s_and_b32 s65, s65, 0xffff\n"
"s_sub_u32 s97, s41, s97\n"
"ds_read_b64_tr_b16 a[22:23], v247 offset:37376\n"
"v_mfma_f32_16x16x32_bf16 v[72:75], a[0:3], a[80:83], v[72:75]\n"
"s_mul_i32 s95, s40, 6554\n"
"s_lshr_b32 s95, s95, 16\n"
"ds_read_b64_tr_b16 a[24:25], v247 offset:32776\n"
"v_mfma_f32_16x16x32_bf16 v[72:75], a[4:7], a[84:87], v[72:75]\n"
"s_mul_i32 s96, s95, 10\n"
"s_sub_u32 s96, s40, s96\n"
"ds_read_b64_tr_b16 a[26:27], v247 offset:33288\n"
"v_mfma_f32_16x16x32_bf16 v[76:79], a[4:7], a[92:95], v[76:79]\n"
"ds_read_b64_tr_b16 a[28:29], v247 offset:36872\n"
"buffer_load_dwordx4 v239, s[24:27], s82 offen offset:3072 lds\n"
"v_mfma_f32_16x16x32_bf16 v[76:79], a[0:3], a[88:91], v[76:79]\n"
"s_add_u32 s82, s82, 16384\n"
"s_and_b32 s82, s82, 0x1ffff\n"
"ds_read_b64_tr_b16 a[30:31], v247 offset:37384\n"
"v_mfma_f32_16x16x32_bf16 v[80:83], a[8:11], a[64:67], v[80:83]\n"
"s_add_u32 m0, s84, 0\n"
"ds_read_b128 a[120:123], v250 offset:6144\n"
"v_mov_b32 v202, 0\n"
"v_mfma_f32_16x16x32_bf16 v[80:83], a[12:15], a[68:71], v[80:83]\n"
"s_lshl_b32 s96, s96, 7\n"
"s_mul_i32 s42, s95, 40960\n"
"ds_read_b128 a[124:127], v251 offset:6144\n"
"v_mfma_f32_16x16x32_bf16 v[84:87], a[12:15], a[76:79], v[84:87]\n"
"s_add_u32 s42, s42, s96\n"
"v_mov_b32 v203, 0\n"
"v_mfma_f32_16x16x32_bf16 v[84:87], a[8:11], a[72:75], v[84:87]\n"
"s_mul_i32 s95, s97, 6554\n"
"s_lshr_b32 s95, s95, 16\n"
"v_mfma_f32_16x16x32_bf16 v[88:91], a[8:11], a[80:83], v[88:91]\n"
"s_mul_i32 s96, s95, 10\n"
"s_sub_u32 s96, s97, s96\n"
"v_mfma_f32_16x16x32_bf16 v[88:91], a[12:15], a[84:87], v[88:91]\n"
"s_lshl_b32 s96, s96, 7\n"
"s_mul_i32 s43, s95, 40960\n"
"v_mfma_f32_16x16x32_bf16 v[92:95], a[12:15], a[92:95], v[92:95]\n"
"s_add_u32 s43, s43, s96\n"
"buffer_load_dwordx4 v236, s[24:27], s82 offen offset:0 lds\n"
"v_mfma_f32_16x16x32_bf16 v[92:95], a[8:11], a[88:91], v[92:95]\n"
"s_waitcnt lgkmcnt(0)\n"
"v_mov_b32 v204, 0\n"
"v_mfma_f32_16x16x32_bf16 v[96:99], a[16:19], a[64:67], v[96:99]\n"
"s_waitcnt vmcnt(11)\n"
"s_barrier\n"
"v_mfma_f32_16x16x32_bf16 v[96:99], a[20:23], a[68:71], v[96:99]\n"
"s_lshl_b32 s40, s40, 12\n"
"s_add_u32 s40, s40, 3145728\n"
"ds_read_b64_tr_b16 a[0:1], v244 offset:0\n"
"v_mfma_f32_16x16x32_bf16 v[100:103], a[20:23], a[76:79], v[100:103]\n"
"s_cmp_lt_u32 s41, 1820\n"
"s_cselect_b32 s95, 3145728, s38\n"
"ds_read_b64_tr_b16 a[2:3], v244 offset:512\n"
"v_mfma_f32_16x16x32_bf16 v[100:103], a[16:19], a[72:75], v[100:103]\n"
"ds_read_b64_tr_b16 a[4:5], v244 offset:4096\n"
"v_mov_b32 v205, 0\n"
"v_mfma_f32_16x16x32_bf16 v[104:107], a[16:19], a[80:83], v[104:107]\n"
"s_lshl_b32 s41, s41, 12\n"
"s_add_u32 s41, s41, s95\n"
"ds_read_b64_tr_b16 a[6:7], v244 offset:4608\n"
"v_mfma_f32_16x16x32_bf16 v[104:107], a[20:23], a[84:87], v[104:107]\n"
"ds_read_b64_tr_b16 a[8:9], v244 offset:8\n"
"v_mfma_f32_16x16x32_bf16 v[108:111], a[20:23], a[92:95], v[108:111]\n"
"ds_read_b64_tr_b16 a[10:11], v244 offset:520\n"
"buffer_load_dwordx4 v237, s[24:27], s82 offen offset:1024 lds\n"
"v_mfma_f32_16x16x32_bf16 v[108:111], a[16:19], a[88:91], v[108:111]\n"
"ds_read_b64_tr_b16 a[12:13], v244 offset:4104\n"
"v_mov_b32 v206, 0\n"
"v_mfma_f32_16x16x32_bf16 v[112:115], a[24:27], a[64:67], v[112:115]\n"
"ds_read_b64_tr_b16 a[14:15], v244 offset:4616\n"
"v_mfma_f32_16x16x32_bf16 v[112:115], a[28:31], a[68:71], v[112:115]\n"
"v_mfma_f32_16x16x32_bf16 v[116:119], a[28:31], a[76:79], v[116:119]\n"
"s_nop 0\n"
"ds_read_b128 a[32:35], v248 offset:0\n"
"v_mfma_f32_16x16x32_bf16 v[116:119], a[24:27], a[72:75], v[116:119]\n"
"ds_read_b128 a[36:39], v249 offset:0\n"
"v_mov_b32 v207, 0\n"
"v_mfma_f32_16x16x32_bf16 v[120:123], a[24:27], a[80:83], v[120:123]\n"
"ds_read_b128 a[40:43], v248 offset:2048\n"
"v_mfma_f32_16x16x32_bf16 v[120:123], a[28:31], a[84:87], v[120:123]\n"
"ds_read_b128 a[44:47], v249 offset:2048\n"
"v_mfma_f32_16x16x32_bf16 v[124:127], a[28:31], a[92:95], v[124:127]\n"
"ds_read_b128 a[48:51], v248 offset:4096\n"
"buffer_load_dwordx4 v238, s[24:27], s82 offen offset:2048 lds\n"
"v_mfma_f32_16x16x32_bf16 v[124:127], a[24:27], a[88:91], v[124:127]\n"
"s_waitcnt lgkmcnt(5)\n"
"v_mfma_f32_16x16x32_bf16 v[0:3], a[0:3], a[96:99], v[0:3]\n"
"s_waitcnt vmcnt(11)\n"
"s_nop 0\n"
"ds_read_b64_tr_b16 a[16:17], v244 offset:16384\n"
"v_mfma_f32_16x16x32_bf16 v[0:3], a[4:7], a[100:103], v[0:3]\n"
"ds_read_b64_tr_b16 a[18:19], v244 offset:16896\n"
"v_xor_b32 v137, s73, v137\n"
"v_mfma_f32_16x16x32_bf16 v[4:7], a[4:7], a[108:111], v[4:7]\n"
"ds_read_b64_tr_b16 a[20:21], v244 offset:20480\n"
"v_xor_b32 v139, s73, v139\n"
"v_mfma_f32_16x16x32_bf16 v[4:7], a[0:3], a[104:107], v[4:7]\n"
"ds_read_b64_tr_b16 a[22:23], v244 offset:20992\n"
"v_xor_b32 v141, s73, v141\n"
"v_mfma_f32_16x16x32_bf16 v[8:11], a[0:3], a[112:115], v[8:11]\n"
"ds_read_b64_tr_b16 a[24:25], v244 offset:16392\n"
"v_xor_b32 v143, s73, v143\n"
"v_mfma_f32_16x16x32_bf16 v[8:11], a[4:7], a[116:119], v[8:11]\n"
"ds_read_b64_tr_b16 a[26:27], v244 offset:16904\n"
"v_or3_b32 v137, v137, v139, v141\n"
"v_mfma_f32_16x16x32_bf16 v[12:15], a[4:7], a[124:127], v[12:15]\n"
"ds_read_b64_tr_b16 a[28:29], v244 offset:20488\n"
"buffer_load_dwordx4 v239, s[24:27], s82 offen offset:3072 lds\n"
"v_mfma_f32_16x16x32_bf16 v[12:15], a[0:3], a[120:123], v[12:15]\n"
"s_add_u32 s82, s82, 16384\n"
"s_and_b32 s82, s82, 0x1ffff\n"
"ds_read_b64_tr_b16 a[30:31], v244 offset:21000\n"
"v_mfma_f32_16x16x32_bf16 v[128:131], a[0:3], v[208:211], v[128:131]\n"
"s_add_u32 m0, s85, 0\n"
"ds_read_b128 a[52:55], v249 offset:4096\n"
"v_or_b32 v137, v137, v143\n"
"v_mfma_f32_16x16x32_bf16 v[128:131], a[4:7], v[208:211], v[128:131]\n"
"ds_read_b128 a[56:59], v248 offset:6144\n"
"v_cmp_ne_u32 vcc, 0, v137\n"
"v_mfma_f32_16x16x32_bf16 v[16:19], a[8:11], a[96:99], v[16:19]\n"
"s_cbranch_vccnz .L_stats_retry\n"
".L_stats_ok:\n"
"ds_read_b128 a[60:63], v249 offset:6144\n"
"v_mfma_f32_16x16x32_bf16 v[16:19], a[12:15], a[100:103], v[16:19]\n"
".L_pad3:\n"
"s_cbranch_execz .L_pad7\n"
"v_and_b32 v176, 0xffff0000, v136\n"
"v_lshlrev_b32 v177, 16, v136\n"
"v_mfma_f32_16x16x32_bf16 v[20:23], a[12:15], a[108:111], v[20:23]\n"
"ds_write_b64 v168, v[146:147] offset:0\n"
"v_and_b32 v178, 0xffff0000, v138\n"
"v_mfma_f32_16x16x32_bf16 v[20:23], a[8:11], a[104:107], v[20:23]\n"
"v_lshlrev_b32 v179, 16, v138\n"
"v_and_b32 v180, 0xffff0000, v140\n"
"v_mfma_f32_16x16x32_bf16 v[24:27], a[8:11], a[112:115], v[24:27]\n"
"buffer_load_dwordx4 v230, s[28:31], s83 offen offset:0 lds\n"
"v_lshlrev_b32 v181, 16, v140\n"
"v_mfma_f32_16x16x32_bf16 v[24:27], a[12:15], a[116:119], v[24:27]\n"
"v_and_b32 v182, 0xffff0000, v142\n"
"v_lshlrev_b32 v183, 16, v142\n"
"v_mfma_f32_16x16x32_bf16 v[28:31], a[12:15], a[124:127], v[28:31]\n"
"v_rcp_f32 v176, v176\n"
"v_mfma_f32_16x16x32_bf16 v[28:31], a[8:11], a[120:123], v[28:31]\n"
"v_rcp_f32 v178, v178\n"
"v_mfma_f32_16x16x32_bf16 v[132:135], a[8:11], v[208:211], v[132:135]\n"
"v_rcp_f32 v180, v180\n"
"v_mfma_f32_16x16x32_bf16 v[132:135], a[12:15], v[208:211], v[132:135]\n"
"s_waitcnt lgkmcnt(1)\n"
"ds_write_b64 v168, v[148:149] offset:256\n"
"v_mfma_f32_16x16x32_bf16 v[0:3], a[16:19], a[32:35], v[0:3]\n"
"ds_read_b64_tr_b16 a[0:1], v245 offset:0\n"
"v_mfma_f32_16x16x32_bf16 v[0:3], a[20:23], a[36:39], v[0:3]\n"
"ds_read_b64_tr_b16 a[2:3], v245 offset:512\n"
"v_mfma_f32_16x16x32_bf16 v[4:7], a[20:23], a[44:47], v[4:7]\n"
"ds_read_b64_tr_b16 a[4:5], v245 offset:4096\n"
"buffer_load_dwordx4 v231, s[28:31], s83 offen offset:1024 lds\n"
"v_mfma_f32_16x16x32_bf16 v[4:7], a[16:19], a[40:43], v[4:7]\n"
"ds_read_b64_tr_b16 a[6:7], v245 offset:4608\n"
"v_mfma_f32_16x16x32_bf16 v[8:11], a[16:19], a[48:51], v[8:11]\n"
"ds_read_b64_tr_b16 a[8:9], v245 offset:8\n"
"v_mfma_f32_16x16x32_bf16 v[8:11], a[20:23], a[52:55], v[8:11]\n"
"ds_read_b64_tr_b16 a[10:11], v245 offset:520\n"
"v_mfma_f32_16x16x32_bf16 v[12:15], a[20:23], a[60:63], v[12:15]\n"
"ds_read_b64_tr_b16 a[12:13], v245 offset:4104\n"
"v_mfma_f32_16x16x32_bf16 v[12:15], a[16:19], a[56:59], v[12:15]\n"
"ds_read_b64_tr_b16 a[14:15], v245 offset:4616\n"
"v_mfma_f32_16x16x32_bf16 v[128:131], a[16:19], v[208:211], v[128:131]\n"
"ds_write_b64 v168, v[150:151] offset:512\n"
"v_mfma_f32_16x16x32_bf16 v[128:131], a[20:23], v[208:211], v[128:131]\n"
"v_rcp_f32 v182, v182\n"
"v_mfma_f32_16x16x32_bf16 v[16:19], a[24:27], a[32:35], v[16:19]\n"
"buffer_load_dwordx4 v230, s[28:31], s83 offen offset:2048 lds\n"
"v_mul_f32 v176, 0x3e0293ee, v176\n"
"v_mfma_f32_16x16x32_bf16 v[16:19], a[28:31], a[36:39], v[16:19]\n"
"v_mul_f32 v178, 0x3e0293ee, v178\n"
"v_mul_f32 v180, 0x3e0293ee, v180\n"
"v_mfma_f32_16x16x32_bf16 v[20:23], a[28:31], a[44:47], v[20:23]\n"
"v_mul_f32 v182, 0x3e0293ee, v182\n"
"v_mul_f32 v177, v177, v176\n"
"v_mfma_f32_16x16x32_bf16 v[20:23], a[24:27], a[40:43], v[20:23]\n"
"v_mul_f32 v179, v179, v178\n"
"v_mul_f32 v181, v181, v180\n"
"v_mfma_f32_16x16x32_bf16 v[24:27], a[24:27], a[48:51], v[24:27]\n"
"ds_write_b64 v168, v[152:153] offset:768\n"
"v_mul_f32 v183, v183, v182\n"
"v_mfma_f32_16x16x32_bf16 v[24:27], a[28:31], a[52:55], v[24:27]\n"
"v_sub_f32 v177, 0, v177\n"
"v_sub_f32 v179, 0, v179\n"
"v_mfma_f32_16x16x32_bf16 v[28:31], a[28:31], a[60:63], v[28:31]\n"
"v_sub_f32 v181, 0, v181\n"
"v_sub_f32 v183, 0, v183\n"
"v_mfma_f32_16x16x32_bf16 v[28:31], a[24:27], a[56:59], v[28:31]\n"
"v_mfma_f32_16x16x32_bf16 v[132:135], a[24:27], v[208:211], v[132:135]\n"
"buffer_load_dwordx4 v231, s[28:31], s83 offen offset:3072 lds\n"
"v_and_b32 v184, 7, v229\n"
"v_mfma_f32_16x16x32_bf16 v[132:135], a[28:31], v[208:211], v[132:135]\n"
"s_waitcnt lgkmcnt(2)\n"
"s_add_u32 s83, s83, 16384\n"
"s_and_b32 s83, s83, 0x1ffff\n"
"v_mfma_f32_16x16x32_bf16 v[32:35], a[0:3], a[96:99], v[32:35]\n"
"s_add_u32 m0, s85, 16384\n"
"ds_read_b64_tr_b16 a[16:17], v245 offset:16384\n"
"v_mfma_f32_16x16x32_bf16 v[32:35], a[4:7], a[100:103], v[32:35]\n"
"ds_read_b64_tr_b16 a[18:19], v245 offset:16896\n"
"v_mfma_f32_16x16x32_bf16 v[36:39], a[4:7], a[108:111], v[36:39]\n"
"ds_read_b64_tr_b16 a[20:21], v245 offset:20480\n"
"v_mfma_f32_16x16x32_bf16 v[36:39], a[0:3], a[104:107], v[36:39]\n"
"ds_read_b64_tr_b16 a[22:23], v245 offset:20992\n"
"v_lshrrev_b32 v185, 3, v229\n"
"v_mfma_f32_16x16x32_bf16 v[40:43], a[0:3], a[112:115], v[40:43]\n"
"ds_read_b64_tr_b16 a[24:25], v245 offset:16392\n"
"v_mfma_f32_16x16x32_bf16 v[40:43], a[4:7], a[116:119], v[40:43]\n"
"s_lshl_b32 s90, s68, 7\n"
"ds_read_b64_tr_b16 a[26:27], v245 offset:16904\n"
"v_lshrrev_b32 v221, 4, v229\n"
"v_mfma_f32_16x16x32_bf16 v[44:47], a[4:7], a[124:127], v[44:47]\n"
"ds_read_b64_tr_b16 a[28:29], v245 offset:20488\n"
"buffer_load_dwordx4 v230, s[28:31], s83 offen offset:0 lds\n"
"v_mfma_f32_16x16x32_bf16 v[44:47], a[0:3], a[120:123], v[44:47]\n"
"ds_read_b64_tr_b16 a[30:31], v245 offset:21000\n"
"v_lshlrev_b32 v221, 4, v221\n"
"v_mfma_f32_16x16x32_bf16 v[48:51], a[8:11], a[96:99], v[48:51]\n"
"ds_write_b64 v168, v[154:155] offset:1024\n"
"v_add_u32 v223, s90, v221\n"
"v_mfma_f32_16x16x32_bf16 v[48:51], a[12:15], a[100:103], v[48:51]\n"
"ds_write_b128 v223, v[128:131] offset:45056\n"
"v_mul_u32_u24 v186, 1296, v184\n"
"v_mfma_f32_16x16x32_bf16 v[52:55], a[12:15], a[108:111], v[52:55]\n"
"ds_write_b128 v223, v[132:135] offset:45120\n"
"v_mul_f32 v0, v176, v0\n"
"v_mfma_f32_16x16x32_bf16 v[52:55], a[8:11], a[104:107], v[52:55]\n"
"v_mul_f32 v1, v176, v1\n"
"v_mul_f32 v2, v176, v2\n"
"v_mfma_f32_16x16x32_bf16 v[56:59], a[8:11], a[112:115], v[56:59]\n"
"v_mul_f32 v3, v176, v3\n"
"v_mul_f32 v16, v176, v16\n"
"v_mfma_f32_16x16x32_bf16 v[56:59], a[12:15], a[116:119], v[56:59]\n"
"v_lshl_add_u32 v186, v185, 4, v186\n"
"v_mul_f32 v17, v176, v17\n"
"v_mfma_f32_16x16x32_bf16 v[60:63], a[12:15], a[124:127], v[60:63]\n"
"buffer_load_dwordx4 v231, s[28:31], s83 offen offset:1024 lds\n"
"v_mul_f32 v18, v176, v18\n"
"v_mfma_f32_16x16x32_bf16 v[60:63], a[8:11], a[120:123], v[60:63]\n"
"s_waitcnt lgkmcnt(3)\n"
"v_mul_f32 v19, v176, v19\n"
"v_mul_f32 v4, v178, v4\n"
"v_mfma_f32_16x16x32_bf16 v[32:35], a[16:19], a[32:35], v[32:35]\n"
"ds_read_b64_tr_b16 a[0:1], v246 offset:0\n"
"v_mul_f32 v5, v178, v5\n"
"v_mfma_f32_16x16x32_bf16 v[32:35], a[20:23], a[36:39], v[32:35]\n"
"ds_read_b64_tr_b16 a[2:3], v246 offset:512\n"
"v_mul_f32 v6, v178, v6\n"
"v_mfma_f32_16x16x32_bf16 v[36:39], a[20:23], a[44:47], v[36:39]\n"
"s_waitcnt vmcnt(14) lgkmcnt(2)\n"
"s_barrier\n"
"ds_read_b64_tr_b16 a[4:5], v246 offset:4096\n"
"v_mfma_f32_16x16x32_bf16 v[36:39], a[16:19], a[40:43], v[36:39]\n"
"s_lshl_b32 s45, s68, 7\n"
"ds_read_b64_tr_b16 a[6:7], v246 offset:4608\n"
"v_xor_b32 v221, 0, v223\n"
"v_mfma_f32_16x16x32_bf16 v[40:43], a[16:19], a[48:51], v[40:43]\n"
"s_add_u32 s45, s45, 32768\n"
"ds_read_b64_tr_b16 a[8:9], v246 offset:8\n"
"v_mul_f32 v7, v178, v7\n"
"v_mfma_f32_16x16x32_bf16 v[40:43], a[20:23], a[52:55], v[40:43]\n"
"s_and_b32 s46, s68, 1\n"
"ds_read_b64_tr_b16 a[10:11], v246 offset:520\n"
"v_mul_f32 v20, v178, v20\n"
"v_mfma_f32_16x16x32_bf16 v[44:47], a[20:23], a[60:63], v[44:47]\n"
"ds_read_b64_tr_b16 a[12:13], v246 offset:4104\n"
"buffer_load_dwordx4 v230, s[28:31], s83 offen offset:2048 lds\n"
"v_mfma_f32_16x16x32_bf16 v[44:47], a[16:19], a[56:59], v[44:47]\n"
"s_lshl_b32 s47, s46, 7\n"
"ds_read_b64_tr_b16 a[14:15], v246 offset:4616\n"
"v_mul_f32 v21, v178, v21\n"
"v_mfma_f32_16x16x32_bf16 v[48:51], a[24:27], a[32:35], v[48:51]\n"
"s_add_u32 s47, s47, 33792\n"
"ds_read_b128 v[144:147], v221 offset:45056\n"
"v_mul_f32 v22, v178, v22\n"
"v_mfma_f32_16x16x32_bf16 v[48:51], a[28:31], a[36:39], v[48:51]\n"
"ds_read_b128 v[148:151], v221 offset:45120\n"
"v_xor_b32 v221, 128, v223\n"
"v_mfma_f32_16x16x32_bf16 v[52:55], a[28:31], a[44:47], v[52:55]\n"
"ds_read_b128 v[152:155], v221 offset:45056\n"
"v_add_u32 v187, s47, v186\n"
"v_mfma_f32_16x16x32_bf16 v[52:55], a[24:27], a[40:43], v[52:55]\n"
"ds_read_b128 v[156:159], v221 offset:45120\n"
"v_xor_b32 v221, 256, v223\n"
"v_mfma_f32_16x16x32_bf16 v[56:59], a[24:27], a[48:51], v[56:59]\n"
"ds_read_b128 v[160:163], v221 offset:45056\n"
"v_mul_f32 v23, v178, v23\n"
"v_mfma_f32_16x16x32_bf16 v[56:59], a[28:31], a[52:55], v[56:59]\n"
"ds_read_b128 v[164:167], v221 offset:45120\n"
"v_xor_b32 v221, 384, v223\n"
"v_mfma_f32_16x16x32_bf16 v[60:63], a[28:31], a[60:63], v[60:63]\n"
"buffer_load_dwordx4 v231, s[28:31], s83 offen offset:3072 lds\n"
"ds_read_b128 v[168:171], v221 offset:45056\n"
"v_mfma_f32_16x16x32_bf16 v[60:63], a[24:27], a[56:59], v[60:63]\n"
"s_waitcnt lgkmcnt(7)\n"
"s_add_u32 s83, s83, 16384\n"
"s_and_b32 s83, s83, 0x1ffff\n"
"v_mfma_f32_16x16x32_bf16 v[64:67], a[0:3], a[96:99], v[64:67]\n"
"ds_read_b64_tr_b16 a[16:17], v246 offset:16384\n"
"v_add_u32 v186, s45, v186\n"
"v_mfma_f32_16x16x32_bf16 v[64:67], a[4:7], a[100:103], v[64:67]\n"
"ds_read_b64_tr_b16 a[18:19], v246 offset:16896\n"
"v_mul_f32 v8, v180, v8\n"
"v_mfma_f32_16x16x32_bf16 v[68:71], a[4:7], a[108:111], v[68:71]\n"
"ds_read_b64_tr_b16 a[20:21], v246 offset:20480\n"
"v_mul_f32 v9, v180, v9\n"
"v_mfma_f32_16x16x32_bf16 v[68:71], a[0:3], a[104:107], v[68:71]\n"
"ds_read_b64_tr_b16 a[22:23], v246 offset:20992\n"
"v_mul_f32 v10, v180, v10\n"
"v_mfma_f32_16x16x32_bf16 v[72:75], a[0:3], a[112:115], v[72:75]\n"
"s_waitcnt lgkmcnt(7)\n"
"ds_read_b64_tr_b16 a[24:25], v246 offset:16392\n"
"v_mul_f32 v11, v180, v11\n"
"v_mfma_f32_16x16x32_bf16 v[72:75], a[4:7], a[116:119], v[72:75]\n"
"ds_read_b64_tr_b16 a[26:27], v246 offset:16904\n"
"v_mul_f32 v24, v180, v24\n"
"v_mfma_f32_16x16x32_bf16 v[76:79], a[4:7], a[124:127], v[76:79]\n"
"ds_read_b64_tr_b16 a[28:29], v246 offset:20488\n"
"v_mul_f32 v25, v180, v25\n"
"v_mfma_f32_16x16x32_bf16 v[76:79], a[0:3], a[120:123], v[76:79]\n"
"ds_read_b64_tr_b16 a[30:31], v246 offset:21000\n"
"v_mul_f32 v26, v180, v26\n"
"v_mfma_f32_16x16x32_bf16 v[80:83], a[8:11], a[96:99], v[80:83]\n"
"ds_read_b128 v[172:175], v221 offset:45120\n"
"v_mul_f32 v27, v180, v27\n"
"v_mfma_f32_16x16x32_bf16 v[80:83], a[12:15], a[100:103], v[80:83]\n"
"ds_read_b128 a[240:243], v186\n"
"v_mul_f32 v12, v182, v12\n"
"v_mfma_f32_16x16x32_bf16 v[84:87], a[12:15], a[108:111], v[84:87]\n"
"ds_read_b128 a[244:247], v186 offset:512\n"
"v_mul_f32 v13, v182, v13\n"
"v_mfma_f32_16x16x32_bf16 v[84:87], a[8:11], a[104:107], v[84:87]\n"
"v_mul_f32 v14, v182, v14\n"
"v_mul_f32 v15, v182, v15\n"
"v_mfma_f32_16x16x32_bf16 v[88:91], a[8:11], a[112:115], v[88:91]\n"
"v_mul_f32 v28, v182, v28\n"
"v_mul_f32 v29, v182, v29\n"
"v_mfma_f32_16x16x32_bf16 v[88:91], a[12:15], a[116:119], v[88:91]\n"
"v_mul_f32 v30, v182, v30\n"
"v_mul_f32 v31, v182, v31\n"
"v_mfma_f32_16x16x32_bf16 v[92:95], a[12:15], a[124:127], v[92:95]\n"
"v_fmac_f32 v0, v177, v144\n"
"v_fmac_f32 v1, v177, v145\n"
"v_mfma_f32_16x16x32_bf16 v[92:95], a[8:11], a[120:123], v[92:95]\n"
"s_waitcnt lgkmcnt(3)\n"
"ds_read_b128 a[248:251], v187\n"
"v_fmac_f32 v2, v177, v146\n"
"v_mfma_f32_16x16x32_bf16 v[64:67], a[16:19], a[32:35], v[64:67]\n"
"ds_read_b64_tr_b16 a[0:1], v247 offset:0\n"
"v_fmac_f32 v3, v177, v147\n"
"v_mfma_f32_16x16x32_bf16 v[64:67], a[20:23], a[36:39], v[64:67]\n"
"ds_read_b64_tr_b16 a[2:3], v247 offset:512\n"
"v_cvt_pk_bf16_f32 v0, v0, v1\n"
"v_mfma_f32_16x16x32_bf16 v[68:71], a[20:23], a[44:47], v[68:71]\n"
"ds_read_b64_tr_b16 a[4:5], v247 offset:4096\n"
"v_cvt_pk_bf16_f32 v1, v2, v3\n"
"v_mfma_f32_16x16x32_bf16 v[68:71], a[16:19], a[40:43], v[68:71]\n"
"ds_read_b64_tr_b16 a[6:7], v247 offset:4608\n"
"v_accvgpr_write_b32 a144, v0\n"
"v_mfma_f32_16x16x32_bf16 v[72:75], a[16:19], a[48:51], v[72:75]\n"
"ds_read_b64_tr_b16 a[8:9], v247 offset:8\n"
"v_lshlrev_b32 v185, 8, v185\n"
"v_mfma_f32_16x16x32_bf16 v[72:75], a[20:23], a[52:55], v[72:75]\n"
"ds_read_b64_tr_b16 a[10:11], v247 offset:520\n"
"v_accvgpr_write_b32 a145, v1\n"
"v_mfma_f32_16x16x32_bf16 v[76:79], a[20:23], a[60:63], v[76:79]\n"
"ds_read_b64_tr_b16 a[12:13], v247 offset:4104\n"
"v_fmac_f32 v16, v177, v148\n"
"v_mfma_f32_16x16x32_bf16 v[76:79], a[16:19], a[56:59], v[76:79]\n"
"ds_read_b64_tr_b16 a[14:15], v247 offset:4616\n"
"v_fmac_f32 v17, v177, v149\n"
"v_mfma_f32_16x16x32_bf16 v[80:83], a[24:27], a[32:35], v[80:83]\n"
"v_fmac_f32 v18, v177, v150\n"
"v_fmac_f32 v19, v177, v151\n"
"v_mfma_f32_16x16x32_bf16 v[80:83], a[28:31], a[36:39], v[80:83]\n"
"v_lshl_add_u32 v185, v184, 4, v185\n"
"v_cvt_pk_bf16_f32 v16, v16, v17\n"
"v_mfma_f32_16x16x32_bf16 v[84:87], a[28:31], a[44:47], v[84:87]\n"
"v_cvt_pk_bf16_f32 v17, v18, v19\n"
"v_accvgpr_write_b32 a146, v16\n"
"v_mfma_f32_16x16x32_bf16 v[84:87], a[24:27], a[40:43], v[84:87]\n"
"v_accvgpr_write_b32 a147, v17\n"
"v_fmac_f32 v4, v179, v144\n"
"v_mfma_f32_16x16x32_bf16 v[88:91], a[24:27], a[48:51], v[88:91]\n"
"v_fmac_f32 v5, v179, v145\n"
"v_fmac_f32 v6, v179, v146\n"
"v_mfma_f32_16x16x32_bf16 v[88:91], a[28:31], a[52:55], v[88:91]\n"
"v_fmac_f32 v7, v179, v147\n"
"v_cvt_pk_bf16_f32 v4, v4, v5\n"
"v_mfma_f32_16x16x32_bf16 v[92:95], a[28:31], a[60:63], v[92:95]\n"
"s_lshr_b32 s45, s75, 1\n"
"v_cvt_pk_bf16_f32 v5, v6, v7\n"
"v_accvgpr_write_b32 a160, v4\n"
"v_mfma_f32_16x16x32_bf16 v[92:95], a[24:27], a[56:59], v[92:95]\n"
"s_waitcnt lgkmcnt(0)\n"
"v_accvgpr_write_b32 a161, v5\n"
"v_fmac_f32 v20, v179, v148\n"
"v_mfma_f32_16x16x32_bf16 v[96:99], a[0:3], a[96:99], v[96:99]\n"
"s_mul_i32 s45, s45, 20480\n"
"ds_read_b64_tr_b16 a[16:17], v247 offset:16384\n"
"v_fmac_f32 v21, v179, v149\n"
"v_mfma_f32_16x16x32_bf16 v[96:99], a[4:7], a[100:103], v[96:99]\n"
"s_and_b32 s47, s75, 1\n"
"ds_read_b64_tr_b16 a[18:19], v247 offset:16896\n"
"v_fmac_f32 v22, v179, v150\n"
"v_mfma_f32_16x16x32_bf16 v[100:103], a[4:7], a[108:111], v[100:103]\n"
"s_lshl_b32 s47, s47, 7\n"
"ds_read_b64_tr_b16 a[20:21], v247 offset:20480\n"
"v_fmac_f32 v23, v179, v151\n"
"v_mfma_f32_16x16x32_bf16 v[100:103], a[0:3], a[104:107], v[100:103]\n"
"ds_read_b64_tr_b16 a[22:23], v247 offset:20992\n"
"v_cvt_pk_bf16_f32 v20, v20, v21\n"
"v_mfma_f32_16x16x32_bf16 v[104:107], a[0:3], a[112:115], v[104:107]\n"
"s_add_u32 s45, s45, s47\n"
"ds_read_b64_tr_b16 a[24:25], v247 offset:16392\n"
"v_cvt_pk_bf16_f32 v21, v22, v23\n"
"v_mfma_f32_16x16x32_bf16 v[104:107], a[4:7], a[116:119], v[104:107]\n"
"s_add_u32 s45, s45, 65536\n"
"ds_read_b64_tr_b16 a[26:27], v247 offset:16904\n"
"v_accvgpr_write_b32 a162, v20\n"
"v_mfma_f32_16x16x32_bf16 v[108:111], a[4:7], a[124:127], v[108:111]\n"
"s_lshl_b32 s46, s46, 11\n"
"ds_read_b64_tr_b16 a[28:29], v247 offset:20488\n"
"v_accvgpr_write_b32 a163, v21\n"
"v_mfma_f32_16x16x32_bf16 v[108:111], a[0:3], a[120:123], v[108:111]\n"
"ds_read_b64_tr_b16 a[30:31], v247 offset:21000\n"
"v_fmac_f32 v8, v181, v144\n"
"v_mfma_f32_16x16x32_bf16 v[112:115], a[8:11], a[96:99], v[112:115]\n"
"s_add_u32 s46, s46, s45\n"
"v_fmac_f32 v9, v181, v145\n"
"v_fmac_f32 v10, v181, v146\n"
"v_mfma_f32_16x16x32_bf16 v[112:115], a[12:15], a[100:103], v[112:115]\n"
"s_add_u32 s46, s46, 16384\n"
"v_fmac_f32 v11, v181, v147\n"
"v_cvt_pk_bf16_f32 v8, v8, v9\n"
"v_mfma_f32_16x16x32_bf16 v[116:119], a[12:15], a[108:111], v[116:119]\n"
"s_lshl_b32 s47, s68, 11\n"
"v_cvt_pk_bf16_f32 v9, v10, v11\n"
"v_accvgpr_write_b32 a176, v8\n"
"v_mfma_f32_16x16x32_bf16 v[116:119], a[8:11], a[104:107], v[116:119]\n"
"v_accvgpr_write_b32 a177, v9\n"
"v_fmac_f32 v24, v181, v148\n"
"v_mfma_f32_16x16x32_bf16 v[120:123], a[8:11], a[112:115], v[120:123]\n"
"s_add_u32 s45, s45, s47\n"
"v_fmac_f32 v25, v181, v149\n"
"v_fmac_f32 v26, v181, v150\n"
"v_mfma_f32_16x16x32_bf16 v[120:123], a[12:15], a[116:119], v[120:123]\n"
"s_add_u32 s47, s45, 8192\n"
"v_fmac_f32 v27, v181, v151\n"
"v_cvt_pk_bf16_f32 v24, v24, v25\n"
"v_mfma_f32_16x16x32_bf16 v[124:127], a[12:15], a[124:127], v[124:127]\n"
"s_nop 0\n"
"v_cvt_pk_bf16_f32 v25, v26, v27\n"
"v_accvgpr_write_b32 a178, v24\n"
"v_mfma_f32_16x16x32_bf16 v[124:127], a[8:11], a[120:123], v[124:127]\n"
"s_waitcnt lgkmcnt(0)\n"
"v_accvgpr_write_b32 a179, v25\n"
"v_fmac_f32 v12, v183, v144\n"
"v_mfma_f32_16x16x32_bf16 v[96:99], a[16:19], a[32:35], v[96:99]\n"
"buffer_store_dwordx4 a[240:243], v185, s[36:39], s45 offen sc1\n"
"v_fmac_f32 v13, v183, v145\n"
"v_mfma_f32_16x16x32_bf16 v[96:99], a[20:23], a[36:39], v[96:99]\n"
"v_fmac_f32 v14, v183, v146\n"
"v_fmac_f32 v15, v183, v147\n"
"v_mfma_f32_16x16x32_bf16 v[100:103], a[20:23], a[44:47], v[100:103]\n"
"v_cvt_pk_bf16_f32 v12, v12, v13\n"
"v_cvt_pk_bf16_f32 v13, v14, v15\n"
"v_mfma_f32_16x16x32_bf16 v[100:103], a[16:19], a[40:43], v[100:103]\n"
"v_accvgpr_write_b32 a192, v12\n"
"v_accvgpr_write_b32 a193, v13\n"
"v_mfma_f32_16x16x32_bf16 v[104:107], a[16:19], a[48:51], v[104:107]\n"
"v_fmac_f32 v28, v183, v148\n"
"v_fmac_f32 v29, v183, v149\n"
"v_mfma_f32_16x16x32_bf16 v[104:107], a[20:23], a[52:55], v[104:107]\n"
"buffer_store_dwordx4 a[244:247], v185, s[36:39], s47 offen sc1\n"
"v_fmac_f32 v30, v183, v150\n"
"v_mfma_f32_16x16x32_bf16 v[108:111], a[20:23], a[60:63], v[108:111]\n"
"v_fmac_f32 v31, v183, v151\n"
"v_cvt_pk_bf16_f32 v28, v28, v29\n"
"v_mfma_f32_16x16x32_bf16 v[108:111], a[16:19], a[56:59], v[108:111]\n"
"v_cvt_pk_bf16_f32 v29, v30, v31\n"
"v_accvgpr_write_b32 a194, v28\n"
"v_mfma_f32_16x16x32_bf16 v[112:115], a[24:27], a[32:35], v[112:115]\n"
"v_accvgpr_write_b32 a195, v29\n"
"v_mfma_f32_16x16x32_bf16 v[112:115], a[28:31], a[36:39], v[112:115]\n"
"v_mfma_f32_16x16x32_bf16 v[116:119], a[28:31], a[44:47], v[116:119]\n"
"buffer_store_dwordx4 a[248:251], v185, s[36:39], s46 offen sc1\n"
"v_mfma_f32_16x16x32_bf16 v[116:119], a[24:27], a[40:43], v[116:119]\n"
"v_mfma_f32_16x16x32_bf16 v[120:123], a[24:27], a[48:51], v[120:123]\n"
"v_mfma_f32_16x16x32_bf16 v[120:123], a[28:31], a[52:55], v[120:123]\n"
"v_mfma_f32_16x16x32_bf16 v[124:127], a[28:31], a[60:63], v[124:127]\n"
"v_mfma_f32_16x16x32_bf16 v[124:127], a[24:27], a[56:59], v[124:127]\n"
"ds_read_b128 a[208:211], v240 offset:32768\n"
"ds_read_b128 a[212:215], v241 offset:32768\n"
"ds_read_b128 a[216:219], v242 offset:32768\n"
"ds_read_b128 a[220:223], v243 offset:32768\n"
"s_nop 0\n"
"v_pk_mul_f32 v[32:33], v[176:177], v[32:33] op_sel_hi:[0,1]\n"
"v_pk_mul_f32 v[34:35], v[176:177], v[34:35] op_sel_hi:[0,1]\n"
"v_pk_mul_f32 v[48:49], v[176:177], v[48:49] op_sel_hi:[0,1]\n"
"v_pk_mul_f32 v[50:51], v[176:177], v[50:51] op_sel_hi:[0,1]\n"
"v_pk_fma_f32 v[32:33], v[176:177], v[152:153], v[32:33] op_sel:[1,0,0] op_sel_hi:[1,1,1]\n"
"v_pk_fma_f32 v[34:35], v[176:177], v[154:155], v[34:35] op_sel:[1,0,0] op_sel_hi:[1,1,1]\n"
"v_pk_fma_f32 v[48:49], v[176:177], v[156:157], v[48:49] op_sel:[1,0,0] op_sel_hi:[1,1,1]\n"
"v_pk_fma_f32 v[50:51], v[176:177], v[158:159], v[50:51] op_sel:[1,0,0] op_sel_hi:[1,1,1]\n"
"v_pk_mul_f32 v[36:37], v[178:179], v[36:37] op_sel_hi:[0,1]\n"
"v_pk_mul_f32 v[38:39], v[178:179], v[38:39] op_sel_hi:[0,1]\n"
"v_pk_mul_f32 v[52:53], v[178:179], v[52:53] op_sel_hi:[0,1]\n"
"v_pk_mul_f32 v[54:55], v[178:179], v[54:55] op_sel_hi:[0,1]\n"
"v_pk_fma_f32 v[36:37], v[178:179], v[152:153], v[36:37] op_sel:[1,0,0] op_sel_hi:[1,1,1]\n"
"v_pk_fma_f32 v[38:39], v[178:179], v[154:155], v[38:39] op_sel:[1,0,0] op_sel_hi:[1,1,1]\n"
"v_pk_fma_f32 v[52:53], v[178:179], v[156:157], v[52:53] op_sel:[1,0,0] op_sel_hi:[1,1,1]\n"
"v_pk_fma_f32 v[54:55], v[178:179], v[158:159], v[54:55] op_sel:[1,0,0] op_sel_hi:[1,1,1]\n"
"v_pk_mul_f32 v[40:41], v[180:181], v[40:41] op_sel_hi:[0,1]\n"
"v_pk_mul_f32 v[42:43], v[180:181], v[42:43] op_sel_hi:[0,1]\n"
"v_pk_mul_f32 v[56:57], v[180:181], v[56:57] op_sel_hi:[0,1]\n"
"v_pk_mul_f32 v[58:59], v[180:181], v[58:59] op_sel_hi:[0,1]\n"
"v_pk_fma_f32 v[40:41], v[180:181], v[152:153], v[40:41] op_sel:[1,0,0] op_sel_hi:[1,1,1]\n"
"v_pk_fma_f32 v[42:43], v[180:181], v[154:155], v[42:43] op_sel:[1,0,0] op_sel_hi:[1,1,1]\n"
"v_pk_fma_f32 v[56:57], v[180:181], v[156:157], v[56:57] op_sel:[1,0,0] op_sel_hi:[1,1,1]\n"
"v_pk_fma_f32 v[58:59], v[180:181], v[158:159], v[58:59] op_sel:[1,0,0] op_sel_hi:[1,1,1]\n"
"v_pk_mul_f32 v[44:45], v[182:183], v[44:45] op_sel_hi:[0,1]\n"
"v_pk_mul_f32 v[46:47], v[182:183], v[46:47] op_sel_hi:[0,1]\n"
"v_pk_mul_f32 v[60:61], v[182:183], v[60:61] op_sel_hi:[0,1]\n"
"v_pk_mul_f32 v[62:63], v[182:183], v[62:63] op_sel_hi:[0,1]\n"
"v_pk_fma_f32 v[44:45], v[182:183], v[152:153], v[44:45] op_sel:[1,0,0] op_sel_hi:[1,1,1]\n"
"v_pk_fma_f32 v[46:47], v[182:183], v[154:155], v[46:47] op_sel:[1,0,0] op_sel_hi:[1,1,1]\n"
"v_pk_fma_f32 v[60:61], v[182:183], v[156:157], v[60:61] op_sel:[1,0,0] op_sel_hi:[1,1,1]\n"
"v_pk_fma_f32 v[62:63], v[182:183], v[158:159], v[62:63] op_sel:[1,0,0] op_sel_hi:[1,1,1]\n"
"v_cvt_pk_bf16_f32 v32, v32, v33\n"
"v_cvt_pk_bf16_f32 v33, v34, v35\n"
"v_cvt_pk_bf16_f32 v48, v48, v49\n"
"v_cvt_pk_bf16_f32 v49, v50, v51\n"
"v_cvt_pk_bf16_f32 v36, v36, v37\n"
"v_cvt_pk_bf16_f32 v37, v38, v39\n"
"v_cvt_pk_bf16_f32 v52, v52, v53\n"
"v_cvt_pk_bf16_f32 v53, v54, v55\n"
"v_cvt_pk_bf16_f32 v40, v40, v41\n"
"v_cvt_pk_bf16_f32 v41, v42, v43\n"
"v_cvt_pk_bf16_f32 v56, v56, v57\n"
"v_cvt_pk_bf16_f32 v57, v58, v59\n"
"v_cvt_pk_bf16_f32 v44, v44, v45\n"
"v_cvt_pk_bf16_f32 v45, v46, v47\n"
"v_cvt_pk_bf16_f32 v60, v60, v61\n"
"v_cvt_pk_bf16_f32 v61, v62, v63\n"
"v_accvgpr_write_b32 a148, v32\n"
"v_accvgpr_write_b32 a149, v33\n"
"v_accvgpr_write_b32 a150, v48\n"
"v_accvgpr_write_b32 a151, v49\n"
"v_accvgpr_write_b32 a164, v36\n"
"v_accvgpr_write_b32 a165, v37\n"
"v_accvgpr_write_b32 a166, v52\n"
"v_accvgpr_write_b32 a167, v53\n"
"v_accvgpr_write_b32 a180, v40\n"
"v_accvgpr_write_b32 a181, v41\n"
"v_accvgpr_write_b32 a182, v56\n"
"v_accvgpr_write_b32 a183, v57\n"
"v_accvgpr_write_b32 a196, v44\n"
"v_accvgpr_write_b32 a197, v45\n"
"v_accvgpr_write_b32 a198, v60\n"
"v_accvgpr_write_b32 a199, v61\n"
"v_pk_mul_f32 v[64:65], v[176:177], v[64:65] op_sel_hi:[0,1]\n"
"v_pk_mul_f32 v[66:67], v[176:177], v[66:67] op_sel_hi:[0,1]\n"
"v_pk_mul_f32 v[80:81], v[176:177], v[80:81] op_sel_hi:[0,1]\n"
"v_pk_mul_f32 v[82:83], v[176:177], v[82:83] op_sel_hi:[0,1]\n"
"v_pk_fma_f32 v[64:65], v[176:177], v[160:161], v[64:65] op_sel:[1,0,0] op_sel_hi:[1,1,1]\n"
"v_pk_fma_f32 v[66:67], v[176:177], v[162:163], v[66:67] op_sel:[1,0,0] op_sel_hi:[1,1,1]\n"
"v_pk_fma_f32 v[80:81], v[176:177], v[164:165], v[80:81] op_sel:[1,0,0] op_sel_hi:[1,1,1]\n"
"v_pk_fma_f32 v[82:83], v[176:177], v[166:167], v[82:83] op_sel:[1,0,0] op_sel_hi:[1,1,1]\n"
"v_pk_mul_f32 v[68:69], v[178:179], v[68:69] op_sel_hi:[0,1]\n"
"v_pk_mul_f32 v[70:71], v[178:179], v[70:71] op_sel_hi:[0,1]\n"
"v_pk_mul_f32 v[84:85], v[178:179], v[84:85] op_sel_hi:[0,1]\n"
"v_pk_mul_f32 v[86:87], v[178:179], v[86:87] op_sel_hi:[0,1]\n"
"v_pk_fma_f32 v[68:69], v[178:179], v[160:161], v[68:69] op_sel:[1,0,0] op_sel_hi:[1,1,1]\n"
"v_pk_fma_f32 v[70:71], v[178:179], v[162:163], v[70:71] op_sel:[1,0,0] op_sel_hi:[1,1,1]\n"
"v_pk_fma_f32 v[84:85], v[178:179], v[164:165], v[84:85] op_sel:[1,0,0] op_sel_hi:[1,1,1]\n"
"v_pk_fma_f32 v[86:87], v[178:179], v[166:167], v[86:87] op_sel:[1,0,0] op_sel_hi:[1,1,1]\n"
"v_pk_mul_f32 v[72:73], v[180:181], v[72:73] op_sel_hi:[0,1]\n"
"v_pk_mul_f32 v[74:75], v[180:181], v[74:75] op_sel_hi:[0,1]\n"
"v_pk_mul_f32 v[88:89], v[180:181], v[88:89] op_sel_hi:[0,1]\n"
"v_pk_mul_f32 v[90:91], v[180:181], v[90:91] op_sel_hi:[0,1]\n"
"v_pk_fma_f32 v[72:73], v[180:181], v[160:161], v[72:73] op_sel:[1,0,0] op_sel_hi:[1,1,1]\n"
"v_pk_fma_f32 v[74:75], v[180:181], v[162:163], v[74:75] op_sel:[1,0,0] op_sel_hi:[1,1,1]\n"
"v_pk_fma_f32 v[88:89], v[180:181], v[164:165], v[88:89] op_sel:[1,0,0] op_sel_hi:[1,1,1]\n"
"v_pk_fma_f32 v[90:91], v[180:181], v[166:167], v[90:91] op_sel:[1,0,0] op_sel_hi:[1,1,1]\n"
"v_pk_mul_f32 v[76:77], v[182:183], v[76:77] op_sel_hi:[0,1]\n"
"v_pk_mul_f32 v[78:79], v[182:183], v[78:79] op_sel_hi:[0,1]\n"
"v_pk_mul_f32 v[92:93], v[182:183], v[92:93] op_sel_hi:[0,1]\n"
"v_pk_mul_f32 v[94:95], v[182:183], v[94:95] op_sel_hi:[0,1]\n"
"v_pk_fma_f32 v[76:77], v[182:183], v[160:161], v[76:77] op_sel:[1,0,0] op_sel_hi:[1,1,1]\n"
"v_pk_fma_f32 v[78:79], v[182:183], v[162:163], v[78:79] op_sel:[1,0,0] op_sel_hi:[1,1,1]\n"
"v_pk_fma_f32 v[92:93], v[182:183], v[164:165], v[92:93] op_sel:[1,0,0] op_sel_hi:[1,1,1]\n"
"v_pk_fma_f32 v[94:95], v[182:183], v[166:167], v[94:95] op_sel:[1,0,0] op_sel_hi:[1,1,1]\n"
"v_cvt_pk_bf16_f32 v64, v64, v65\n"
"v_cvt_pk_bf16_f32 v65, v66, v67\n"
"v_cvt_pk_bf16_f32 v80, v80, v81\n"
"v_cvt_pk_bf16_f32 v81, v82, v83\n"
"v_cvt_pk_bf16_f32 v68, v68, v69\n"
"v_cvt_pk_bf16_f32 v69, v70, v71\n"
"v_cvt_pk_bf16_f32 v84, v84, v85\n"
"v_cvt_pk_bf16_f32 v85, v86, v87\n"
"v_cvt_pk_bf16_f32 v72, v72, v73\n"
"v_cvt_pk_bf16_f32 v73, v74, v75\n"
"v_cvt_pk_bf16_f32 v88, v88, v89\n"
"v_cvt_pk_bf16_f32 v89, v90, v91\n"
"v_cvt_pk_bf16_f32 v76, v76, v77\n"
"v_cvt_pk_bf16_f32 v77, v78, v79\n"
"v_cvt_pk_bf16_f32 v92, v92, v93\n"
"v_cvt_pk_bf16_f32 v93, v94, v95\n"
"v_accvgpr_write_b32 a152, v64\n"
"v_accvgpr_write_b32 a153, v65\n"
"v_accvgpr_write_b32 a154, v80\n"
"v_accvgpr_write_b32 a155, v81\n"
"v_accvgpr_write_b32 a168, v68\n"
"v_accvgpr_write_b32 a169, v69\n"
"v_accvgpr_write_b32 a170, v84\n"
"v_accvgpr_write_b32 a171, v85\n"
"v_accvgpr_write_b32 a184, v72\n"
"v_accvgpr_write_b32 a185, v73\n"
"v_accvgpr_write_b32 a186, v88\n"
"v_accvgpr_write_b32 a187, v89\n"
"v_accvgpr_write_b32 a200, v76\n"
"v_accvgpr_write_b32 a201, v77\n"
"v_accvgpr_write_b32 a202, v92\n"
"v_accvgpr_write_b32 a203, v93\n"
"v_pk_mul_f32 v[96:97], v[176:177], v[96:97] op_sel_hi:[0,1]\n"
"v_pk_mul_f32 v[98:99], v[176:177], v[98:99] op_sel_hi:[0,1]\n"
"v_pk_mul_f32 v[112:113], v[176:177], v[112:113] op_sel_hi:[0,1]\n"
"v_pk_mul_f32 v[114:115], v[176:177], v[114:115] op_sel_hi:[0,1]\n"
"v_pk_fma_f32 v[96:97], v[176:177], v[168:169], v[96:97] op_sel:[1,0,0] op_sel_hi:[1,1,1]\n"
"v_pk_fma_f32 v[98:99], v[176:177], v[170:171], v[98:99] op_sel:[1,0,0] op_sel_hi:[1,1,1]\n"
"v_pk_fma_f32 v[112:113], v[176:177], v[172:173], v[112:113] op_sel:[1,0,0] op_sel_hi:[1,1,1]\n"
"v_pk_fma_f32 v[114:115], v[176:177], v[174:175], v[114:115] op_sel:[1,0,0] op_sel_hi:[1,1,1]\n"
"v_pk_mul_f32 v[100:101], v[178:179], v[100:101] op_sel_hi:[0,1]\n"
"v_pk_mul_f32 v[102:103], v[178:179], v[102:103] op_sel_hi:[0,1]\n"
"v_pk_mul_f32 v[116:117], v[178:179], v[116:117] op_sel_hi:[0,1]\n"
"v_pk_mul_f32 v[118:119], v[178:179], v[118:119] op_sel_hi:[0,1]\n"
"v_pk_fma_f32 v[100:101], v[178:179], v[168:169], v[100:101] op_sel:[1,0,0] op_sel_hi:[1,1,1]\n"
"v_pk_fma_f32 v[102:103], v[178:179], v[170:171], v[102:103] op_sel:[1,0,0] op_sel_hi:[1,1,1]\n"
"v_pk_fma_f32 v[116:117], v[178:179], v[172:173], v[116:117] op_sel:[1,0,0] op_sel_hi:[1,1,1]\n"
"v_pk_fma_f32 v[118:119], v[178:179], v[174:175], v[118:119] op_sel:[1,0,0] op_sel_hi:[1,1,1]\n"
"v_pk_mul_f32 v[104:105], v[180:181], v[104:105] op_sel_hi:[0,1]\n"
"v_pk_mul_f32 v[106:107], v[180:181], v[106:107] op_sel_hi:[0,1]\n"
"v_pk_mul_f32 v[120:121], v[180:181], v[120:121] op_sel_hi:[0,1]\n"
"v_pk_mul_f32 v[122:123], v[180:181], v[122:123] op_sel_hi:[0,1]\n"
"v_pk_fma_f32 v[104:105], v[180:181], v[168:169], v[104:105] op_sel:[1,0,0] op_sel_hi:[1,1,1]\n"
"v_pk_fma_f32 v[106:107], v[180:181], v[170:171], v[106:107] op_sel:[1,0,0] op_sel_hi:[1,1,1]\n"
"v_pk_fma_f32 v[120:121], v[180:181], v[172:173], v[120:121] op_sel:[1,0,0] op_sel_hi:[1,1,1]\n"
"v_pk_fma_f32 v[122:123], v[180:181], v[174:175], v[122:123] op_sel:[1,0,0] op_sel_hi:[1,1,1]\n"
"v_pk_mul_f32 v[108:109], v[182:183], v[108:109] op_sel_hi:[0,1]\n"
"v_pk_mul_f32 v[110:111], v[182:183], v[110:111] op_sel_hi:[0,1]\n"
"v_pk_mul_f32 v[124:125], v[182:183], v[124:125] op_sel_hi:[0,1]\n"
"v_pk_mul_f32 v[126:127], v[182:183], v[126:127] op_sel_hi:[0,1]\n"
"v_pk_fma_f32 v[108:109], v[182:183], v[168:169], v[108:109] op_sel:[1,0,0] op_sel_hi:[1,1,1]\n"
"v_pk_fma_f32 v[110:111], v[182:183], v[170:171], v[110:111] op_sel:[1,0,0] op_sel_hi:[1,1,1]\n"
"v_pk_fma_f32 v[124:125], v[182:183], v[172:173], v[124:125] op_sel:[1,0,0] op_sel_hi:[1,1,1]\n"
"v_pk_fma_f32 v[126:127], v[182:183], v[174:175], v[126:127] op_sel:[1,0,0] op_sel_hi:[1,1,1]\n"
"v_cvt_pk_bf16_f32 v96, v96, v97\n"
"v_cvt_pk_bf16_f32 v97, v98, v99\n"
"v_cvt_pk_bf16_f32 v112, v112, v113\n"
"v_cvt_pk_bf16_f32 v113, v114, v115\n"
"v_cvt_pk_bf16_f32 v100, v100, v101\n"
"v_cvt_pk_bf16_f32 v101, v102, v103\n"
"v_cvt_pk_bf16_f32 v116, v116, v117\n"
"v_cvt_pk_bf16_f32 v117, v118, v119\n"
"v_cvt_pk_bf16_f32 v104, v104, v105\n"
"v_cvt_pk_bf16_f32 v105, v106, v107\n"
"v_cvt_pk_bf16_f32 v120, v120, v121\n"
"v_cvt_pk_bf16_f32 v121, v122, v123\n"
"v_cvt_pk_bf16_f32 v108, v108, v109\n"
"v_cvt_pk_bf16_f32 v109, v110, v111\n"
"v_cvt_pk_bf16_f32 v124, v124, v125\n"
"v_cvt_pk_bf16_f32 v125, v126, v127\n"
"v_accvgpr_write_b32 a156, v96\n"
"v_accvgpr_write_b32 a157, v97\n"
"v_accvgpr_write_b32 a158, v112\n"
"v_accvgpr_write_b32 a159, v113\n"
"v_accvgpr_write_b32 a172, v100\n"
"v_accvgpr_write_b32 a173, v101\n"
"v_accvgpr_write_b32 a174, v116\n"
"v_accvgpr_write_b32 a175, v117\n"
"v_accvgpr_write_b32 a188, v104\n"
"v_accvgpr_write_b32 a189, v105\n"
"v_accvgpr_write_b32 a190, v120\n"
"v_accvgpr_write_b32 a191, v121\n"
"v_accvgpr_write_b32 a204, v108\n"
"v_accvgpr_write_b32 a205, v109\n"
"v_accvgpr_write_b32 a206, v124\n"
"v_accvgpr_write_b32 a207, v125\n"
"s_cmp_lg_u32 s89, 0\n"
"s_cbranch_scc1 .L_maxpass\n"
".L_attn:\n"
"s_waitcnt lgkmcnt(0)\n"
"v_mfma_f32_16x16x32_bf16 v[0:3], a[208:211], a[144:147], v[192:195]\n"
".L_pad4:\n"
"s_cbranch_execz .L_pad8\n"
"ds_read_b128 a[224:227], v240 offset:36864\n"
"v_lshrrev_b32 v212, 3, v229\n"
"v_mfma_f32_16x16x32_bf16 v[0:3], a[212:215], a[148:151], v[0:3]\n"
"v_xor_b32 v213, v212, v229\n"
"v_and_b32 v213, 6, v213\n"
"v_mfma_f32_16x16x32_bf16 v[4:7], a[212:215], a[164:167], v[196:199]\n"
"ds_read_b128 a[228:231], v241 offset:36864\n"
"v_and_or_b32 v213, v229, 1, v213\n"
"v_mfma_f32_16x16x32_bf16 v[4:7], a[208:211], a[160:163], v[4:7]\n"
"v_mul_u32_u24 v212, 1280, v212\n"
"v_lshl_add_u32 v212, v213, 4, v212\n"
"v_mfma_f32_16x16x32_bf16 v[8:11], a[208:211], a[176:179], v[200:203]\n"
"ds_read_b128 a[232:235], v242 offset:36864\n"
"v_lshrrev_b32 v214, 3, v229\n"
"v_mfma_f32_16x16x32_bf16 v[8:11], a[212:215], a[180:183], v[8:11]\n"
"v_and_b32 v215, 7, v229\n"
"v_and_b32 v216, 2, v214\n"
"v_mfma_f32_16x16x32_bf16 v[12:15], a[212:215], a[196:199], v[204:207]\n"
"ds_read_b128 a[236:239], v243 offset:36864\n"
"v_xor_b32 v215, v215, v216\n"
"v_mfma_f32_16x16x32_bf16 v[12:15], a[208:211], a[192:195], v[12:15]\n"
"v_mul_u32_u24 v214, 3456, v214\n"
"v_lshl_add_u32 v252, v215, 4, v214\n"
"v_mfma_f32_16x16x32_bf16 v[0:3], a[216:219], a[152:155], v[0:3]\n"
"v_xor_b32 v253, 64, v252\n"
"v_add_u32 v253, 27648, v253\n"
"v_mfma_f32_16x16x32_bf16 v[0:3], a[220:223], a[156:159], v[0:3]\n"
"v_subrev_u32 v244, 131072, v232\n"
"v_add_u32 v248, 65536, v244\n"
"v_mfma_f32_16x16x32_bf16 v[4:7], a[220:223], a[172:175], v[4:7]\n"
"v_subrev_u32 v245, 131072, v233\n"
"v_add_u32 v249, 65536, v245\n"
"v_mfma_f32_16x16x32_bf16 v[4:7], a[216:219], a[168:171], v[4:7]\n"
"v_subrev_u32 v246, 131072, v234\n"
"v_add_u32 v250, 65536, v246\n"
"v_mfma_f32_16x16x32_bf16 v[8:11], a[216:219], a[184:187], v[8:11]\n"
"v_subrev_u32 v247, 131072, v235\n"
"v_add_u32 v251, 65536, v247\n"
"v_mfma_f32_16x16x32_bf16 v[8:11], a[220:223], a[188:191], v[8:11]\n"
"v_lshrrev_b32 v214, 2, v229\n"
"v_lshlrev_b32 v214, 7, v214\n"
"v_mfma_f32_16x16x32_bf16 v[12:15], a[220:223], a[204:207], v[12:15]\n"
"v_and_b32 v215, 3, v229\n"
"v_lshl_add_u32 v214, v215, 3, v214\n"
"v_mfma_f32_16x16x32_bf16 v[12:15], a[216:219], a[200:203], v[12:15]\n"
"s_waitcnt lgkmcnt(2)\n"
"v_bfe_u32 v215, v229, 3, 2\n"
"v_lshl_add_u32 v214, v215, 5, v214\n"
"v_mfma_f32_16x16x32_bf16 v[16:19], a[224:227], a[144:147], v[192:195]\n"
"s_sub_u32 s91, s84, 32768\n"
"ds_read_b128 a[208:211], v240 offset:40960\n"
"v_add_u32 v217, s91, v214\n"
"v_mfma_f32_16x16x32_bf16 v[16:19], a[228:231], a[148:151], v[16:19]\n"
"v_min_u32 v218, 7, v229\n"
"v_mul_u32_u24 v218, 3456, v218\n"
"v_mfma_f32_16x16x32_bf16 v[20:23], a[228:231], a[164:167], v[196:199]\n"
"ds_read_b128 a[212:215], v241 offset:40960\n"
"v_mfma_f32_16x16x32_bf16 v[20:23], a[224:227], a[160:163], v[20:23]\n"
"v_mfma_f32_16x16x32_bf16 v[24:27], a[224:227], a[176:179], v[200:203]\n"
"s_mul_i32 s80, s68, 55296\n"
"s_lshr_b32 s96, s87, 4\n"
"ds_read_b128 a[216:219], v242 offset:40960\n"
"v_mfma_f32_16x16x32_bf16 v[24:27], a[228:231], a[180:183], v[24:27]\n"
"s_mul_i32 s79, s96, 20480\n"
"v_mfma_f32_16x16x32_bf16 v[28:31], a[228:231], a[196:199], v[204:207]\n"
"s_add_u32 s79, s79, 65536\n"
"s_add_u32 s96, s69, s72\n"
"ds_read_b128 a[220:223], v243 offset:40960\n"
"v_mfma_f32_16x16x32_bf16 v[28:31], a[224:227], a[192:195], v[28:31]\n"
"s_waitcnt lgkmcnt(4)\n"
"v_mfma_f32_16x16x32_bf16 v[16:19], a[232:235], a[152:155], v[16:19]\n"
"s_and_b32 s96, s96, 3\n"
"s_mul_i32 s96, s96, 5\n"
"s_add_u32 s96, s96, 1\n"
"v_mfma_f32_16x16x32_bf16 v[16:19], a[236:239], a[156:159], v[16:19]\n"
"s_lshr_b32 s96, s96, 1\n"
"v_mfma_f32_16x16x32_bf16 v[20:23], a[236:239], a[172:175], v[20:23]\n"
"s_lshl_b32 s58, s96, 11\n"
"s_mul_i32 s96, s96, 221184\n"
"s_add_u32 s80, s80, s96\n"
"v_mfma_f32_16x16x32_bf16 v[20:23], a[232:235], a[168:171], v[20:23]\n"
"s_and_b32 s93, s69, 3\n"
"v_mfma_f32_16x16x32_bf16 v[24:27], a[232:235], a[184:187], v[24:27]\n"
"s_lshl2_add_u32 s93, s93, s68\n"
"s_add_u32 s93, s93, 64\n"
"s_mul_i32 s93, s93, 27648\n"
"v_mfma_f32_16x16x32_bf16 v[24:27], a[236:239], a[188:191], v[24:27]\n"
"s_lshr_b32 s45, s72, 2\n"
"v_mfma_f32_16x16x32_bf16 v[28:31], a[236:239], a[204:207], v[28:31]\n"
"s_and_b32 s46, s72, 3\n"
"s_lshl1_add_u32 s47, s69, s45\n"
"s_lshl2_add_u32 s47, s47, s46\n"
"v_mfma_f32_16x16x32_bf16 v[28:31], a[232:235], a[200:203], v[28:31]\n"
"s_waitcnt lgkmcnt(2)\n"
"v_mfma_f32_16x16x32_bf16 v[32:35], a[208:211], a[144:147], v[192:195]\n"
"ds_read_b128 a[224:227], v240 offset:45056\n"
"v_mfma_f32_16x16x32_bf16 v[32:35], a[212:215], a[148:151], v[32:35]\n"
"v_mfma_f32_16x16x32_bf16 v[36:39], a[212:215], a[164:167], v[196:199]\n"
"ds_read_b128 a[228:231], v241 offset:45056\n"
"v_mfma_f32_16x16x32_bf16 v[36:39], a[208:211], a[160:163], v[36:39]\n"
"v_mfma_f32_16x16x32_bf16 v[40:43], a[208:211], a[176:179], v[200:203]\n"
"ds_read_b128 a[232:235], v242 offset:45056\n"
"v_mfma_f32_16x16x32_bf16 v[40:43], a[212:215], a[180:183], v[40:43]\n"
"v_mfma_f32_16x16x32_bf16 v[44:47], a[212:215], a[196:199], v[204:207]\n"
"ds_read_b128 a[236:239], v243 offset:45056\n"
"v_mfma_f32_16x16x32_bf16 v[44:47], a[208:211], a[192:195], v[44:47]\n"
"s_waitcnt lgkmcnt(4)\n"
"v_exp_f32 v0, v0\n"
"v_mfma_f32_16x16x32_bf16 v[32:35], a[216:219], a[152:155], v[32:35]\n"
"v_exp_f32 v1, v1\n"
"v_mfma_f32_16x16x32_bf16 v[32:35], a[220:223], a[156:159], v[32:35]\n"
"v_exp_f32 v2, v2\n"
"v_mfma_f32_16x16x32_bf16 v[36:39], a[220:223], a[172:175], v[36:39]\n"
"v_exp_f32 v3, v3\n"
"v_mfma_f32_16x16x32_bf16 v[36:39], a[216:219], a[168:171], v[36:39]\n"
"v_exp_f32 v16, v16\n"
"v_mfma_f32_16x16x32_bf16 v[40:43], a[216:219], a[184:187], v[40:43]\n"
"v_exp_f32 v17, v17\n"
"v_mfma_f32_16x16x32_bf16 v[40:43], a[220:223], a[188:191], v[40:43]\n"
"v_exp_f32 v18, v18\n"
"v_mfma_f32_16x16x32_bf16 v[44:47], a[220:223], a[204:207], v[44:47]\n"
"v_exp_f32 v19, v19\n"
"v_mfma_f32_16x16x32_bf16 v[44:47], a[216:219], a[200:203], v[44:47]\n"
"s_waitcnt lgkmcnt(0)\n"
"v_exp_f32 v4, v4\n"
"v_mfma_f32_16x16x32_bf16 v[48:51], a[224:227], a[144:147], v[192:195]\n"
"s_waitcnt vmcnt(15)\n"
"s_barrier\n"
"v_cvt_pk_bf16_f32 v128, v0, v1\n"
"v_mfma_f32_16x16x32_bf16 v[48:51], a[228:231], a[148:151], v[48:51]\n"
"s_add_u32 m0, s84, 32768\n"
"v_exp_f32 v5, v5\n"
"v_mfma_f32_16x16x32_bf16 v[52:55], a[228:231], a[164:167], v[196:199]\n"
"ds_read_b128 a[208:211], v240 offset:16384\n"
"buffer_load_dwordx4 v236, s[24:27], s82 offen offset:0 lds\n"
"v_mfma_f32_16x16x32_bf16 v[52:55], a[224:227], a[160:163], v[52:55]\n"
"v_exp_f32 v6, v6\n"
"v_mfma_f32_16x16x32_bf16 v[56:59], a[224:227], a[176:179], v[200:203]\n"
"ds_read_b128 a[212:215], v241 offset:16384\n"
"v_cvt_pk_bf16_f32 v129, v2, v3\n"
"v_mfma_f32_16x16x32_bf16 v[56:59], a[228:231], a[180:183], v[56:59]\n"
"v_exp_f32 v7, v7\n"
"v_mfma_f32_16x16x32_bf16 v[60:63], a[228:231], a[196:199], v[204:207]\n"
"ds_read_b128 a[216:219], v242 offset:16384\n"
"v_cvt_pk_bf16_f32 v130, v16, v17\n"
"v_mfma_f32_16x16x32_bf16 v[60:63], a[224:227], a[192:195], v[60:63]\n"
"v_exp_f32 v20, v20\n"
"v_mfma_f32_16x16x32_bf16 v[48:51], a[232:235], a[152:155], v[48:51]\n"
"ds_read_b128 a[220:223], v243 offset:16384\n"
"v_cvt_pk_bf16_f32 v131, v18, v19\n"
"v_mfma_f32_16x16x32_bf16 v[48:51], a[236:239], a[156:159], v[48:51]\n"
"v_exp_f32 v21, v21\n"
"v_mfma_f32_16x16x32_bf16 v[52:55], a[236:239], a[172:175], v[52:55]\n"
"buffer_load_dwordx4 v237, s[24:27], s82 offen offset:1024 lds\n"
"v_cvt_pk_bf16_f32 v132, v4, v5\n"
"v_mfma_f32_16x16x32_bf16 v[52:55], a[232:235], a[168:171], v[52:55]\n"
"v_exp_f32 v22, v22\n"
"v_mfma_f32_16x16x32_bf16 v[56:59], a[232:235], a[184:187], v[56:59]\n"
"v_exp_f32 v23, v23\n"
"v_mfma_f32_16x16x32_bf16 v[56:59], a[236:239], a[188:191], v[56:59]\n"
"v_exp_f32 v8, v8\n"
"v_mfma_f32_16x16x32_bf16 v[60:63], a[236:239], a[204:207], v[60:63]\n"
"v_exp_f32 v9, v9\n"
"v_mfma_f32_16x16x32_bf16 v[60:63], a[232:235], a[200:203], v[60:63]\n"
"s_waitcnt lgkmcnt(2)\n"
"v_exp_f32 v10, v10\n"
"v_mfma_f32_16x16x32_bf16 v[64:67], a[208:211], a[144:147], v[192:195]\n"
"ds_read_b128 a[224:227], v240 offset:20480\n"
"v_cvt_pk_bf16_f32 v133, v6, v7\n"
"v_mfma_f32_16x16x32_bf16 v[64:67], a[212:215], a[148:151], v[64:67]\n"
"v_exp_f32 v11, v11\n"
"v_mfma_f32_16x16x32_bf16 v[68:71], a[212:215], a[164:167], v[196:199]\n"
"ds_read_b128 a[228:231], v241 offset:20480\n"
"buffer_load_dwordx4 v238, s[24:27], s82 offen offset:2048 lds\n"
"v_mfma_f32_16x16x32_bf16 v[68:71], a[208:211], a[160:163], v[68:71]\n"
"v_exp_f32 v24, v24\n"
"v_mfma_f32_16x16x32_bf16 v[72:75], a[208:211], a[176:179], v[200:203]\n"
"ds_read_b128 a[232:235], v242 offset:20480\n"
"v_cvt_pk_bf16_f32 v134, v20, v21\n"
"v_mfma_f32_16x16x32_bf16 v[72:75], a[212:215], a[180:183], v[72:75]\n"
"v_exp_f32 v25, v25\n"
"v_mfma_f32_16x16x32_bf16 v[76:79], a[212:215], a[196:199], v[204:207]\n"
"ds_read_b128 a[236:239], v243 offset:20480\n"
"v_cvt_pk_bf16_f32 v135, v22, v23\n"
"v_mfma_f32_16x16x32_bf16 v[76:79], a[208:211], a[192:195], v[76:79]\n"
"s_waitcnt lgkmcnt(4)\n"
"v_exp_f32 v26, v26\n"
"v_mfma_f32_16x16x32_bf16 v[64:67], a[216:219], a[152:155], v[64:67]\n"
"v_exp_f32 v27, v27\n"
"v_mfma_f32_16x16x32_bf16 v[64:67], a[220:223], a[156:159], v[64:67]\n"
"v_exp_f32 v12, v12\n"
"v_mfma_f32_16x16x32_bf16 v[68:71], a[220:223], a[172:175], v[68:71]\n"
"buffer_load_dwordx4 v239, s[24:27], s82 offen offset:3072 lds\n"
"v_cvt_pk_bf16_f32 v136, v8, v9\n"
"v_mfma_f32_16x16x32_bf16 v[68:71], a[216:219], a[168:171], v[68:71]\n"
"s_add_u32 s82, s82, 16384\n"
"v_exp_f32 v13, v13\n"
"v_mfma_f32_16x16x32_bf16 v[72:75], a[216:219], a[184:187], v[72:75]\n"
"s_and_b32 s82, s82, 0x1ffff\n"
"s_sub_u32 m0, s85, 98304\n"
"v_exp_f32 v14, v14\n"
"v_mfma_f32_16x16x32_bf16 v[72:75], a[220:223], a[188:191], v[72:75]\n"
"v_exp_f32 v15, v15\n"
"v_mfma_f32_16x16x32_bf16 v[76:79], a[220:223], a[204:207], v[76:79]\n"
"v_exp_f32 v28, v28\n"
"v_mfma_f32_16x16x32_bf16 v[76:79], a[216:219], a[200:203], v[76:79]\n"
"s_waitcnt lgkmcnt(2)\n"
"v_exp_f32 v29, v29\n"
"v_mfma_f32_16x16x32_bf16 v[80:83], a[224:227], a[144:147], v[192:195]\n"
"ds_read_b128 a[208:211], v240 offset:24576\n"
"v_exp_f32 v30, v30\n"
"v_mfma_f32_16x16x32_bf16 v[80:83], a[228:231], a[148:151], v[80:83]\n"
"v_exp_f32 v31, v31\n"
"v_mfma_f32_16x16x32_bf16 v[84:87], a[228:231], a[164:167], v[196:199]\n"
"ds_read_b128 a[212:215], v241 offset:24576\n"
"buffer_load_dwordx4 v230, s[28:31], s83 offen offset:0 lds\n"
"v_mfma_f32_16x16x32_bf16 v[84:87], a[224:227], a[160:163], v[84:87]\n"
"v_exp_f32 v32, v32\n"
"v_mfma_f32_16x16x32_bf16 v[88:91], a[224:227], a[176:179], v[200:203]\n"
"ds_read_b128 a[216:219], v242 offset:24576\n"
"v_cvt_pk_bf16_f32 v137, v10, v11\n"
"v_mfma_f32_16x16x32_bf16 v[88:91], a[228:231], a[180:183], v[88:91]\n"
"v_exp_f32 v33, v33\n"
"v_mfma_f32_16x16x32_bf16 v[92:95], a[228:231], a[196:199], v[204:207]\n"
"ds_read_b128 a[220:223], v243 offset:24576\n"
"v_cvt_pk_bf16_f32 v138, v24, v25\n"
"v_mfma_f32_16x16x32_bf16 v[92:95], a[224:227], a[192:195], v[92:95]\n"
"s_waitcnt lgkmcnt(4)\n"
"v_exp_f32 v34, v34\n"
"v_mfma_f32_16x16x32_bf16 v[80:83], a[232:235], a[152:155], v[80:83]\n"
"v_exp_f32 v35, v35\n"
"v_mfma_f32_16x16x32_bf16 v[80:83], a[236:239], a[156:159], v[80:83]\n"
"v_exp_f32 v48, v48\n"
"v_mfma_f32_16x16x32_bf16 v[84:87], a[236:239], a[172:175], v[84:87]\n"
"buffer_load_dwordx4 v231, s[28:31], s83 offen offset:1024 lds\n"
"v_cvt_pk_bf16_f32 v139, v26, v27\n"
"v_mfma_f32_16x16x32_bf16 v[84:87], a[232:235], a[168:171], v[84:87]\n"
"v_exp_f32 v49, v49\n"
"v_mfma_f32_16x16x32_bf16 v[88:91], a[232:235], a[184:187], v[88:91]\n"
"v_exp_f32 v50, v50\n"
"v_mfma_f32_16x16x32_bf16 v[88:91], a[236:239], a[188:191], v[88:91]\n"
"v_exp_f32 v51, v51\n"
"v_mfma_f32_16x16x32_bf16 v[92:95], a[236:239], a[204:207], v[92:95]\n"
"v_exp_f32 v36, v36\n"
"v_cvt_pk_bf16_f32 v147, v50, v51\n"
"v_mfma_f32_16x16x32_bf16 v[92:95], a[232:235], a[200:203], v[92:95]\n"
"s_waitcnt lgkmcnt(2)\n"
"v_exp_f32 v37, v37\n"
"v_mfma_f32_16x16x32_bf16 v[96:99], a[208:211], a[144:147], v[192:195]\n"
"ds_read_b128 a[224:227], v240 offset:28672\n"
"v_cvt_pk_bf16_f32 v140, v12, v13\n"
"v_cvt_pk_bf16_f32 v148, v36, v37\n"
"v_mfma_f32_16x16x32_bf16 v[96:99], a[212:215], a[148:151], v[96:99]\n"
"v_exp_f32 v38, v38\n"
"v_mfma_f32_16x16x32_bf16 v[100:103], a[212:215], a[164:167], v[196:199]\n"
"ds_read_b128 a[228:231], v241 offset:28672\n"
"buffer_load_dwordx4 v230, s[28:31], s83 offen offset:2048 lds\n"
"v_mfma_f32_16x16x32_bf16 v[100:103], a[208:211], a[160:163], v[100:103]\n"
"v_exp_f32 v39, v39\n"
"v_mfma_f32_16x16x32_bf16 v[104:107], a[208:211], a[176:179], v[200:203]\n"
"ds_read_b128 a[232:235], v242 offset:28672\n"
"v_cvt_pk_bf16_f32 v141, v14, v15\n"
"v_cvt_pk_bf16_f32 v149, v38, v39\n"
"v_mfma_f32_16x16x32_bf16 v[104:107], a[212:215], a[180:183], v[104:107]\n"
"v_exp_f32 v52, v52\n"
"v_mfma_f32_16x16x32_bf16 v[108:111], a[212:215], a[196:199], v[204:207]\n"
"ds_read_b128 a[236:239], v243 offset:28672\n"
"v_cvt_pk_bf16_f32 v142, v28, v29\n"
"v_mfma_f32_16x16x32_bf16 v[108:111], a[208:211], a[192:195], v[108:111]\n"
"s_waitcnt lgkmcnt(4)\n"
"v_exp_f32 v53, v53\n"
"v_mfma_f32_16x16x32_bf16 v[96:99], a[216:219], a[152:155], v[96:99]\n"
"v_exp_f32 v54, v54\n"
"v_cvt_pk_bf16_f32 v150, v52, v53\n"
"v_mfma_f32_16x16x32_bf16 v[96:99], a[220:223], a[156:159], v[96:99]\n"
"v_exp_f32 v55, v55\n"
"v_mfma_f32_16x16x32_bf16 v[100:103], a[220:223], a[172:175], v[100:103]\n"
"buffer_load_dwordx4 v231, s[28:31], s83 offen offset:3072 lds\n"
"v_exp_f32 v40, v40\n"
"v_mfma_f32_16x16x32_bf16 v[100:103], a[216:219], a[168:171], v[100:103]\n"
"s_add_u32 s83, s83, 16384\n"
"v_exp_f32 v41, v41\n"
"v_cvt_pk_bf16_f32 v151, v54, v55\n"
"v_mfma_f32_16x16x32_bf16 v[104:107], a[216:219], a[184:187], v[104:107]\n"
"s_and_b32 s83, s83, 0x1ffff\n"
"v_exp_f32 v42, v42\n"
"v_cvt_pk_bf16_f32 v152, v40, v41\n"
"v_mfma_f32_16x16x32_bf16 v[104:107], a[220:223], a[188:191], v[104:107]\n"
"v_exp_f32 v43, v43\n"
"v_mfma_f32_16x16x32_bf16 v[108:111], a[220:223], a[204:207], v[108:111]\n"
"v_exp_f32 v56, v56\n"
"v_cvt_pk_bf16_f32 v153, v42, v43\n"
"v_mfma_f32_16x16x32_bf16 v[108:111], a[216:219], a[200:203], v[108:111]\n"
"s_waitcnt lgkmcnt(0)\n"
"v_exp_f32 v57, v57\n"
"v_mfma_f32_16x16x32_bf16 v[112:115], a[224:227], a[144:147], v[192:195]\n"
"s_waitcnt vmcnt(8)\n"
"s_barrier\n"
"v_exp_f32 v58, v58\n"
"v_cvt_pk_bf16_f32 v154, v56, v57\n"
"v_mfma_f32_16x16x32_bf16 v[112:115], a[228:231], a[148:151], v[112:115]\n"
"s_lshl_b32 s92, s75, 6\n"
"v_exp_f32 v59, v59\n"
"v_mfma_f32_16x16x32_bf16 v[116:119], a[228:231], a[164:167], v[196:199]\n"
"s_add_u32 s92, s92, 2719744\n"
"ds_read_b128 a[208:211], v240 offset:0\n"
"v_mov_b32 v219, s73\n"
"v_cvt_pk_bf16_f32 v155, v58, v59\n"
"v_mfma_f32_16x16x32_bf16 v[116:119], a[224:227], a[160:163], v[116:119]\n"
"s_add_u32 m0, s84, 16384\n"
"v_exp_f32 v44, v44\n"
"v_mfma_f32_16x16x32_bf16 v[120:123], a[224:227], a[176:179], v[200:203]\n"
"ds_read_b128 a[212:215], v241 offset:0\n"
"buffer_store_dword v219, v225, s[36:39], s92 offen sc1\n"
"v_mfma_f32_16x16x32_bf16 v[120:123], a[228:231], a[180:183], v[120:123]\n"
"v_exp_f32 v45, v45\n"
"v_mfma_f32_16x16x32_bf16 v[124:127], a[228:231], a[196:199], v[204:207]\n"
"ds_read_b128 a[216:219], v242 offset:0\n"
"buffer_load_dwordx4 v236, s[24:27], s82 offen offset:0 lds\n"
"v_cvt_pk_bf16_f32 v156, v44, v45\n"
"v_mfma_f32_16x16x32_bf16 v[124:127], a[224:227], a[192:195], v[124:127]\n"
"v_exp_f32 v46, v46\n"
"v_mfma_f32_16x16x32_bf16 v[112:115], a[232:235], a[152:155], v[112:115]\n"
"ds_read_b128 a[220:223], v243 offset:0\n"
"v_cvt_pk_bf16_f32 v143, v30, v31\n"
"v_mfma_f32_16x16x32_bf16 v[112:115], a[236:239], a[156:159], v[112:115]\n"
"v_exp_f32 v47, v47\n"
"v_mfma_f32_16x16x32_bf16 v[116:119], a[236:239], a[172:175], v[116:119]\n"
"v_exp_f32 v60, v60\n"
"v_cvt_pk_bf16_f32 v157, v46, v47\n"
"v_mfma_f32_16x16x32_bf16 v[116:119], a[232:235], a[168:171], v[116:119]\n"
"v_exp_f32 v61, v61\n"
"v_mfma_f32_16x16x32_bf16 v[120:123], a[232:235], a[184:187], v[120:123]\n"
"v_exp_f32 v62, v62\n"
"v_cvt_pk_bf16_f32 v158, v60, v61\n"
"v_mfma_f32_16x16x32_bf16 v[120:123], a[236:239], a[188:191], v[120:123]\n"
"v_exp_f32 v63, v63\n"
"v_mfma_f32_16x16x32_bf16 v[124:127], a[236:239], a[204:207], v[124:127]\n"
"buffer_load_dwordx4 v237, s[24:27], s82 offen offset:1024 lds\n"
"v_cvt_pk_bf16_f32 v144, v32, v33\n"
"v_cvt_pk_bf16_f32 v159, v62, v63\n"
"v_mfma_f32_16x16x32_bf16 v[124:127], a[232:235], a[200:203], v[124:127]\n"
"s_waitcnt lgkmcnt(2)\n"
"v_cvt_pk_bf16_f32 v145, v34, v35\n"
"v_cvt_pk_bf16_f32 v146, v48, v49\n"
"v_mfma_f32_16x16x32_bf16 v[0:3], a[208:211], a[144:147], v[192:195]\n"
"ds_read_b128 a[224:227], v240 offset:4096\n"
"v_and_b32 v221, 31, v229\n"
"v_mfma_f32_16x16x32_bf16 v[0:3], a[212:215], a[148:151], v[0:3]\n"
"v_exp_f32 v64, v64\n"
"v_mfma_f32_16x16x32_bf16 v[4:7], a[212:215], a[164:167], v[196:199]\n"
"ds_read_b128 a[228:231], v241 offset:4096\n"
"v_mfma_f32_16x16x32_bf16 v[4:7], a[208:211], a[160:163], v[4:7]\n"
"v_exp_f32 v65, v65\n"
"v_mfma_f32_16x16x32_bf16 v[8:11], a[208:211], a[176:179], v[200:203]\n"
"ds_read_b128 a[232:235], v242 offset:4096\n"
"v_cvt_pk_bf16_f32 v160, v64, v65\n"
"v_mfma_f32_16x16x32_bf16 v[8:11], a[212:215], a[180:183], v[8:11]\n"
"v_exp_f32 v66, v66\n"
"v_mfma_f32_16x16x32_bf16 v[12:15], a[212:215], a[196:199], v[204:207]\n"
"ds_read_b128 a[236:239], v243 offset:4096\n"
"buffer_load_dwordx4 v238, s[24:27], s82 offen offset:2048 lds\n"
"v_mfma_f32_16x16x32_bf16 v[12:15], a[208:211], a[192:195], v[12:15]\n"
"s_waitcnt lgkmcnt(4)\n"
"v_exp_f32 v67, v67\n"
"v_mfma_f32_16x16x32_bf16 v[0:3], a[216:219], a[152:155], v[0:3]\n"
"v_exp_f32 v80, v80\n"
"v_mfma_f32_16x16x32_bf16 v[0:3], a[220:223], a[156:159], v[0:3]\n"
"v_exp_f32 v81, v81\n"
"v_mfma_f32_16x16x32_bf16 v[4:7], a[220:223], a[172:175], v[4:7]\n"
"v_lshlrev_b32 v221, 6, v221\n"
"v_cvt_pk_bf16_f32 v161, v66, v67\n"
"v_mfma_f32_16x16x32_bf16 v[4:7], a[216:219], a[168:171], v[4:7]\n"
"v_exp_f32 v82, v82\n"
"v_mfma_f32_16x16x32_bf16 v[8:11], a[216:219], a[184:187], v[8:11]\n"
"v_cvt_pk_bf16_f32 v162, v80, v81\n"
"v_mfma_f32_16x16x32_bf16 v[8:11], a[220:223], a[188:191], v[8:11]\n"
"v_exp_f32 v83, v83\n"
"v_mfma_f32_16x16x32_bf16 v[12:15], a[220:223], a[204:207], v[12:15]\n"
"buffer_load_dwordx4 v239, s[24:27], s82 offen offset:3072 lds\n"
"v_cvt_pk_bf16_f32 v163, v82, v83\n"
"v_mfma_f32_16x16x32_bf16 v[12:15], a[216:219], a[200:203], v[12:15]\n"
"s_waitcnt lgkmcnt(2)\n"
"v_exp_f32 v68, v68\n"
"v_mfma_f32_16x16x32_bf16 v[16:19], a[224:227], a[144:147], v[192:195]\n"
"s_add_u32 s82, s82, 16384\n"
"s_and_b32 s82, s82, 0x1ffff\n"
"ds_read_b128 a[208:211], v240 offset:8192\n"
"v_mfma_f32_16x16x32_bf16 v[16:19], a[228:231], a[148:151], v[16:19]\n"
"s_add_u32 s90, s42, 0\n"
"v_exp_f32 v69, v69\n"
"v_mfma_f32_16x16x32_bf16 v[20:23], a[228:231], a[164:167], v[196:199]\n"
"s_add_u32 m0, s84, -32768\n"
"ds_read_b128 a[212:215], v241 offset:8192\n"
"v_cvt_pk_bf16_f32 v164, v68, v69\n"
"v_mfma_f32_16x16x32_bf16 v[20:23], a[224:227], a[160:163], v[20:23]\n"
"v_exp_f32 v70, v70\n"
"v_mfma_f32_16x16x32_bf16 v[24:27], a[224:227], a[176:179], v[200:203]\n"
"s_lshl_b32 s95, s69, 11\n"
"ds_read_b128 a[216:219], v242 offset:8192\n"
"v_mfma_f32_16x16x32_bf16 v[24:27], a[228:231], a[180:183], v[24:27]\n"
"v_exp_f32 v71, v71\n"
"v_mfma_f32_16x16x32_bf16 v[28:31], a[228:231], a[196:199], v[204:207]\n"
"ds_read_b128 a[220:223], v243 offset:8192\n"
"buffer_load_dwordx4 v212, s[60:63], s90 offen nt lds\n"
"v_mfma_f32_16x16x32_bf16 v[28:31], a[224:227], a[192:195], v[28:31]\n"
"s_waitcnt lgkmcnt(4)\n"
"v_exp_f32 v84, v84\n"
"v_mfma_f32_16x16x32_bf16 v[16:19], a[232:235], a[152:155], v[16:19]\n"
"s_add_u32 s95, s95, 2719744\n"
"s_add_u32 s90, s42, 10240\n"
"s_add_u32 m0, s84, -31744\n"
"v_mfma_f32_16x16x32_bf16 v[16:19], a[236:239], a[156:159], v[16:19]\n"
"v_exp_f32 v85, v85\n"
"v_mfma_f32_16x16x32_bf16 v[20:23], a[236:239], a[172:175], v[20:23]\n"
"v_min_u32 v222, 15, v229\n"
"v_cvt_pk_bf16_f32 v165, v70, v71\n"
"v_mfma_f32_16x16x32_bf16 v[20:23], a[232:235], a[168:171], v[20:23]\n"
"v_exp_f32 v86, v86\n"
"v_mfma_f32_16x16x32_bf16 v[24:27], a[232:235], a[184:187], v[24:27]\n"
"v_cvt_pk_bf16_f32 v166, v84, v85\n"
"v_mfma_f32_16x16x32_bf16 v[24:27], a[236:239], a[188:191], v[24:27]\n"
"v_exp_f32 v87, v87\n"
"v_mfma_f32_16x16x32_bf16 v[28:31], a[236:239], a[204:207], v[28:31]\n"
"buffer_load_dwordx4 v212, s[60:63], s90 offen nt lds\n"
"v_cvt_pk_bf16_f32 v167, v86, v87\n"
"v_mfma_f32_16x16x32_bf16 v[28:31], a[232:235], a[200:203], v[28:31]\n"
"s_waitcnt lgkmcnt(2)\n"
"v_exp_f32 v72, v72\n"
"v_mfma_f32_16x16x32_bf16 v[32:35], a[208:211], a[144:147], v[192:195]\n"
"s_add_u32 s90, s42, 20480\n"
"s_add_u32 m0, s84, -30720\n"
"ds_read_b128 a[224:227], v240 offset:12288\n"
"v_mfma_f32_16x16x32_bf16 v[32:35], a[212:215], a[148:151], v[32:35]\n"
"v_exp_f32 v73, v73\n"
"v_mfma_f32_16x16x32_bf16 v[36:39], a[212:215], a[164:167], v[196:199]\n"
"ds_read_b128 a[228:231], v241 offset:12288\n"
"v_cvt_pk_bf16_f32 v168, v72, v73\n"
"v_mfma_f32_16x16x32_bf16 v[36:39], a[208:211], a[160:163], v[36:39]\n"
"v_exp_f32 v74, v74\n"
"v_mfma_f32_16x16x32_bf16 v[40:43], a[208:211], a[176:179], v[200:203]\n"
"ds_read_b128 a[232:235], v242 offset:12288\n"
"v_lshlrev_b32 v222, 7, v222\n"
"v_mfma_f32_16x16x32_bf16 v[40:43], a[212:215], a[180:183], v[40:43]\n"
"v_exp_f32 v75, v75\n"
"v_mfma_f32_16x16x32_bf16 v[44:47], a[212:215], a[196:199], v[204:207]\n"
"ds_read_b128 a[236:239], v243 offset:12288\n"
"buffer_load_dwordx4 v212, s[60:63], s90 offen nt lds\n"
"v_mfma_f32_16x16x32_bf16 v[44:47], a[208:211], a[192:195], v[44:47]\n"
"s_waitcnt lgkmcnt(4)\n"
"v_exp_f32 v88, v88\n"
"v_mfma_f32_16x16x32_bf16 v[32:35], a[216:219], a[152:155], v[32:35]\n"
"s_add_u32 s90, s42, 30720\n"
"s_add_u32 m0, s84, -29696\n"
"v_cvt_pk_bf16_f32 v169, v74, v75\n"
"v_mfma_f32_16x16x32_bf16 v[32:35], a[220:223], a[156:159], v[32:35]\n"
"v_exp_f32 v89, v89\n"
"v_mfma_f32_16x16x32_bf16 v[36:39], a[220:223], a[172:175], v[36:39]\n"
"v_cvt_pk_bf16_f32 v170, v88, v89\n"
"v_mfma_f32_16x16x32_bf16 v[36:39], a[216:219], a[168:171], v[36:39]\n"
"v_exp_f32 v90, v90\n"
"v_mfma_f32_16x16x32_bf16 v[40:43], a[216:219], a[184:187], v[40:43]\n"
"v_mfma_f32_16x16x32_bf16 v[40:43], a[220:223], a[188:191], v[40:43]\n"
"v_exp_f32 v91, v91\n"
"v_mfma_f32_16x16x32_bf16 v[44:47], a[220:223], a[204:207], v[44:47]\n"
"s_lshl2_add_u32 s96, s72, s68\n"
"buffer_load_dwordx4 v212, s[60:63], s90 offen nt lds\n"
"v_cvt_pk_bf16_f32 v171, v90, v91\n"
"v_mfma_f32_16x16x32_bf16 v[44:47], a[216:219], a[200:203], v[44:47]\n"
"s_waitcnt lgkmcnt(2)\n"
"v_exp_f32 v76, v76\n"
"v_mfma_f32_16x16x32_bf16 v[48:51], a[224:227], a[144:147], v[192:195]\n"
"s_add_u32 m0, s81, 0\n"
"ds_read_b64_tr_b16 a[208:209], v232 offset:0\n"
"v_mfma_f32_16x16x32_bf16 v[48:51], a[228:231], a[148:151], v[48:51]\n"
"s_mul_i32 s96, s96, 10240\n"
"v_exp_f32 v77, v77\n"
"v_mfma_f32_16x16x32_bf16 v[52:55], a[228:231], a[164:167], v[196:199]\n"
"ds_read_b64_tr_b16 a[210:211], v232 offset:4096\n"
"v_cvt_pk_bf16_f32 v172, v76, v77\n"
"v_mfma_f32_16x16x32_bf16 v[52:55], a[224:227], a[160:163], v[52:55]\n"
"v_exp_f32 v78, v78\n"
"v_mfma_f32_16x16x32_bf16 v[56:59], a[224:227], a[176:179], v[200:203]\n"
"s_mul_i32 s97, s69, 327680\n"
"ds_read_b64_tr_b16 a[212:213], v232 offset:8192\n"
"v_mfma_f32_16x16x32_bf16 v[56:59], a[228:231], a[180:183], v[56:59]\n"
"v_exp_f32 v79, v79\n"
"v_mfma_f32_16x16x32_bf16 v[60:63], a[228:231], a[196:199], v[204:207]\n"
"s_add_u32 s96, s96, s97\n"
"ds_read_b64_tr_b16 a[214:215], v232 offset:12288\n"
"buffer_load_dwordx4 v252, s[16:19], s80 offen lds\n"
"v_mfma_f32_16x16x32_bf16 v[60:63], a[224:227], a[192:195], v[60:63]\n"
"s_waitcnt lgkmcnt(4)\n"
"v_exp_f32 v92, v92\n"
"v_mfma_f32_16x16x32_bf16 v[48:51], a[232:235], a[152:155], v[48:51]\n"
"s_add_u32 m0, s81, 1024\n"
"ds_read_b64_tr_b16 a[216:217], v233 offset:0\n"
"v_cvt_pk_bf16_f32 v173, v78, v79\n"
"v_mfma_f32_16x16x32_bf16 v[48:51], a[236:239], a[156:159], v[48:51]\n"
"s_add_u32 s96, s96, 65536\n"
"v_exp_f32 v93, v93\n"
"v_mfma_f32_16x16x32_bf16 v[52:55], a[236:239], a[172:175], v[52:55]\n"
"ds_read_b64_tr_b16 a[218:219], v233 offset:4096\n"
"v_cvt_pk_bf16_f32 v174, v92, v93\n"
"v_mfma_f32_16x16x32_bf16 v[52:55], a[232:235], a[168:171], v[52:55]\n"
"v_exp_f32 v94, v94\n"
"v_mfma_f32_16x16x32_bf16 v[56:59], a[232:235], a[184:187], v[56:59]\n"
"ds_read_b64_tr_b16 a[220:221], v233 offset:8192\n"
"v_mfma_f32_16x16x32_bf16 v[56:59], a[236:239], a[188:191], v[56:59]\n"
"v_exp_f32 v95, v95\n"
"v_mfma_f32_16x16x32_bf16 v[60:63], a[236:239], a[204:207], v[60:63]\n"
"ds_read_b64_tr_b16 a[222:223], v233 offset:12288\n"
"buffer_load_dwordx4 v253, s[16:19], s80 offen lds\n"
"v_mfma_f32_16x16x32_bf16 v[60:63], a[232:235], a[200:203], v[60:63]\n"
"s_waitcnt lgkmcnt(4)\n"
"v_exp_f32 v96, v96\n"
"v_mfma_f32_16x16x32_bf16 a[0:3], a[208:211], v[128:131], 0\n"
"s_add_u32 m0, s81, 8192\n"
"ds_read_b64_tr_b16 a[224:225], v234 offset:0\n"
"v_cvt_pk_bf16_f32 v175, v94, v95\n"
"v_mfma_f32_16x16x32_bf16 a[0:3], a[212:215], v[144:147], a[0:3]\n"
"v_exp_f32 v97, v97\n"
"v_mfma_f32_16x16x32_bf16 a[4:7], a[212:215], v[148:151], 0\n"
"ds_read_b64_tr_b16 a[226:227], v234 offset:4096\n"
"v_cvt_pk_bf16_f32 v176, v96, v97\n"
"v_mfma_f32_16x16x32_bf16 a[4:7], a[208:211], v[132:135], a[4:7]\n"
"v_exp_f32 v98, v98\n"
"v_mfma_f32_16x16x32_bf16 a[8:11], a[208:211], v[136:139], 0\n"
"ds_read_b64_tr_b16 a[228:229], v234 offset:8192\n"
"v_mfma_f32_16x16x32_bf16 a[8:11], a[212:215], v[152:155], a[8:11]\n"
"v_exp_f32 v99, v99\n"
"v_mfma_f32_16x16x32_bf16 a[12:15], a[212:215], v[156:159], 0\n"
"ds_read_b64_tr_b16 a[230:231], v234 offset:12288\n"
"buffer_load_dwordx4 v252, s[20:23], s80 offen lds\n"
"v_mfma_f32_16x16x32_bf16 a[12:15], a[208:211], v[140:143], a[12:15]\n"
"s_waitcnt lgkmcnt(4)\n"
"v_exp_f32 v112, v112\n"
"v_mfma_f32_16x16x32_bf16 a[16:19], a[216:219], v[128:131], 0\n"
"s_add_u32 m0, s81, 9216\n"
"ds_read_b64_tr_b16 a[232:233], v235 offset:0\n"
"v_cvt_pk_bf16_f32 v177, v98, v99\n"
"v_mfma_f32_16x16x32_bf16 a[16:19], a[220:223], v[144:147], a[16:19]\n"
"v_exp_f32 v113, v113\n"
"v_mfma_f32_16x16x32_bf16 a[20:23], a[220:223], v[148:151], 0\n"
"ds_read_b64_tr_b16 a[234:235], v235 offset:4096\n"
"v_cvt_pk_bf16_f32 v178, v112, v113\n"
"v_mfma_f32_16x16x32_bf16 a[20:23], a[216:219], v[132:135], a[20:23]\n"
"v_exp_f32 v114, v114\n"
"v_mfma_f32_16x16x32_bf16 a[24:27], a[216:219], v[136:139], 0\n"
"ds_read_b64_tr_b16 a[236:237], v235 offset:8192\n"
"v_mfma_f32_16x16x32_bf16 a[24:27], a[220:223], v[152:155], a[24:27]\n"
"v_exp_f32 v115, v115\n"
"v_mfma_f32_16x16x32_bf16 a[28:31], a[220:223], v[156:159], 0\n"
"ds_read_b64_tr_b16 a[238:239], v235 offset:12288\n"
"buffer_load_dwordx4 v253, s[20:23], s80 offen lds\n"
"v_mfma_f32_16x16x32_bf16 a[28:31], a[216:219], v[140:143], a[28:31]\n"
"s_waitcnt lgkmcnt(4)\n"
"v_exp_f32 v100, v100\n"
"v_mfma_f32_16x16x32_bf16 a[32:35], a[224:227], v[128:131], 0\n"
"s_add_u32 s80, s80, 221184\n"
"s_sub_u32 s86, s80, 2211840\n"
"ds_read_b64_tr_b16 a[208:209], v232 offset:512\n"
"v_mfma_f32_16x16x32_bf16 a[32:35], a[228:231], v[144:147], a[32:35]\n"
"s_min_u32 s80, s80, s86\n"
"v_exp_f32 v101, v101\n"
"v_mfma_f32_16x16x32_bf16 a[36:39], a[228:231], v[148:151], 0\n"
"ds_read_b64_tr_b16 a[210:211], v232 offset:4608\n"
"v_cvt_pk_bf16_f32 v179, v114, v115\n"
"v_mfma_f32_16x16x32_bf16 a[36:39], a[224:227], v[132:135], a[36:39]\n"
"v_exp_f32 v102, v102\n"
"v_mfma_f32_16x16x32_bf16 a[40:43], a[224:227], v[136:139], 0\n"
"ds_read_b64_tr_b16 a[212:213], v232 offset:8704\n"
"v_cvt_pk_bf16_f32 v180, v100, v101\n"
"v_mfma_f32_16x16x32_bf16 a[40:43], a[228:231], v[152:155], a[40:43]\n"
"v_exp_f32 v103, v103\n"
"v_mfma_f32_16x16x32_bf16 a[44:47], a[228:231], v[156:159], 0\n"
"ds_read_b64_tr_b16 a[214:215], v232 offset:12800\n"
"v_cvt_pk_bf16_f32 v181, v102, v103\n"
"v_mfma_f32_16x16x32_bf16 a[44:47], a[224:227], v[140:143], a[44:47]\n"
"s_waitcnt lgkmcnt(4)\n"
"v_exp_f32 v116, v116\n"
"v_mfma_f32_16x16x32_bf16 a[48:51], a[232:235], v[128:131], 0\n"
"ds_read_b64_tr_b16 a[216:217], v233 offset:512\n"
"v_mfma_f32_16x16x32_bf16 a[48:51], a[236:239], v[144:147], a[48:51]\n"
"v_exp_f32 v117, v117\n"
"v_mfma_f32_16x16x32_bf16 a[52:55], a[236:239], v[148:151], 0\n"
"ds_read_b64_tr_b16 a[218:219], v233 offset:4608\n"
"v_cvt_pk_bf16_f32 v182, v116, v117\n"
"v_mfma_f32_16x16x32_bf16 a[52:55], a[232:235], v[132:135], a[52:55]\n"
"v_exp_f32 v118, v118\n"
"v_mfma_f32_16x16x32_bf16 a[56:59], a[232:235], v[136:139], 0\n"
"ds_read_b64_tr_b16 a[220:221], v233 offset:8704\n"
"v_mfma_f32_16x16x32_bf16 a[56:59], a[236:239], v[152:155], a[56:59]\n"
"v_exp_f32 v119, v119\n"
"v_mfma_f32_16x16x32_bf16 a[60:63], a[236:239], v[156:159], 0\n"
"ds_read_b64_tr_b16 a[222:223], v233 offset:12800\n"
"v_cvt_pk_bf16_f32 v183, v118, v119\n"
"v_mfma_f32_16x16x32_bf16 a[60:63], a[232:235], v[140:143], a[60:63]\n"
"s_waitcnt lgkmcnt(4)\n"
"v_exp_f32 v104, v104\n"
"v_mfma_f32_16x16x32_bf16 a[64:67], a[208:211], v[128:131], 0\n"
".L_pad5:\n"
"s_cbranch_execz .L_padret0\n"
"ds_read_b64_tr_b16 a[224:225], v234 offset:512\n"
"v_mfma_f32_16x16x32_bf16 a[64:67], a[212:215], v[144:147], a[64:67]\n"
"ds_read_b64_tr_b16 a[226:227], v234 offset:4608\n"
"v_mfma_f32_16x16x32_bf16 a[68:71], a[212:215], v[148:151], 0\n"
"v_mfma_f32_16x16x32_bf16 a[68:71], a[208:211], v[132:135], a[68:71]\n"
"ds_read_b64_tr_b16 a[228:229], v234 offset:8704\n"
"v_mfma_f32_16x16x32_bf16 a[72:75], a[208:211], v[136:139], 0\n"
"ds_read_b64_tr_b16 a[230:231], v234 offset:12800\n"
"v_mfma_f32_16x16x32_bf16 a[72:75], a[212:215], v[152:155], a[72:75]\n"
"v_exp_f32 v105, v105\n"
"v_mfma_f32_16x16x32_bf16 a[76:79], a[212:215], v[156:159], 0\n"
"ds_read_b64_tr_b16 a[232:233], v235 offset:512\n"
"v_cvt_pk_bf16_f32 v184, v104, v105\n"
"v_mfma_f32_16x16x32_bf16 a[76:79], a[208:211], v[140:143], a[76:79]\n"
"s_waitcnt lgkmcnt(5)\n"
"ds_read_b64_tr_b16 a[234:235], v235 offset:4608\n"
"v_mfma_f32_16x16x32_bf16 a[80:83], a[216:219], v[128:131], 0\n"
"v_mfma_f32_16x16x32_bf16 a[80:83], a[220:223], v[144:147], a[80:83]\n"
"ds_read_b64_tr_b16 a[236:237], v235 offset:8704\n"
"v_mfma_f32_16x16x32_bf16 a[84:87], a[220:223], v[148:151], 0\n"
"ds_read_b64_tr_b16 a[238:239], v235 offset:12800\n"
"v_mfma_f32_16x16x32_bf16 a[84:87], a[216:219], v[132:135], a[84:87]\n"
"v_exp_f32 v106, v106\n"
"v_mfma_f32_16x16x32_bf16 a[88:91], a[216:219], v[136:139], 0\n"
"v_mfma_f32_16x16x32_bf16 a[88:91], a[220:223], v[152:155], a[88:91]\n"
"v_exp_f32 v107, v107\n"
"v_mfma_f32_16x16x32_bf16 a[92:95], a[220:223], v[156:159], 0\n"
"v_cvt_pk_bf16_f32 v185, v106, v107\n"
"v_mfma_f32_16x16x32_bf16 a[92:95], a[216:219], v[140:143], a[92:95]\n"
"s_waitcnt lgkmcnt(0)\n"
"v_exp_f32 v120, v120\n"
"v_mfma_f32_16x16x32_bf16 a[96:99], a[224:227], v[128:131], 0\n"
"s_waitcnt vmcnt(17)\n"
"s_barrier\n"
"v_mfma_f32_16x16x32_bf16 a[96:99], a[228:231], v[144:147], a[96:99]\n"
"s_add_u32 m0, s84, 0\n"
"v_exp_f32 v121, v121\n"
"v_mfma_f32_16x16x32_bf16 a[100:103], a[228:231], v[148:151], 0\n"
"ds_read_b128 a[208:211], v240 offset:32768\n"
"buffer_load_dwordx4 v236, s[24:27], s82 offen offset:0 lds\n"
"v_mfma_f32_16x16x32_bf16 a[100:103], a[224:227], v[132:135], a[100:103]\n"
"v_exp_f32 v122, v122\n"
"v_mfma_f32_16x16x32_bf16 a[104:107], a[224:227], v[136:139], 0\n"
"ds_read_b128 a[212:215], v241 offset:32768\n"
"v_cvt_pk_bf16_f32 v186, v120, v121\n"
"v_mfma_f32_16x16x32_bf16 a[104:107], a[228:231], v[152:155], a[104:107]\n"
"v_exp_f32 v123, v123\n"
"v_mfma_f32_16x16x32_bf16 a[108:111], a[228:231], v[156:159], 0\n"
"ds_read_b128 a[216:219], v242 offset:32768\n"
"v_cvt_pk_bf16_f32 v187, v122, v123\n"
"v_mfma_f32_16x16x32_bf16 a[108:111], a[224:227], v[140:143], a[108:111]\n"
"v_exp_f32 v108, v108\n"
"v_mfma_f32_16x16x32_bf16 a[112:115], a[232:235], v[128:131], 0\n"
"ds_read_b128 a[220:223], v243 offset:32768\n"
"v_mfma_f32_16x16x32_bf16 a[112:115], a[236:239], v[144:147], a[112:115]\n"
"v_exp_f32 v109, v109\n"
"v_mfma_f32_16x16x32_bf16 a[116:119], a[236:239], v[148:151], 0\n"
"buffer_load_dwordx4 v237, s[24:27], s82 offen offset:1024 lds\n"
"v_cvt_pk_bf16_f32 v188, v108, v109\n"
"v_mfma_f32_16x16x32_bf16 a[116:119], a[232:235], v[132:135], a[116:119]\n"
"v_exp_f32 v110, v110\n"
"v_mfma_f32_16x16x32_bf16 a[120:123], a[232:235], v[136:139], 0\n"
"v_mfma_f32_16x16x32_bf16 a[120:123], a[236:239], v[152:155], a[120:123]\n"
"v_exp_f32 v111, v111\n"
"v_mfma_f32_16x16x32_bf16 a[124:127], a[236:239], v[156:159], 0\n"
"v_cvt_pk_bf16_f32 v189, v110, v111\n"
"v_mfma_f32_16x16x32_bf16 a[124:127], a[232:235], v[140:143], a[124:127]\n"
"v_exp_f32 v124, v124\n"
"v_mfma_f32_16x16x32_bf16 a[128:131], v[208:211], v[128:131], 0\n"
"v_mfma_f32_16x16x32_bf16 a[128:131], v[208:211], v[144:147], a[128:131]\n"
"v_exp_f32 v125, v125\n"
"v_mfma_f32_16x16x32_bf16 a[132:135], v[208:211], v[132:135], 0\n"
"buffer_load_dwordx4 v238, s[24:27], s82 offen offset:2048 lds\n"
"v_cvt_pk_bf16_f32 v190, v124, v125\n"
"v_mfma_f32_16x16x32_bf16 a[132:135], v[208:211], v[148:151], a[132:135]\n"
"v_exp_f32 v126, v126\n"
"v_mfma_f32_16x16x32_bf16 a[136:139], v[208:211], v[136:139], 0\n"
"v_mfma_f32_16x16x32_bf16 a[136:139], v[208:211], v[152:155], a[136:139]\n"
"v_exp_f32 v127, v127\n"
"v_mfma_f32_16x16x32_bf16 a[140:143], v[208:211], v[140:143], 0\n"
"v_cvt_pk_bf16_f32 v191, v126, v127\n"
"v_mfma_f32_16x16x32_bf16 a[140:143], v[208:211], v[156:159], a[140:143]\n"
"s_waitcnt lgkmcnt(2)\n"
"v_mfma_f32_16x16x32_bf16 v[64:67], a[208:211], a[144:147], v[192:195]\n"
"ds_read_b128 a[224:227], v240 offset:36864\n"
"v_mfma_f32_16x16x32_bf16 v[64:67], a[212:215], a[148:151], v[64:67]\n"
"v_exp_f32 v0, v0\n"
"v_mfma_f32_16x16x32_bf16 v[68:71], a[212:215], a[164:167], v[196:199]\n"
"ds_read_b128 a[228:231], v241 offset:36864\n"
"buffer_load_dwordx4 v239, s[24:27], s82 offen offset:3072 lds\n"
"v_mfma_f32_16x16x32_bf16 v[68:71], a[208:211], a[160:163], v[68:71]\n"
"s_add_u32 s82, s82, 16384\n"
"v_exp_f32 v1, v1\n"
"v_mfma_f32_16x16x32_bf16 v[72:75], a[208:211], a[176:179], v[200:203]\n"
"s_and_b32 s82, s82, 0x1ffff\n"
"s_add_u32 m0, s85, 0\n"
"ds_read_b128 a[232:235], v242 offset:36864\n"
"v_mfma_f32_16x16x32_bf16 v[72:75], a[212:215], a[180:183], v[72:75]\n"
"v_exp_f32 v2, v2\n"
"v_mfma_f32_16x16x32_bf16 v[76:79], a[212:215], a[196:199], v[204:207]\n"
"ds_read_b128 a[236:239], v243 offset:36864\n"
"v_cvt_pk_bf16_f32 v128, v0, v1\n"
"v_mfma_f32_16x16x32_bf16 v[76:79], a[208:211], a[192:195], v[76:79]\n"
"s_waitcnt lgkmcnt(4)\n"
"v_exp_f32 v3, v3\n"
"v_mfma_f32_16x16x32_bf16 v[64:67], a[216:219], a[152:155], v[64:67]\n"
"v_exp_f32 v16, v16\n"
"v_mfma_f32_16x16x32_bf16 v[64:67], a[220:223], a[156:159], v[64:67]\n"
"v_exp_f32 v17, v17\n"
"v_mfma_f32_16x16x32_bf16 v[68:71], a[220:223], a[172:175], v[68:71]\n"
"buffer_load_dwordx4 v230, s[28:31], s83 offen offset:0 lds\n"
"v_cvt_pk_bf16_f32 v129, v2, v3\n"
"v_mfma_f32_16x16x32_bf16 v[68:71], a[216:219], a[168:171], v[68:71]\n"
"v_exp_f32 v18, v18\n"
"v_mfma_f32_16x16x32_bf16 v[72:75], a[216:219], a[184:187], v[72:75]\n"
"v_cvt_pk_bf16_f32 v130, v16, v17\n"
"v_mfma_f32_16x16x32_bf16 v[72:75], a[220:223], a[188:191], v[72:75]\n"
"v_exp_f32 v19, v19\n"
"v_mfma_f32_16x16x32_bf16 v[76:79], a[220:223], a[204:207], v[76:79]\n"
"v_cvt_pk_bf16_f32 v131, v18, v19\n"
"v_mfma_f32_16x16x32_bf16 v[76:79], a[216:219], a[200:203], v[76:79]\n"
"s_waitcnt lgkmcnt(2)\n"
"v_exp_f32 v4, v4\n"
"v_mfma_f32_16x16x32_bf16 v[80:83], a[224:227], a[144:147], v[192:195]\n"
"ds_read_b128 a[208:211], v240 offset:40960\n"
"v_mfma_f32_16x16x32_bf16 v[80:83], a[228:231], a[148:151], v[80:83]\n"
"v_exp_f32 v5, v5\n"
"v_mfma_f32_16x16x32_bf16 v[84:87], a[228:231], a[164:167], v[196:199]\n"
"ds_read_b128 a[212:215], v241 offset:40960\n"
"buffer_load_dwordx4 v231, s[28:31], s83 offen offset:1024 lds\n"
"v_mfma_f32_16x16x32_bf16 v[84:87], a[224:227], a[160:163], v[84:87]\n"
"v_exp_f32 v6, v6\n"
"v_mfma_f32_16x16x32_bf16 v[88:91], a[224:227], a[176:179], v[200:203]\n"
"ds_read_b128 a[216:219], v242 offset:40960\n"
"v_cvt_pk_bf16_f32 v132, v4, v5\n"
"v_mfma_f32_16x16x32_bf16 v[88:91], a[228:231], a[180:183], v[88:91]\n"
"v_exp_f32 v7, v7\n"
"v_mfma_f32_16x16x32_bf16 v[92:95], a[228:231], a[196:199], v[204:207]\n"
"ds_read_b128 a[220:223], v243 offset:40960\n"
"v_cvt_pk_bf16_f32 v133, v6, v7\n"
"v_mfma_f32_16x16x32_bf16 v[92:95], a[224:227], a[192:195], v[92:95]\n"
"s_waitcnt lgkmcnt(4)\n"
"v_exp_f32 v20, v20\n"
"v_mfma_f32_16x16x32_bf16 v[80:83], a[232:235], a[152:155], v[80:83]\n"
"v_mfma_f32_16x16x32_bf16 v[80:83], a[236:239], a[156:159], v[80:83]\n"
"v_exp_f32 v21, v21\n"
"v_mfma_f32_16x16x32_bf16 v[84:87], a[236:239], a[172:175], v[84:87]\n"
"buffer_load_dwordx4 v230, s[28:31], s83 offen offset:2048 lds\n"
"v_cvt_pk_bf16_f32 v134, v20, v21\n"
"v_mfma_f32_16x16x32_bf16 v[84:87], a[232:235], a[168:171], v[84:87]\n"
"v_exp_f32 v22, v22\n"
"v_mfma_f32_16x16x32_bf16 v[88:91], a[232:235], a[184:187], v[88:91]\n"
"v_mfma_f32_16x16x32_bf16 v[88:91], a[236:239], a[188:191], v[88:91]\n"
"v_exp_f32 v23, v23\n"
"v_mfma_f32_16x16x32_bf16 v[92:95], a[236:239], a[204:207], v[92:95]\n"
"v_cvt_pk_bf16_f32 v135, v22, v23\n"
"v_mfma_f32_16x16x32_bf16 v[92:95], a[232:235], a[200:203], v[92:95]\n"
"s_waitcnt lgkmcnt(2)\n"
"v_exp_f32 v8, v8\n"
"v_mfma_f32_16x16x32_bf16 v[96:99], a[208:211], a[144:147], v[192:195]\n"
"ds_read_b128 a[224:227], v240 offset:45056\n"
"v_mfma_f32_16x16x32_bf16 v[96:99], a[212:215], a[148:151], v[96:99]\n"
"v_exp_f32 v9, v9\n"
"v_mfma_f32_16x16x32_bf16 v[100:103], a[212:215], a[164:167], v[196:199]\n"
"ds_read_b128 a[228:231], v241 offset:45056\n"
"buffer_load_dwordx4 v231, s[28:31], s83 offen offset:3072 lds\n"
"v_mfma_f32_16x16x32_bf16 v[100:103], a[208:211], a[160:163], v[100:103]\n"
"s_add_u32 s83, s83, 16384\n"
"v_exp_f32 v10, v10\n"
"v_mfma_f32_16x16x32_bf16 v[104:107], a[208:211], a[176:179], v[200:203]\n"
"s_and_b32 s83, s83, 0x1ffff\n"
"s_add_u32 s90, s43, 0\n"
"ds_read_b128 a[232:235], v242 offset:45056\n"
"v_mfma_f32_16x16x32_bf16 v[104:107], a[212:215], a[180:183], v[104:107]\n"
"s_add_u32 m0, s84, -16384\n"
"v_exp_f32 v11, v11\n"
"v_mfma_f32_16x16x32_bf16 v[108:111], a[212:215], a[196:199], v[204:207]\n"
"ds_read_b128 a[236:239], v243 offset:45056\n"
"v_cvt_pk_bf16_f32 v136, v8, v9\n"
"v_mfma_f32_16x16x32_bf16 v[108:111], a[208:211], a[192:195], v[108:111]\n"
"s_waitcnt lgkmcnt(4)\n"
"v_exp_f32 v24, v24\n"
"v_mfma_f32_16x16x32_bf16 v[96:99], a[216:219], a[152:155], v[96:99]\n"
"v_cvt_pk_bf16_f32 v137, v10, v11\n"
"v_mfma_f32_16x16x32_bf16 v[96:99], a[220:223], a[156:159], v[96:99]\n"
"v_exp_f32 v25, v25\n"
"v_mfma_f32_16x16x32_bf16 v[100:103], a[220:223], a[172:175], v[100:103]\n"
"buffer_load_dwordx4 v212, s[64:67], s90 offen nt lds\n"
"v_cvt_pk_bf16_f32 v138, v24, v25\n"
"v_mfma_f32_16x16x32_bf16 v[100:103], a[216:219], a[168:171], v[100:103]\n"
"s_add_u32 s90, s43, 10240\n"
"v_exp_f32 v26, v26\n"
"v_mfma_f32_16x16x32_bf16 v[104:107], a[216:219], a[184:187], v[104:107]\n"
"s_add_u32 m0, s84, -15360\n"
"v_mfma_f32_16x16x32_bf16 v[104:107], a[220:223], a[188:191], v[104:107]\n"
"v_exp_f32 v27, v27\n"
"v_mfma_f32_16x16x32_bf16 v[108:111], a[220:223], a[204:207], v[108:111]\n"
"v_cvt_pk_bf16_f32 v139, v26, v27\n"
"v_mfma_f32_16x16x32_bf16 v[108:111], a[216:219], a[200:203], v[108:111]\n"
"s_waitcnt lgkmcnt(2)\n"
"v_exp_f32 v12, v12\n"
"v_mfma_f32_16x16x32_bf16 v[112:115], a[224:227], a[144:147], v[192:195]\n"
"ds_read_b64_tr_b16 a[208:209], v232 offset:16384\n"
"v_mfma_f32_16x16x32_bf16 v[112:115], a[228:231], a[148:151], v[112:115]\n"
"v_exp_f32 v13, v13\n"
"v_mfma_f32_16x16x32_bf16 v[116:119], a[228:231], a[164:167], v[196:199]\n"
"ds_read_b64_tr_b16 a[210:211], v232 offset:20480\n"
"buffer_load_dwordx4 v212, s[64:67], s90 offen nt lds\n"
"v_mfma_f32_16x16x32_bf16 v[116:119], a[224:227], a[160:163], v[116:119]\n"
"s_add_u32 s90, s43, 20480\n"
"v_exp_f32 v14, v14\n"
"v_mfma_f32_16x16x32_bf16 v[120:123], a[224:227], a[176:179], v[200:203]\n"
"s_add_u32 m0, s84, -14336\n"
"ds_read_b64_tr_b16 a[212:213], v232 offset:24576\n"
"v_cvt_pk_bf16_f32 v140, v12, v13\n"
"v_mfma_f32_16x16x32_bf16 v[120:123], a[228:231], a[180:183], v[120:123]\n"
"v_exp_f32 v15, v15\n"
"v_mfma_f32_16x16x32_bf16 v[124:127], a[228:231], a[196:199], v[204:207]\n"
"ds_read_b64_tr_b16 a[214:215], v232 offset:28672\n"
"v_cvt_pk_bf16_f32 v141, v14, v15\n"
"v_mfma_f32_16x16x32_bf16 v[124:127], a[224:227], a[192:195], v[124:127]\n"
"s_waitcnt lgkmcnt(4)\n"
"v_exp_f32 v28, v28\n"
"v_mfma_f32_16x16x32_bf16 v[112:115], a[232:235], a[152:155], v[112:115]\n"
"ds_read_b64_tr_b16 a[216:217], v233 offset:16384\n"
"v_mfma_f32_16x16x32_bf16 v[112:115], a[236:239], a[156:159], v[112:115]\n"
"v_exp_f32 v29, v29\n"
"v_mfma_f32_16x16x32_bf16 v[116:119], a[236:239], a[172:175], v[116:119]\n"
"ds_read_b64_tr_b16 a[218:219], v233 offset:20480\n"
"buffer_load_dwordx4 v212, s[64:67], s90 offen nt lds\n"
"v_mfma_f32_16x16x32_bf16 v[116:119], a[232:235], a[168:171], v[116:119]\n"
"s_add_u32 s90, s43, 30720\n"
"v_exp_f32 v30, v30\n"
"v_mfma_f32_16x16x32_bf16 v[120:123], a[232:235], a[184:187], v[120:123]\n"
"s_add_u32 m0, s84, -13312\n"
"ds_read_b64_tr_b16 a[220:221], v233 offset:24576\n"
"v_cvt_pk_bf16_f32 v142, v28, v29\n"
"v_mfma_f32_16x16x32_bf16 v[120:123], a[236:239], a[188:191], v[120:123]\n"
"v_exp_f32 v31, v31\n"
"v_mfma_f32_16x16x32_bf16 v[124:127], a[236:239], a[204:207], v[124:127]\n"
"ds_read_b64_tr_b16 a[222:223], v233 offset:28672\n"
"v_cvt_pk_bf16_f32 v143, v30, v31\n"
"v_mfma_f32_16x16x32_bf16 v[124:127], a[232:235], a[200:203], v[124:127]\n"
"s_waitcnt lgkmcnt(4)\n"
"v_exp_f32 v32, v32\n"
"v_mfma_f32_16x16x32_bf16 a[0:3], a[208:211], v[160:163], a[0:3]\n"
"ds_read_b64_tr_b16 a[224:225], v234 offset:16384\n"
"v_mfma_f32_16x16x32_bf16 a[0:3], a[212:215], v[176:179], a[0:3]\n"
"v_exp_f32 v33, v33\n"
"v_mfma_f32_16x16x32_bf16 a[4:7], a[212:215], v[180:183], a[4:7]\n"
"ds_read_b64_tr_b16 a[226:227], v234 offset:20480\n"
"buffer_load_dwordx4 v212, s[64:67], s90 offen nt lds\n"
"v_mfma_f32_16x16x32_bf16 a[4:7], a[208:211], v[164:167], a[4:7]\n"
"v_exp_f32 v34, v34\n"
"v_mfma_f32_16x16x32_bf16 a[8:11], a[208:211], v[168:171], a[8:11]\n"
"ds_read_b64_tr_b16 a[228:229], v234 offset:24576\n"
"v_cvt_pk_bf16_f32 v144, v32, v33\n"
"v_mfma_f32_16x16x32_bf16 a[8:11], a[212:215], v[184:187], a[8:11]\n"
"v_exp_f32 v35, v35\n"
"v_mfma_f32_16x16x32_bf16 a[12:15], a[212:215], v[188:191], a[12:15]\n"
"ds_read_b64_tr_b16 a[230:231], v234 offset:28672\n"
"v_cvt_pk_bf16_f32 v145, v34, v35\n"
"v_mfma_f32_16x16x32_bf16 a[12:15], a[208:211], v[172:175], a[12:15]\n"
"s_waitcnt lgkmcnt(4)\n"
"v_exp_f32 v48, v48\n"
"v_mfma_f32_16x16x32_bf16 a[16:19], a[216:219], v[160:163], a[16:19]\n"
"ds_read_b64_tr_b16 a[232:233], v235 offset:16384\n"
"v_mfma_f32_16x16x32_bf16 a[16:19], a[220:223], v[176:179], a[16:19]\n"
"v_exp_f32 v49, v49\n"
"v_mfma_f32_16x16x32_bf16 a[20:23], a[220:223], v[180:183], a[20:23]\n"
"ds_read_b64_tr_b16 a[234:235], v235 offset:20480\n"
"v_cvt_pk_bf16_f32 v146, v48, v49\n"
"v_mfma_f32_16x16x32_bf16 a[20:23], a[216:219], v[164:167], a[20:23]\n"
"v_exp_f32 v50, v50\n"
"v_mfma_f32_16x16x32_bf16 a[24:27], a[216:219], v[168:171], a[24:27]\n"
"ds_read_b64_tr_b16 a[236:237], v235 offset:24576\n"
"v_mfma_f32_16x16x32_bf16 a[24:27], a[220:223], v[184:187], a[24:27]\n"
"v_exp_f32 v51, v51\n"
"v_mfma_f32_16x16x32_bf16 a[28:31], a[220:223], v[188:191], a[28:31]\n"
"ds_read_b64_tr_b16 a[238:239], v235 offset:28672\n"
"v_cvt_pk_bf16_f32 v147, v50, v51\n"
"v_mfma_f32_16x16x32_bf16 a[28:31], a[216:219], v[172:175], a[28:31]\n"
"s_waitcnt lgkmcnt(4)\n"
"v_exp_f32 v36, v36\n"
"v_mfma_f32_16x16x32_bf16 a[32:35], a[224:227], v[160:163], a[32:35]\n"
"ds_read_b64_tr_b16 a[208:209], v232 offset:16896\n"
"v_mfma_f32_16x16x32_bf16 a[32:35], a[228:231], v[176:179], a[32:35]\n"
"v_exp_f32 v37, v37\n"
"v_mfma_f32_16x16x32_bf16 a[36:39], a[228:231], v[180:183], a[36:39]\n"
"ds_read_b64_tr_b16 a[210:211], v232 offset:20992\n"
"v_cvt_pk_bf16_f32 v148, v36, v37\n"
"v_mfma_f32_16x16x32_bf16 a[36:39], a[224:227], v[164:167], a[36:39]\n"
"v_exp_f32 v38, v38\n"
"v_mfma_f32_16x16x32_bf16 a[40:43], a[224:227], v[168:171], a[40:43]\n"
"ds_read_b64_tr_b16 a[212:213], v232 offset:25088\n"
"v_mfma_f32_16x16x32_bf16 a[40:43], a[228:231], v[184:187], a[40:43]\n"
"v_exp_f32 v39, v39\n"
"v_mfma_f32_16x16x32_bf16 a[44:47], a[228:231], v[188:191], a[44:47]\n"
"ds_read_b64_tr_b16 a[214:215], v232 offset:29184\n"
"v_cvt_pk_bf16_f32 v149, v38, v39\n"
"v_mfma_f32_16x16x32_bf16 a[44:47], a[224:227], v[172:175], a[44:47]\n"
"s_waitcnt lgkmcnt(4)\n"
"v_exp_f32 v52, v52\n"
"v_mfma_f32_16x16x32_bf16 a[48:51], a[232:235], v[160:163], a[48:51]\n"
"ds_read_b64_tr_b16 a[216:217], v233 offset:16896\n"
"v_mfma_f32_16x16x32_bf16 a[48:51], a[236:239], v[176:179], a[48:51]\n"
"v_exp_f32 v53, v53\n"
"v_mfma_f32_16x16x32_bf16 a[52:55], a[236:239], v[180:183], a[52:55]\n"
"ds_read_b64_tr_b16 a[218:219], v233 offset:20992\n"
"v_cvt_pk_bf16_f32 v150, v52, v53\n"
"v_mfma_f32_16x16x32_bf16 a[52:55], a[232:235], v[164:167], a[52:55]\n"
"v_exp_f32 v54, v54\n"
"v_mfma_f32_16x16x32_bf16 a[56:59], a[232:235], v[168:171], a[56:59]\n"
"ds_read_b64_tr_b16 a[220:221], v233 offset:25088\n"
"v_mfma_f32_16x16x32_bf16 a[56:59], a[236:239], v[184:187], a[56:59]\n"
"v_exp_f32 v55, v55\n"
"v_mfma_f32_16x16x32_bf16 a[60:63], a[236:239], v[188:191], a[60:63]\n"
"ds_read_b64_tr_b16 a[222:223], v233 offset:29184\n"
"v_cvt_pk_bf16_f32 v151, v54, v55\n"
"v_mfma_f32_16x16x32_bf16 a[60:63], a[232:235], v[172:175], a[60:63]\n"
"s_waitcnt lgkmcnt(4)\n"
"v_exp_f32 v40, v40\n"
"v_mfma_f32_16x16x32_bf16 a[64:67], a[208:211], v[160:163], a[64:67]\n"
"ds_read_b64_tr_b16 a[224:225], v234 offset:16896\n"
"v_mfma_f32_16x16x32_bf16 a[64:67], a[212:215], v[176:179], a[64:67]\n"
"ds_read_b64_tr_b16 a[226:227], v234 offset:20992\n"
"v_mfma_f32_16x16x32_bf16 a[68:71], a[212:215], v[180:183], a[68:71]\n"
"v_mfma_f32_16x16x32_bf16 a[68:71], a[208:211], v[164:167], a[68:71]\n"
"ds_read_b64_tr_b16 a[228:229], v234 offset:25088\n"
"v_mfma_f32_16x16x32_bf16 a[72:75], a[208:211], v[168:171], a[72:75]\n"
"ds_read_b64_tr_b16 a[230:231], v234 offset:29184\n"
"v_mfma_f32_16x16x32_bf16 a[72:75], a[212:215], v[184:187], a[72:75]\n"
"v_exp_f32 v41, v41\n"
"v_mfma_f32_16x16x32_bf16 a[76:79], a[212:215], v[188:191], a[76:79]\n"
"ds_read_b64_tr_b16 a[232:233], v235 offset:16896\n"
"v_cvt_pk_bf16_f32 v152, v40, v41\n"
"v_mfma_f32_16x16x32_bf16 a[76:79], a[208:211], v[172:175], a[76:79]\n"
"s_waitcnt lgkmcnt(5)\n"
"ds_read_b64_tr_b16 a[234:235], v235 offset:20992\n"
"v_mfma_f32_16x16x32_bf16 a[80:83], a[216:219], v[160:163], a[80:83]\n"
"v_mfma_f32_16x16x32_bf16 a[80:83], a[220:223], v[176:179], a[80:83]\n"
"ds_read_b64_tr_b16 a[236:237], v235 offset:25088\n"
"v_mfma_f32_16x16x32_bf16 a[84:87], a[220:223], v[180:183], a[84:87]\n"
"ds_read_b64_tr_b16 a[238:239], v235 offset:29184\n"
"v_mfma_f32_16x16x32_bf16 a[84:87], a[216:219], v[164:167], a[84:87]\n"
"v_exp_f32 v42, v42\n"
"v_mfma_f32_16x16x32_bf16 a[88:91], a[216:219], v[168:171], a[88:91]\n"
"v_mfma_f32_16x16x32_bf16 a[88:91], a[220:223], v[184:187], a[88:91]\n"
"v_exp_f32 v43, v43\n"
"v_mfma_f32_16x16x32_bf16 a[92:95], a[220:223], v[188:191], a[92:95]\n"
"v_cvt_pk_bf16_f32 v153, v42, v43\n"
"v_mfma_f32_16x16x32_bf16 a[92:95], a[216:219], v[172:175], a[92:95]\n"
"s_waitcnt lgkmcnt(0)\n"
"v_exp_f32 v56, v56\n"
"v_mfma_f32_16x16x32_bf16 a[96:99], a[224:227], v[160:163], a[96:99]\n"
"s_waitcnt vmcnt(20)\n"
"s_barrier\n"
"v_mfma_f32_16x16x32_bf16 a[96:99], a[228:231], v[176:179], a[96:99]\n"
"s_add_u32 m0, s84, 32768\n"
"v_exp_f32 v57, v57\n"
"v_mfma_f32_16x16x32_bf16 a[100:103], a[228:231], v[180:183], a[100:103]\n"
"ds_read_b128 a[208:211], v240 offset:16384\n"
"buffer_load_dwordx4 v236, s[24:27], s82 offen offset:0 lds\n"
"v_mfma_f32_16x16x32_bf16 a[100:103], a[224:227], v[164:167], a[100:103]\n"
"v_exp_f32 v58, v58\n"
"v_mfma_f32_16x16x32_bf16 a[104:107], a[224:227], v[168:171], a[104:107]\n"
"ds_read_b128 a[212:215], v241 offset:16384\n"
"v_cvt_pk_bf16_f32 v154, v56, v57\n"
"v_mfma_f32_16x16x32_bf16 a[104:107], a[228:231], v[184:187], a[104:107]\n"
"v_exp_f32 v59, v59\n"
"v_mfma_f32_16x16x32_bf16 a[108:111], a[228:231], v[188:191], a[108:111]\n"
"ds_read_b128 a[216:219], v242 offset:16384\n"
"v_cvt_pk_bf16_f32 v155, v58, v59\n"
"v_mfma_f32_16x16x32_bf16 a[108:111], a[224:227], v[172:175], a[108:111]\n"
"v_exp_f32 v44, v44\n"
"v_mfma_f32_16x16x32_bf16 a[112:115], a[232:235], v[160:163], a[112:115]\n"
"ds_read_b128 a[220:223], v243 offset:16384\n"
"v_mfma_f32_16x16x32_bf16 a[112:115], a[236:239], v[176:179], a[112:115]\n"
"v_exp_f32 v45, v45\n"
"v_mfma_f32_16x16x32_bf16 a[116:119], a[236:239], v[180:183], a[116:119]\n"
"buffer_load_dwordx4 v237, s[24:27], s82 offen offset:1024 lds\n"
"v_cvt_pk_bf16_f32 v156, v44, v45\n"
"v_mfma_f32_16x16x32_bf16 a[116:119], a[232:235], v[164:167], a[116:119]\n"
"v_exp_f32 v46, v46\n"
"v_mfma_f32_16x16x32_bf16 a[120:123], a[232:235], v[168:171], a[120:123]\n"
"v_mfma_f32_16x16x32_bf16 a[120:123], a[236:239], v[184:187], a[120:123]\n"
"v_exp_f32 v47, v47\n"
"v_mfma_f32_16x16x32_bf16 a[124:127], a[236:239], v[188:191], a[124:127]\n"
"v_cvt_pk_bf16_f32 v157, v46, v47\n"
"v_mfma_f32_16x16x32_bf16 a[124:127], a[232:235], v[172:175], a[124:127]\n"
"v_exp_f32 v60, v60\n"
"v_mfma_f32_16x16x32_bf16 a[128:131], v[208:211], v[160:163], a[128:131]\n"
"v_mfma_f32_16x16x32_bf16 a[128:131], v[208:211], v[176:179], a[128:131]\n"
"v_exp_f32 v61, v61\n"
"v_mfma_f32_16x16x32_bf16 a[132:135], v[208:211], v[164:167], a[132:135]\n"
"buffer_load_dwordx4 v238, s[24:27], s82 offen offset:2048 lds\n"
"v_cvt_pk_bf16_f32 v158, v60, v61\n"
"v_mfma_f32_16x16x32_bf16 a[132:135], v[208:211], v[180:183], a[132:135]\n"
"v_exp_f32 v62, v62\n"
"v_mfma_f32_16x16x32_bf16 a[136:139], v[208:211], v[168:171], a[136:139]\n"
"v_mfma_f32_16x16x32_bf16 a[136:139], v[208:211], v[184:187], a[136:139]\n"
"v_exp_f32 v63, v63\n"
"v_mfma_f32_16x16x32_bf16 a[140:143], v[208:211], v[172:175], a[140:143]\n"
"v_cvt_pk_bf16_f32 v159, v62, v63\n"
"v_mfma_f32_16x16x32_bf16 a[140:143], v[208:211], v[188:191], a[140:143]\n"
"s_waitcnt lgkmcnt(2)\n"
"v_mfma_f32_16x16x32_bf16 v[0:3], a[208:211], a[144:147], v[192:195]\n"
"ds_read_b128 a[224:227], v240 offset:20480\n"
"v_mfma_f32_16x16x32_bf16 v[0:3], a[212:215], a[148:151], v[0:3]\n"
"v_exp_f32 v64, v64\n"
"v_mfma_f32_16x16x32_bf16 v[4:7], a[212:215], a[164:167], v[196:199]\n"
"ds_read_b128 a[228:231], v241 offset:20480\n"
"buffer_load_dwordx4 v239, s[24:27], s82 offen offset:3072 lds\n"
"v_mfma_f32_16x16x32_bf16 v[4:7], a[208:211], a[160:163], v[4:7]\n"
"s_add_u32 s82, s82, 16384\n"
"v_exp_f32 v65, v65\n"
"v_mfma_f32_16x16x32_bf16 v[8:11], a[208:211], a[176:179], v[200:203]\n"
"s_waitcnt vmcnt(20)\n"
"ds_read_b128 a[232:235], v242 offset:20480\n"
"v_xor_b32 v213, 0, v217\n"
"v_mfma_f32_16x16x32_bf16 v[8:11], a[212:215], a[180:183], v[8:11]\n"
"s_and_b32 s82, s82, 0x1ffff\n"
"ds_read_b64_tr_b16 a[240:241], v213 offset:0\n"
"v_cvt_pk_bf16_f32 v160, v64, v65\n"
"v_mfma_f32_16x16x32_bf16 v[12:15], a[212:215], a[196:199], v[204:207]\n"
"s_add_u32 m0, s85, 16384\n"
"ds_read_b128 a[236:239], v243 offset:20480\n"
"v_mfma_f32_16x16x32_bf16 v[12:15], a[208:211], a[192:195], v[12:15]\n"
"s_waitcnt lgkmcnt(5)\n"
"ds_read_b64_tr_b16 a[242:243], v213 offset:2048\n"
"v_xor_b32 v213, 32, v217\n"
"v_mfma_f32_16x16x32_bf16 v[0:3], a[216:219], a[152:155], v[0:3]\n"
"ds_read_b64_tr_b16 a[244:245], v213 offset:0\n"
"v_mfma_f32_16x16x32_bf16 v[0:3], a[220:223], a[156:159], v[0:3]\n"
"ds_read_b64_tr_b16 a[246:247], v213 offset:2048\n"
"v_xor_b32 v213, 64, v217\n"
"v_mfma_f32_16x16x32_bf16 v[4:7], a[220:223], a[172:175], v[4:7]\n"
"ds_read_b64_tr_b16 a[248:249], v213 offset:0\n"
"buffer_load_dwordx4 v230, s[28:31], s83 offen offset:0 lds\n"
"v_mfma_f32_16x16x32_bf16 v[4:7], a[216:219], a[168:171], v[4:7]\n"
"ds_read_b64_tr_b16 a[250:251], v213 offset:2048\n"
"v_xor_b32 v213, 96, v217\n"
"v_mfma_f32_16x16x32_bf16 v[8:11], a[216:219], a[184:187], v[8:11]\n"
"ds_read_b64_tr_b16 a[252:253], v213 offset:0\n"
"v_mfma_f32_16x16x32_bf16 v[8:11], a[220:223], a[188:191], v[8:11]\n"
"ds_read_b64_tr_b16 a[254:255], v213 offset:2048\n"
"v_mfma_f32_16x16x32_bf16 v[12:15], a[220:223], a[204:207], v[12:15]\n"
"v_exp_f32 v66, v66\n"
"v_mfma_f32_16x16x32_bf16 v[12:15], a[216:219], a[200:203], v[12:15]\n"
"s_waitcnt lgkmcnt(10)\n"
"v_exp_f32 v67, v67\n"
"v_mfma_f32_16x16x32_bf16 v[16:19], a[224:227], a[144:147], v[192:195]\n"
"ds_read_b128 a[208:211], v240 offset:24576\n"
"v_cvt_pk_bf16_f32 v161, v66, v67\n"
"v_mfma_f32_16x16x32_bf16 v[16:19], a[228:231], a[148:151], v[16:19]\n"
"v_exp_f32 v80, v80\n"
"v_mfma_f32_16x16x32_bf16 v[20:23], a[228:231], a[164:167], v[196:199]\n"
"ds_read_b128 a[212:215], v241 offset:24576\n"
"buffer_load_dwordx4 v231, s[28:31], s83 offen offset:1024 lds\n"
"v_mfma_f32_16x16x32_bf16 v[20:23], a[224:227], a[160:163], v[20:23]\n"
"s_waitcnt lgkmcnt(2)\n"
"v_exp_f32 v81, v81\n"
"v_mfma_f32_16x16x32_bf16 v[24:27], a[224:227], a[176:179], v[200:203]\n"
"ds_read_b128 a[216:219], v242 offset:24576\n"
"v_cvt_pk_bf16_f32 v162, v80, v81\n"
"v_mfma_f32_16x16x32_bf16 v[24:27], a[228:231], a[180:183], v[24:27]\n"
"v_exp_f32 v82, v82\n"
"v_mfma_f32_16x16x32_bf16 v[28:31], a[228:231], a[196:199], v[204:207]\n"
"ds_read_b128 a[220:223], v243 offset:24576\n"
"v_mfma_f32_16x16x32_bf16 v[28:31], a[224:227], a[192:195], v[28:31]\n"
"s_nop 0\n"
"v_exp_f32 v83, v83\n"
"v_mfma_f32_16x16x32_bf16 v[16:19], a[232:235], a[152:155], v[16:19]\n"
"v_cvt_pk_bf16_f32 v163, v82, v83\n"
"v_mfma_f32_16x16x32_bf16 v[16:19], a[236:239], a[156:159], v[16:19]\n"
"v_exp_f32 v68, v68\n"
"v_mfma_f32_16x16x32_bf16 v[20:23], a[236:239], a[172:175], v[20:23]\n"
"buffer_load_dwordx4 v230, s[28:31], s83 offen offset:2048 lds\n"
"v_mfma_f32_16x16x32_bf16 v[20:23], a[232:235], a[168:171], v[20:23]\n"
"v_exp_f32 v69, v69\n"
"v_mfma_f32_16x16x32_bf16 v[24:27], a[232:235], a[184:187], v[24:27]\n"
"v_exp_f32 v70, v70\n"
"v_mfma_f32_16x16x32_bf16 v[24:27], a[236:239], a[188:191], v[24:27]\n"
"v_exp_f32 v71, v71\n"
"v_mfma_f32_16x16x32_bf16 v[28:31], a[236:239], a[204:207], v[28:31]\n"
"v_cvt_pk_bf16_f32 v164, v68, v69\n"
"v_cvt_pk_bf16_f32 v165, v70, v71\n"
"v_mfma_f32_16x16x32_bf16 v[28:31], a[232:235], a[200:203], v[28:31]\n"
"s_waitcnt lgkmcnt(2)\n"
"v_exp_f32 v84, v84\n"
"v_mfma_f32_16x16x32_bf16 v[32:35], a[208:211], a[144:147], v[192:195]\n"
"ds_read_b128 a[224:227], v240 offset:28672\n"
"v_mfma_f32_16x16x32_bf16 v[32:35], a[212:215], a[148:151], v[32:35]\n"
"v_exp_f32 v85, v85\n"
"v_mfma_f32_16x16x32_bf16 v[36:39], a[212:215], a[164:167], v[196:199]\n"
"ds_read_b128 a[228:231], v241 offset:28672\n"
"buffer_load_dwordx4 v231, s[28:31], s83 offen offset:3072 lds\n"
"v_mfma_f32_16x16x32_bf16 v[36:39], a[208:211], a[160:163], v[36:39]\n"
"s_add_u32 s83, s83, 16384\n"
"v_exp_f32 v86, v86\n"
"v_mfma_f32_16x16x32_bf16 v[40:43], a[208:211], a[176:179], v[200:203]\n"
"s_and_b32 s83, s83, 0x1ffff\n"
"ds_read_b128 a[232:235], v242 offset:28672\n"
"buffer_store_dwordx4 a[240:243], v255, s[36:39], s40 offen offset:0 sc1\n"
"v_mfma_f32_16x16x32_bf16 v[40:43], a[212:215], a[180:183], v[40:43]\n"
"s_add_u32 m0, s81, 16384\n"
"v_exp_f32 v87, v87\n"
"v_mfma_f32_16x16x32_bf16 v[44:47], a[212:215], a[196:199], v[204:207]\n"
"ds_read_b128 a[236:239], v243 offset:28672\n"
"v_cvt_pk_bf16_f32 v166, v84, v85\n"
"v_mfma_f32_16x16x32_bf16 v[44:47], a[208:211], a[192:195], v[44:47]\n"
"s_waitcnt lgkmcnt(4)\n"
"v_exp_f32 v72, v72\n"
"v_mfma_f32_16x16x32_bf16 v[32:35], a[216:219], a[152:155], v[32:35]\n"
"v_cvt_pk_bf16_f32 v167, v86, v87\n"
"v_mfma_f32_16x16x32_bf16 v[32:35], a[220:223], a[156:159], v[32:35]\n"
"v_exp_f32 v73, v73\n"
"v_mfma_f32_16x16x32_bf16 v[36:39], a[220:223], a[172:175], v[36:39]\n"
"buffer_load_dwordx4 v252, s[16:19], s80 offen lds\n"
"v_cvt_pk_bf16_f32 v168, v72, v73\n"
"v_mfma_f32_16x16x32_bf16 v[36:39], a[216:219], a[168:171], v[36:39]\n"
"s_add_u32 m0, s81, 17408\n"
"v_exp_f32 v74, v74\n"
"v_mfma_f32_16x16x32_bf16 v[40:43], a[216:219], a[184:187], v[40:43]\n"
"v_exp_f32 v75, v75\n"
"v_mfma_f32_16x16x32_bf16 v[40:43], a[220:223], a[188:191], v[40:43]\n"
"v_exp_f32 v88, v88\n"
"v_mfma_f32_16x16x32_bf16 v[44:47], a[220:223], a[204:207], v[44:47]\n"
"v_cvt_pk_bf16_f32 v169, v74, v75\n"
"v_mfma_f32_16x16x32_bf16 v[44:47], a[216:219], a[200:203], v[44:47]\n"
"s_waitcnt lgkmcnt(2)\n"
"v_exp_f32 v89, v89\n"
"v_mfma_f32_16x16x32_bf16 v[48:51], a[224:227], a[144:147], v[192:195]\n"
"ds_read_b64_tr_b16 a[208:209], v244 offset:32768\n"
"v_cvt_pk_bf16_f32 v170, v88, v89\n"
"v_mfma_f32_16x16x32_bf16 v[48:51], a[228:231], a[148:151], v[48:51]\n"
"v_exp_f32 v90, v90\n"
"v_mfma_f32_16x16x32_bf16 v[52:55], a[228:231], a[164:167], v[196:199]\n"
"ds_read_b64_tr_b16 a[210:211], v244 offset:36864\n"
"buffer_load_dwordx4 v253, s[16:19], s80 offen lds\n"
"v_mfma_f32_16x16x32_bf16 v[52:55], a[224:227], a[160:163], v[52:55]\n"
"s_add_u32 m0, s81, 24576\n"
"v_exp_f32 v91, v91\n"
"v_mfma_f32_16x16x32_bf16 v[56:59], a[224:227], a[176:179], v[200:203]\n"
"ds_read_b64_tr_b16 a[212:213], v244 offset:40960\n"
"buffer_store_dwordx4 a[244:247], v255, s[36:39], s40 offen offset:1024 sc1\n"
"v_mfma_f32_16x16x32_bf16 v[56:59], a[228:231], a[180:183], v[56:59]\n"
"v_exp_f32 v76, v76\n"
"v_mfma_f32_16x16x32_bf16 v[60:63], a[228:231], a[196:199], v[204:207]\n"
"ds_read_b64_tr_b16 a[214:215], v244 offset:45056\n"
"v_cvt_pk_bf16_f32 v171, v90, v91\n"
"v_mfma_f32_16x16x32_bf16 v[60:63], a[224:227], a[192:195], v[60:63]\n"
"s_waitcnt lgkmcnt(4)\n"
"v_exp_f32 v77, v77\n"
"v_mfma_f32_16x16x32_bf16 v[48:51], a[232:235], a[152:155], v[48:51]\n"
"ds_read_b64_tr_b16 a[216:217], v245 offset:32768\n"
"v_cvt_pk_bf16_f32 v172, v76, v77\n"
"v_mfma_f32_16x16x32_bf16 v[48:51], a[236:239], a[156:159], v[48:51]\n"
"v_exp_f32 v78, v78\n"
"v_mfma_f32_16x16x32_bf16 v[52:55], a[236:239], a[172:175], v[52:55]\n"
"ds_read_b64_tr_b16 a[218:219], v245 offset:36864\n"
"buffer_load_dwordx4 v252, s[20:23], s80 offen lds\n"
"v_mfma_f32_16x16x32_bf16 v[52:55], a[232:235], a[168:171], v[52:55]\n"
"s_add_u32 m0, s81, 25600\n"
"v_exp_f32 v79, v79\n"
"v_mfma_f32_16x16x32_bf16 v[56:59], a[232:235], a[184:187], v[56:59]\n"
"ds_read_b64_tr_b16 a[220:221], v245 offset:40960\n"
"v_cvt_pk_bf16_f32 v173, v78, v79\n"
"v_mfma_f32_16x16x32_bf16 v[56:59], a[236:239], a[188:191], v[56:59]\n"
"v_exp_f32 v92, v92\n"
"v_mfma_f32_16x16x32_bf16 v[60:63], a[236:239], a[204:207], v[60:63]\n"
"ds_read_b64_tr_b16 a[222:223], v245 offset:45056\n"
"v_mfma_f32_16x16x32_bf16 v[60:63], a[232:235], a[200:203], v[60:63]\n"
"s_waitcnt lgkmcnt(4)\n"
"v_exp_f32 v93, v93\n"
"v_mfma_f32_16x16x32_bf16 a[0:3], a[208:211], v[128:131], a[0:3]\n"
"ds_read_b64_tr_b16 a[224:225], v246 offset:32768\n"
"v_cvt_pk_bf16_f32 v174, v92, v93\n"
"v_mfma_f32_16x16x32_bf16 a[0:3], a[212:215], v[144:147], a[0:3]\n"
"v_exp_f32 v94, v94\n"
"v_mfma_f32_16x16x32_bf16 a[4:7], a[212:215], v[148:151], a[4:7]\n"
"ds_read_b64_tr_b16 a[226:227], v246 offset:36864\n"
"buffer_load_dwordx4 v253, s[20:23], s80 offen lds\n"
"v_mfma_f32_16x16x32_bf16 a[4:7], a[208:211], v[132:135], a[4:7]\n"
"s_add_u32 s80, s80, 221184\n"
"v_exp_f32 v95, v95\n"
".L_pad6:\n"
"s_cbranch_execz .L_padret0\n"
"s_nop 0\n"
"v_mfma_f32_16x16x32_bf16 a[8:11], a[208:211], v[136:139], a[8:11]\n"
"s_sub_u32 s86, s80, 2211840\n"
"ds_read_b64_tr_b16 a[228:229], v246 offset:40960\n"
"buffer_store_dwordx4 a[248:251], v255, s[36:39], s40 offen offset:2048 sc1\n"
"v_mfma_f32_16x16x32_bf16 a[8:11], a[212:215], v[152:155], a[8:11]\n"
"s_min_u32 s80, s80, s86\n"
"v_exp_f32 v96, v96\n"
"v_mfma_f32_16x16x32_bf16 a[12:15], a[212:215], v[156:159], a[12:15]\n"
"ds_read_b64_tr_b16 a[230:231], v246 offset:45056\n"
"v_cvt_pk_bf16_f32 v175, v94, v95\n"
"v_mfma_f32_16x16x32_bf16 a[12:15], a[208:211], v[140:143], a[12:15]\n"
"s_waitcnt lgkmcnt(4)\n"
"v_exp_f32 v97, v97\n"
"v_mfma_f32_16x16x32_bf16 a[16:19], a[216:219], v[128:131], a[16:19]\n"
"ds_read_b64_tr_b16 a[232:233], v247 offset:32768\n"
"v_cvt_pk_bf16_f32 v176, v96, v97\n"
"v_mfma_f32_16x16x32_bf16 a[16:19], a[220:223], v[144:147], a[16:19]\n"
"v_exp_f32 v98, v98\n"
"v_mfma_f32_16x16x32_bf16 a[20:23], a[220:223], v[148:151], a[20:23]\n"
"ds_read_b64_tr_b16 a[234:235], v247 offset:36864\n"
"v_mfma_f32_16x16x32_bf16 a[20:23], a[216:219], v[132:135], a[20:23]\n"
"v_exp_f32 v99, v99\n"
"v_mfma_f32_16x16x32_bf16 a[24:27], a[216:219], v[136:139], a[24:27]\n"
"ds_read_b64_tr_b16 a[236:237], v247 offset:40960\n"
"v_cvt_pk_bf16_f32 v177, v98, v99\n"
"v_mfma_f32_16x16x32_bf16 a[24:27], a[220:223], v[152:155], a[24:27]\n"
"v_exp_f32 v112, v112\n"
"v_mfma_f32_16x16x32_bf16 a[28:31], a[220:223], v[156:159], a[28:31]\n"
"ds_read_b64_tr_b16 a[238:239], v247 offset:45056\n"
"v_mfma_f32_16x16x32_bf16 a[28:31], a[216:219], v[140:143], a[28:31]\n"
"s_waitcnt lgkmcnt(4)\n"
"v_exp_f32 v113, v113\n"
"v_mfma_f32_16x16x32_bf16 a[32:35], a[224:227], v[128:131], a[32:35]\n"
"ds_read_b64_tr_b16 a[208:209], v244 offset:33280\n"
"v_cvt_pk_bf16_f32 v178, v112, v113\n"
"v_mfma_f32_16x16x32_bf16 a[32:35], a[228:231], v[144:147], a[32:35]\n"
"v_exp_f32 v114, v114\n"
"v_mfma_f32_16x16x32_bf16 a[36:39], a[228:231], v[148:151], a[36:39]\n"
"ds_read_b64_tr_b16 a[210:211], v244 offset:37376\n"
"v_mfma_f32_16x16x32_bf16 a[36:39], a[224:227], v[132:135], a[36:39]\n"
"v_exp_f32 v115, v115\n"
"v_mfma_f32_16x16x32_bf16 a[40:43], a[224:227], v[136:139], a[40:43]\n"
"ds_read_b64_tr_b16 a[212:213], v244 offset:41472\n"
"buffer_store_dwordx4 a[252:255], v255, s[36:39], s40 offen offset:3072 sc1\n"
"v_mfma_f32_16x16x32_bf16 a[40:43], a[228:231], v[152:155], a[40:43]\n"
"v_exp_f32 v100, v100\n"
"v_mfma_f32_16x16x32_bf16 a[44:47], a[228:231], v[156:159], a[44:47]\n"
"ds_read_b64_tr_b16 a[214:215], v244 offset:45568\n"
"v_cvt_pk_bf16_f32 v179, v114, v115\n"
"v_mfma_f32_16x16x32_bf16 a[44:47], a[224:227], v[140:143], a[44:47]\n"
"s_waitcnt lgkmcnt(4)\n"
"v_exp_f32 v101, v101\n"
"v_mfma_f32_16x16x32_bf16 a[48:51], a[232:235], v[128:131], a[48:51]\n"
"ds_read_b64_tr_b16 a[216:217], v245 offset:33280\n"
"v_cvt_pk_bf16_f32 v180, v100, v101\n"
"v_mfma_f32_16x16x32_bf16 a[48:51], a[236:239], v[144:147], a[48:51]\n"
"v_exp_f32 v102, v102\n"
"v_mfma_f32_16x16x32_bf16 a[52:55], a[236:239], v[148:151], a[52:55]\n"
"ds_read_b64_tr_b16 a[218:219], v245 offset:37376\n"
"v_mfma_f32_16x16x32_bf16 a[52:55], a[232:235], v[132:135], a[52:55]\n"
"v_exp_f32 v103, v103\n"
"v_mfma_f32_16x16x32_bf16 a[56:59], a[232:235], v[136:139], a[56:59]\n"
"ds_read_b64_tr_b16 a[220:221], v245 offset:41472\n"
"v_cvt_pk_bf16_f32 v181, v102, v103\n"
"v_mfma_f32_16x16x32_bf16 a[56:59], a[236:239], v[152:155], a[56:59]\n"
"v_exp_f32 v116, v116\n"
"v_mfma_f32_16x16x32_bf16 a[60:63], a[236:239], v[156:159], a[60:63]\n"
"ds_read_b64_tr_b16 a[222:223], v245 offset:45568\n"
"v_mfma_f32_16x16x32_bf16 a[60:63], a[232:235], v[140:143], a[60:63]\n"
"s_waitcnt lgkmcnt(4)\n"
"v_exp_f32 v117, v117\n"
"v_mfma_f32_16x16x32_bf16 a[64:67], a[208:211], v[128:131], a[64:67]\n"
"ds_read_b64_tr_b16 a[224:225], v246 offset:33280\n"
"v_cvt_pk_bf16_f32 v182, v116, v117\n"
"v_mfma_f32_16x16x32_bf16 a[64:67], a[212:215], v[144:147], a[64:67]\n"
"ds_read_b64_tr_b16 a[226:227], v246 offset:37376\n"
"v_mfma_f32_16x16x32_bf16 a[68:71], a[212:215], v[148:151], a[68:71]\n"
"v_exp_f32 v118, v118\n"
"v_mfma_f32_16x16x32_bf16 a[68:71], a[208:211], v[132:135], a[68:71]\n"
"ds_read_b64_tr_b16 a[228:229], v246 offset:41472\n"
"v_mfma_f32_16x16x32_bf16 a[72:75], a[208:211], v[136:139], a[72:75]\n"
"ds_read_b64_tr_b16 a[230:231], v246 offset:45568\n"
"v_mfma_f32_16x16x32_bf16 a[72:75], a[212:215], v[152:155], a[72:75]\n"
"v_exp_f32 v119, v119\n"
"v_mfma_f32_16x16x32_bf16 a[76:79], a[212:215], v[156:159], a[76:79]\n"
"ds_read_b64_tr_b16 a[232:233], v247 offset:33280\n"
"v_cvt_pk_bf16_f32 v183, v118, v119\n"
"v_mfma_f32_16x16x32_bf16 a[76:79], a[208:211], v[140:143], a[76:79]\n"
"s_waitcnt lgkmcnt(5)\n"
"ds_read_b64_tr_b16 a[234:235], v247 offset:37376\n"
"v_mfma_f32_16x16x32_bf16 a[80:83], a[216:219], v[128:131], a[80:83]\n"
"v_mfma_f32_16x16x32_bf16 a[80:83], a[220:223], v[144:147], a[80:83]\n"
"ds_read_b64_tr_b16 a[236:237], v247 offset:41472\n"
"v_mfma_f32_16x16x32_bf16 a[84:87], a[220:223], v[148:151], a[84:87]\n"
"ds_read_b64_tr_b16 a[238:239], v247 offset:45568\n"
"v_mfma_f32_16x16x32_bf16 a[84:87], a[216:219], v[132:135], a[84:87]\n"
"v_exp_f32 v104, v104\n"
"v_mfma_f32_16x16x32_bf16 a[88:91], a[216:219], v[136:139], a[88:91]\n"
"v_mfma_f32_16x16x32_bf16 a[88:91], a[220:223], v[152:155], a[88:91]\n"
"v_exp_f32 v105, v105\n"
"v_mfma_f32_16x16x32_bf16 a[92:95], a[220:223], v[156:159], a[92:95]\n"
"v_exp_f32 v106, v106\n"
"v_mfma_f32_16x16x32_bf16 a[92:95], a[216:219], v[140:143], a[92:95]\n"
"s_waitcnt lgkmcnt(0)\n"
"v_exp_f32 v107, v107\n"
"v_mfma_f32_16x16x32_bf16 a[96:99], a[224:227], v[128:131], a[96:99]\n"
"s_waitcnt vmcnt(20)\n"
"s_barrier\n"
"v_cvt_pk_bf16_f32 v184, v104, v105\n"
"v_mfma_f32_16x16x32_bf16 a[96:99], a[228:231], v[144:147], a[96:99]\n"
"s_add_u32 m0, s84, 16384\n"
"v_exp_f32 v120, v120\n"
"v_mfma_f32_16x16x32_bf16 a[100:103], a[228:231], v[148:151], a[100:103]\n"
"ds_read_b128 a[208:211], v240 offset:0\n"
"buffer_load_dwordx4 v236, s[24:27], s82 offen offset:0 lds\n"
"v_mfma_f32_16x16x32_bf16 a[100:103], a[224:227], v[132:135], a[100:103]\n"
"v_exp_f32 v121, v121\n"
"v_mfma_f32_16x16x32_bf16 a[104:107], a[224:227], v[136:139], a[104:107]\n"
"ds_read_b128 a[212:215], v241 offset:0\n"
"v_cvt_pk_bf16_f32 v185, v106, v107\n"
"v_mfma_f32_16x16x32_bf16 a[104:107], a[228:231], v[152:155], a[104:107]\n"
"v_exp_f32 v122, v122\n"
"v_mfma_f32_16x16x32_bf16 a[108:111], a[228:231], v[156:159], a[108:111]\n"
"ds_read_b128 a[216:219], v242 offset:0\n"
"v_cvt_pk_bf16_f32 v186, v120, v121\n"
"v_mfma_f32_16x16x32_bf16 a[108:111], a[224:227], v[140:143], a[108:111]\n"
"v_exp_f32 v123, v123\n"
"v_mfma_f32_16x16x32_bf16 a[112:115], a[232:235], v[128:131], a[112:115]\n"
"ds_read_b128 a[220:223], v243 offset:0\n"
"v_cvt_pk_bf16_f32 v187, v122, v123\n"
"v_mfma_f32_16x16x32_bf16 a[112:115], a[236:239], v[144:147], a[112:115]\n"
"v_exp_f32 v108, v108\n"
"v_mfma_f32_16x16x32_bf16 a[116:119], a[236:239], v[148:151], a[116:119]\n"
"buffer_load_dwordx4 v237, s[24:27], s82 offen offset:1024 lds\n"
"v_mfma_f32_16x16x32_bf16 a[116:119], a[232:235], v[132:135], a[116:119]\n"
"v_exp_f32 v109, v109\n"
"v_mfma_f32_16x16x32_bf16 a[120:123], a[232:235], v[136:139], a[120:123]\n"
"v_cvt_pk_bf16_f32 v188, v108, v109\n"
"v_mfma_f32_16x16x32_bf16 a[120:123], a[236:239], v[152:155], a[120:123]\n"
"v_exp_f32 v110, v110\n"
"v_mfma_f32_16x16x32_bf16 a[124:127], a[236:239], v[156:159], a[124:127]\n"
"v_exp_f32 v111, v111\n"
"v_mfma_f32_16x16x32_bf16 a[124:127], a[232:235], v[140:143], a[124:127]\n"
"v_exp_f32 v124, v124\n"
"v_mfma_f32_16x16x32_bf16 a[128:131], v[208:211], v[128:131], a[128:131]\n"
"v_cvt_pk_bf16_f32 v189, v110, v111\n"
"v_mfma_f32_16x16x32_bf16 a[128:131], v[208:211], v[144:147], a[128:131]\n"
"v_exp_f32 v125, v125\n"
"v_mfma_f32_16x16x32_bf16 a[132:135], v[208:211], v[132:135], a[132:135]\n"
"buffer_load_dwordx4 v238, s[24:27], s82 offen offset:2048 lds\n"
"v_cvt_pk_bf16_f32 v190, v124, v125\n"
"v_mfma_f32_16x16x32_bf16 a[132:135], v[208:211], v[148:151], a[132:135]\n"
"v_exp_f32 v126, v126\n"
"v_mfma_f32_16x16x32_bf16 a[136:139], v[208:211], v[136:139], a[136:139]\n"
"v_mfma_f32_16x16x32_bf16 a[136:139], v[208:211], v[152:155], a[136:139]\n"
"v_exp_f32 v127, v127\n"
"v_mfma_f32_16x16x32_bf16 a[140:143], v[208:211], v[140:143], a[140:143]\n"
"v_cvt_pk_bf16_f32 v191, v126, v127\n"
"v_mfma_f32_16x16x32_bf16 a[140:143], v[208:211], v[156:159], a[140:143]\n"
"s_waitcnt lgkmcnt(2)\n"
"v_mfma_f32_16x16x32_bf16 v[64:67], a[208:211], a[144:147], v[192:195]\n"
"ds_read_b128 a[224:227], v240 offset:4096\n"
"v_mfma_f32_16x16x32_bf16 v[64:67], a[212:215], a[148:151], v[64:67]\n"
"v_exp_f32 v0, v0\n"
"v_mfma_f32_16x16x32_bf16 v[68:71], a[212:215], a[164:167], v[196:199]\n"
"ds_read_b128 a[228:231], v241 offset:4096\n"
"buffer_load_dwordx4 v239, s[24:27], s82 offen offset:3072 lds\n"
"v_mfma_f32_16x16x32_bf16 v[68:71], a[208:211], a[160:163], v[68:71]\n"
"s_add_u32 s82, s82, 16384\n"
"v_exp_f32 v1, v1\n"
"v_mfma_f32_16x16x32_bf16 v[72:75], a[208:211], a[176:179], v[200:203]\n"
"s_waitcnt vmcnt(20)\n"
"ds_read_b128 a[232:235], v242 offset:4096\n"
"v_xor_b32 v213, 0, v217\n"
"v_mfma_f32_16x16x32_bf16 v[72:75], a[212:215], a[180:183], v[72:75]\n"
"s_and_b32 s82, s82, 0x1ffff\n"
"ds_read_b64_tr_b16 a[240:241], v213 offset:16384\n"
"v_cvt_pk_bf16_f32 v128, v0, v1\n"
"v_mfma_f32_16x16x32_bf16 v[76:79], a[212:215], a[196:199], v[204:207]\n"
"ds_read_b128 a[236:239], v243 offset:4096\n"
"buffer_load_dword v220, v221, s[36:39], s95 offen sc1\n"
"v_mfma_f32_16x16x32_bf16 v[76:79], a[208:211], a[192:195], v[76:79]\n"
"s_waitcnt lgkmcnt(5)\n"
"ds_read_b64_tr_b16 a[242:243], v213 offset:18432\n"
"v_xor_b32 v213, 32, v217\n"
"v_mfma_f32_16x16x32_bf16 v[64:67], a[216:219], a[152:155], v[64:67]\n"
"s_sub_u32 m0, s85, 98304\n"
"ds_read_b64_tr_b16 a[244:245], v213 offset:16384\n"
"v_mfma_f32_16x16x32_bf16 v[64:67], a[220:223], a[156:159], v[64:67]\n"
"ds_read_b64_tr_b16 a[246:247], v213 offset:18432\n"
"v_xor_b32 v213, 64, v217\n"
"v_mfma_f32_16x16x32_bf16 v[68:71], a[220:223], a[172:175], v[68:71]\n"
"ds_read_b64_tr_b16 a[248:249], v213 offset:16384\n"
"buffer_load_dwordx4 v230, s[28:31], s83 offen offset:0 lds\n"
"v_mfma_f32_16x16x32_bf16 v[68:71], a[216:219], a[168:171], v[68:71]\n"
"ds_read_b64_tr_b16 a[250:251], v213 offset:18432\n"
"v_xor_b32 v213, 96, v217\n"
"v_mfma_f32_16x16x32_bf16 v[72:75], a[216:219], a[184:187], v[72:75]\n"
"ds_read_b64_tr_b16 a[252:253], v213 offset:16384\n"
"v_mfma_f32_16x16x32_bf16 v[72:75], a[220:223], a[188:191], v[72:75]\n"
"ds_read_b64_tr_b16 a[254:255], v213 offset:18432\n"
"v_mfma_f32_16x16x32_bf16 v[76:79], a[220:223], a[204:207], v[76:79]\n"
"v_exp_f32 v2, v2\n"
"v_mfma_f32_16x16x32_bf16 v[76:79], a[216:219], a[200:203], v[76:79]\n"
"s_waitcnt lgkmcnt(10)\n"
"v_exp_f32 v3, v3\n"
"v_mfma_f32_16x16x32_bf16 v[80:83], a[224:227], a[144:147], v[192:195]\n"
"ds_read_b128 a[208:211], v240 offset:8192\n"
"v_cvt_pk_bf16_f32 v129, v2, v3\n"
"v_mfma_f32_16x16x32_bf16 v[80:83], a[228:231], a[148:151], v[80:83]\n"
"v_exp_f32 v16, v16\n"
"v_mfma_f32_16x16x32_bf16 v[84:87], a[228:231], a[164:167], v[196:199]\n"
"ds_read_b128 a[212:215], v241 offset:8192\n"
"buffer_load_dwordx4 v231, s[28:31], s83 offen offset:1024 lds\n"
"v_mfma_f32_16x16x32_bf16 v[84:87], a[224:227], a[160:163], v[84:87]\n"
"s_waitcnt lgkmcnt(2)\n"
"v_exp_f32 v17, v17\n"
"v_mfma_f32_16x16x32_bf16 v[88:91], a[224:227], a[176:179], v[200:203]\n"
"ds_read_b128 a[216:219], v242 offset:8192\n"
"v_cvt_pk_bf16_f32 v130, v16, v17\n"
"v_mfma_f32_16x16x32_bf16 v[88:91], a[228:231], a[180:183], v[88:91]\n"
"v_exp_f32 v18, v18\n"
"v_mfma_f32_16x16x32_bf16 v[92:95], a[228:231], a[196:199], v[204:207]\n"
"ds_read_b128 a[220:223], v243 offset:8192\n"
"v_mfma_f32_16x16x32_bf16 v[92:95], a[224:227], a[192:195], v[92:95]\n"
"s_nop 0\n"
"v_exp_f32 v19, v19\n"
"v_mfma_f32_16x16x32_bf16 v[80:83], a[232:235], a[152:155], v[80:83]\n"
"v_exp_f32 v4, v4\n"
"v_mfma_f32_16x16x32_bf16 v[80:83], a[236:239], a[156:159], v[80:83]\n"
"v_exp_f32 v5, v5\n"
"v_mfma_f32_16x16x32_bf16 v[84:87], a[236:239], a[172:175], v[84:87]\n"
"buffer_load_dwordx4 v230, s[28:31], s83 offen offset:2048 lds\n"
"v_cvt_pk_bf16_f32 v131, v18, v19\n"
"v_mfma_f32_16x16x32_bf16 v[84:87], a[232:235], a[168:171], v[84:87]\n"
"v_exp_f32 v6, v6\n"
"v_mfma_f32_16x16x32_bf16 v[88:91], a[232:235], a[184:187], v[88:91]\n"
"v_exp_f32 v7, v7\n"
"v_mfma_f32_16x16x32_bf16 v[88:91], a[236:239], a[188:191], v[88:91]\n"
"v_exp_f32 v20, v20\n"
"v_mfma_f32_16x16x32_bf16 v[92:95], a[236:239], a[204:207], v[92:95]\n"
"v_exp_f32 v21, v21\n"
"v_mfma_f32_16x16x32_bf16 v[92:95], a[232:235], a[200:203], v[92:95]\n"
"s_waitcnt lgkmcnt(2)\n"
"v_exp_f32 v22, v22\n"
"v_mfma_f32_16x16x32_bf16 v[96:99], a[208:211], a[144:147], v[192:195]\n"
"ds_read_b128 a[224:227], v240 offset:12288\n"
"v_cvt_pk_bf16_f32 v132, v4, v5\n"
"v_mfma_f32_16x16x32_bf16 v[96:99], a[212:215], a[148:151], v[96:99]\n"
"v_exp_f32 v23, v23\n"
"v_mfma_f32_16x16x32_bf16 v[100:103], a[212:215], a[164:167], v[196:199]\n"
"ds_read_b128 a[228:231], v241 offset:12288\n"
"buffer_load_dwordx4 v231, s[28:31], s83 offen offset:3072 lds\n"
"v_mfma_f32_16x16x32_bf16 v[100:103], a[208:211], a[160:163], v[100:103]\n"
"s_add_u32 s83, s83, 16384\n"
"v_exp_f32 v8, v8\n"
"v_mfma_f32_16x16x32_bf16 v[104:107], a[208:211], a[176:179], v[200:203]\n"
"s_and_b32 s83, s83, 0x1ffff\n"
"ds_read_b128 a[232:235], v242 offset:12288\n"
"buffer_store_dwordx4 a[240:243], v255, s[36:39], s41 offen offset:0 sc1\n"
"v_mfma_f32_16x16x32_bf16 v[104:107], a[212:215], a[180:183], v[104:107]\n"
"s_add_u32 m0, s81, 49152\n"
"v_exp_f32 v9, v9\n"
"v_mfma_f32_16x16x32_bf16 v[108:111], a[212:215], a[196:199], v[204:207]\n"
"ds_read_b128 a[236:239], v243 offset:12288\n"
"v_cvt_pk_bf16_f32 v133, v6, v7\n"
"v_mfma_f32_16x16x32_bf16 v[108:111], a[208:211], a[192:195], v[108:111]\n"
"s_waitcnt lgkmcnt(4)\n"
"v_exp_f32 v10, v10\n"
"v_mfma_f32_16x16x32_bf16 v[96:99], a[216:219], a[152:155], v[96:99]\n"
"v_exp_f32 v11, v11\n"
"v_mfma_f32_16x16x32_bf16 v[96:99], a[220:223], a[156:159], v[96:99]\n"
"v_exp_f32 v24, v24\n"
"v_mfma_f32_16x16x32_bf16 v[100:103], a[220:223], a[172:175], v[100:103]\n"
"buffer_load_dwordx4 v252, s[16:19], s80 offen lds\n"
"v_cvt_pk_bf16_f32 v134, v20, v21\n"
"v_mfma_f32_16x16x32_bf16 v[100:103], a[216:219], a[168:171], v[100:103]\n"
"s_add_u32 m0, s81, 50176\n"
"v_exp_f32 v25, v25\n"
"v_mfma_f32_16x16x32_bf16 v[104:107], a[216:219], a[184:187], v[104:107]\n"
"v_exp_f32 v26, v26\n"
"v_mfma_f32_16x16x32_bf16 v[104:107], a[220:223], a[188:191], v[104:107]\n"
"v_exp_f32 v27, v27\n"
"v_mfma_f32_16x16x32_bf16 v[108:111], a[220:223], a[204:207], v[108:111]\n"
"v_exp_f32 v12, v12\n"
"v_mfma_f32_16x16x32_bf16 v[108:111], a[216:219], a[200:203], v[108:111]\n"
"s_waitcnt lgkmcnt(2)\n"
"v_exp_f32 v13, v13\n"
"v_mfma_f32_16x16x32_bf16 v[112:115], a[224:227], a[144:147], v[192:195]\n"
"ds_read_b64_tr_b16 a[208:209], v232 offset:0\n"
"v_cvt_pk_bf16_f32 v135, v22, v23\n"
"v_mfma_f32_16x16x32_bf16 v[112:115], a[228:231], a[148:151], v[112:115]\n"
"v_exp_f32 v14, v14\n"
"v_mfma_f32_16x16x32_bf16 v[116:119], a[228:231], a[164:167], v[196:199]\n"
"ds_read_b64_tr_b16 a[210:211], v232 offset:4096\n"
"buffer_load_dwordx4 v253, s[16:19], s80 offen lds\n"
"v_mfma_f32_16x16x32_bf16 v[116:119], a[224:227], a[160:163], v[116:119]\n"
"s_add_u32 m0, s81, 57344\n"
"v_exp_f32 v15, v15\n"
"v_mfma_f32_16x16x32_bf16 v[120:123], a[224:227], a[176:179], v[200:203]\n"
"ds_read_b64_tr_b16 a[212:213], v232 offset:8192\n"
"buffer_store_dwordx4 a[244:247], v255, s[36:39], s41 offen offset:1024 sc1\n"
"v_mfma_f32_16x16x32_bf16 v[120:123], a[228:231], a[180:183], v[120:123]\n"
"v_exp_f32 v28, v28\n"
"v_mfma_f32_16x16x32_bf16 v[124:127], a[228:231], a[196:199], v[204:207]\n"
"ds_read_b64_tr_b16 a[214:215], v232 offset:12288\n"
"v_cvt_pk_bf16_f32 v136, v8, v9\n"
"v_mfma_f32_16x16x32_bf16 v[124:127], a[224:227], a[192:195], v[124:127]\n"
"s_waitcnt lgkmcnt(4)\n"
"v_exp_f32 v29, v29\n"
"v_mfma_f32_16x16x32_bf16 v[112:115], a[232:235], a[152:155], v[112:115]\n"
"ds_read_b64_tr_b16 a[216:217], v233 offset:0\n"
"v_cvt_pk_bf16_f32 v137, v10, v11\n"
"v_mfma_f32_16x16x32_bf16 v[112:115], a[236:239], a[156:159], v[112:115]\n"
"v_exp_f32 v30, v30\n"
"v_mfma_f32_16x16x32_bf16 v[116:119], a[236:239], a[172:175], v[116:119]\n"
"ds_read_b64_tr_b16 a[218:219], v233 offset:4096\n"
"buffer_load_dwordx4 v252, s[20:23], s80 offen lds\n"
"v_mfma_f32_16x16x32_bf16 v[116:119], a[232:235], a[168:171], v[116:119]\n"
"s_add_u32 m0, s81, 58368\n"
"v_exp_f32 v31, v31\n"
"v_mfma_f32_16x16x32_bf16 v[120:123], a[232:235], a[184:187], v[120:123]\n"
"ds_read_b64_tr_b16 a[220:221], v233 offset:8192\n"
"v_cvt_pk_bf16_f32 v138, v24, v25\n"
"v_mfma_f32_16x16x32_bf16 v[120:123], a[236:239], a[188:191], v[120:123]\n"
"v_exp_f32 v32, v32\n"
"v_mfma_f32_16x16x32_bf16 v[124:127], a[236:239], a[204:207], v[124:127]\n"
"ds_read_b64_tr_b16 a[222:223], v233 offset:12288\n"
"v_exp_f32 v33, v33\n"
"v_mfma_f32_16x16x32_bf16 v[124:127], a[232:235], a[200:203], v[124:127]\n"
"s_waitcnt lgkmcnt(4)\n"
"v_exp_f32 v34, v34\n"
"v_mfma_f32_16x16x32_bf16 a[0:3], a[208:211], v[160:163], a[0:3]\n"
"ds_read_b64_tr_b16 a[224:225], v234 offset:0\n"
"v_cvt_pk_bf16_f32 v139, v26, v27\n"
"v_mfma_f32_16x16x32_bf16 a[0:3], a[212:215], v[176:179], a[0:3]\n"
"v_exp_f32 v35, v35\n"
"v_mfma_f32_16x16x32_bf16 a[4:7], a[212:215], v[180:183], a[4:7]\n"
"ds_read_b64_tr_b16 a[226:227], v234 offset:4096\n"
"buffer_load_dwordx4 v253, s[20:23], s80 offen lds\n"
"v_mfma_f32_16x16x32_bf16 a[4:7], a[208:211], v[164:167], a[4:7]\n"
"s_add_u32 s80, s80, 221184\n"
"v_exp_f32 v48, v48\n"
"v_mfma_f32_16x16x32_bf16 a[8:11], a[208:211], v[168:171], a[8:11]\n"
"s_sub_u32 s86, s80, 2211840\n"
"ds_read_b64_tr_b16 a[228:229], v234 offset:8192\n"
"buffer_store_dwordx4 a[248:251], v255, s[36:39], s41 offen offset:2048 sc1\n"
"v_mfma_f32_16x16x32_bf16 a[8:11], a[212:215], v[184:187], a[8:11]\n"
"s_min_u32 s80, s80, s86\n"
"v_exp_f32 v49, v49\n"
"v_mfma_f32_16x16x32_bf16 a[12:15], a[212:215], v[188:191], a[12:15]\n"
"ds_read_b64_tr_b16 a[230:231], v234 offset:12288\n"
"v_cvt_pk_bf16_f32 v140, v12, v13\n"
"v_mfma_f32_16x16x32_bf16 a[12:15], a[208:211], v[172:175], a[12:15]\n"
"s_waitcnt lgkmcnt(4)\n"
"v_exp_f32 v50, v50\n"
"v_mfma_f32_16x16x32_bf16 a[16:19], a[216:219], v[160:163], a[16:19]\n"
"ds_read_b64_tr_b16 a[232:233], v235 offset:0\n"
"v_cvt_pk_bf16_f32 v141, v14, v15\n"
"v_mfma_f32_16x16x32_bf16 a[16:19], a[220:223], v[176:179], a[16:19]\n"
"v_exp_f32 v51, v51\n"
"v_mfma_f32_16x16x32_bf16 a[20:23], a[220:223], v[180:183], a[20:23]\n"
"ds_read_b64_tr_b16 a[234:235], v235 offset:4096\n"
"v_cvt_pk_bf16_f32 v142, v28, v29\n"
"v_mfma_f32_16x16x32_bf16 a[20:23], a[216:219], v[164:167], a[20:23]\n"
"v_exp_f32 v36, v36\n"
"v_mfma_f32_16x16x32_bf16 a[24:27], a[216:219], v[168:171], a[24:27]\n"
"ds_read_b64_tr_b16 a[236:237], v235 offset:8192\n"
"v_cvt_pk_bf16_f32 v143, v30, v31\n"
"v_mfma_f32_16x16x32_bf16 a[24:27], a[220:223], v[184:187], a[24:27]\n"
"v_exp_f32 v37, v37\n"
"v_mfma_f32_16x16x32_bf16 a[28:31], a[220:223], v[188:191], a[28:31]\n"
"ds_read_b64_tr_b16 a[238:239], v235 offset:12288\n"
"v_cvt_pk_bf16_f32 v144, v32, v33\n"
"v_mfma_f32_16x16x32_bf16 a[28:31], a[216:219], v[172:175], a[28:31]\n"
"s_waitcnt lgkmcnt(4)\n"
"v_exp_f32 v38, v38\n"
"v_mfma_f32_16x16x32_bf16 a[32:35], a[224:227], v[160:163], a[32:35]\n"
"ds_read_b64_tr_b16 a[208:209], v232 offset:512\n"
"v_cvt_pk_bf16_f32 v145, v34, v35\n"
"v_mfma_f32_16x16x32_bf16 a[32:35], a[228:231], v[176:179], a[32:35]\n"
"v_exp_f32 v39, v39\n"
"v_mfma_f32_16x16x32_bf16 a[36:39], a[228:231], v[180:183], a[36:39]\n"
"ds_read_b64_tr_b16 a[210:211], v232 offset:4608\n"
"v_cvt_pk_bf16_f32 v146, v48, v49\n"
"v_mfma_f32_16x16x32_bf16 a[36:39], a[224:227], v[164:167], a[36:39]\n"
"v_exp_f32 v52, v52\n"
"v_mfma_f32_16x16x32_bf16 a[40:43], a[224:227], v[168:171], a[40:43]\n"
"ds_read_b64_tr_b16 a[212:213], v232 offset:8704\n"
"buffer_store_dwordx4 a[252:255], v255, s[36:39], s41 offen offset:3072 sc1\n"
"v_mfma_f32_16x16x32_bf16 a[40:43], a[228:231], v[184:187], a[40:43]\n"
"v_exp_f32 v53, v53\n"
"v_mfma_f32_16x16x32_bf16 a[44:47], a[228:231], v[188:191], a[44:47]\n"
"ds_read_b64_tr_b16 a[214:215], v232 offset:12800\n"
"v_cvt_pk_bf16_f32 v147, v50, v51\n"
"v_mfma_f32_16x16x32_bf16 a[44:47], a[224:227], v[172:175], a[44:47]\n"
"s_waitcnt lgkmcnt(4)\n"
"v_exp_f32 v54, v54\n"
"v_mfma_f32_16x16x32_bf16 a[48:51], a[232:235], v[160:163], a[48:51]\n"
"ds_read_b64_tr_b16 a[216:217], v233 offset:512\n"
"v_exp_f32 v55, v55\n"
"v_mfma_f32_16x16x32_bf16 a[48:51], a[236:239], v[176:179], a[48:51]\n"
"v_exp_f32 v40, v40\n"
"v_mfma_f32_16x16x32_bf16 a[52:55], a[236:239], v[180:183], a[52:55]\n"
"ds_read_b64_tr_b16 a[218:219], v233 offset:4608\n"
"v_cvt_pk_bf16_f32 v148, v36, v37\n"
"v_mfma_f32_16x16x32_bf16 a[52:55], a[232:235], v[164:167], a[52:55]\n"
"v_exp_f32 v41, v41\n"
"v_mfma_f32_16x16x32_bf16 a[56:59], a[232:235], v[168:171], a[56:59]\n"
"ds_read_b64_tr_b16 a[220:221], v233 offset:8704\n"
"v_cvt_pk_bf16_f32 v149, v38, v39\n"
"v_mfma_f32_16x16x32_bf16 a[56:59], a[236:239], v[184:187], a[56:59]\n"
"v_exp_f32 v42, v42\n"
"v_mfma_f32_16x16x32_bf16 a[60:63], a[236:239], v[188:191], a[60:63]\n"
"ds_read_b64_tr_b16 a[222:223], v233 offset:12800\n"
"v_cvt_pk_bf16_f32 v150, v52, v53\n"
"v_mfma_f32_16x16x32_bf16 a[60:63], a[232:235], v[172:175], a[60:63]\n"
"s_waitcnt lgkmcnt(4)\n"
"v_exp_f32 v43, v43\n"
"v_mfma_f32_16x16x32_bf16 a[64:67], a[208:211], v[160:163], a[64:67]\n"
"ds_read_b64_tr_b16 a[224:225], v234 offset:512\n"
"v_cvt_pk_bf16_f32 v151, v54, v55\n"
"v_mfma_f32_16x16x32_bf16 a[64:67], a[212:215], v[176:179], a[64:67]\n"
"ds_read_b64_tr_b16 a[226:227], v234 offset:4608\n"
"v_cvt_pk_bf16_f32 v152, v40, v41\n"
"v_mfma_f32_16x16x32_bf16 a[68:71], a[212:215], v[180:183], a[68:71]\n"
"v_exp_f32 v56, v56\n"
"v_mfma_f32_16x16x32_bf16 a[68:71], a[208:211], v[164:167], a[68:71]\n"
"ds_read_b64_tr_b16 a[228:229], v234 offset:8704\n"
"v_cvt_pk_bf16_f32 v153, v42, v43\n"
"v_mfma_f32_16x16x32_bf16 a[72:75], a[208:211], v[168:171], a[72:75]\n"
"ds_read_b64_tr_b16 a[230:231], v234 offset:12800\n"
"v_mfma_f32_16x16x32_bf16 a[72:75], a[212:215], v[184:187], a[72:75]\n"
"v_exp_f32 v57, v57\n"
"v_mfma_f32_16x16x32_bf16 a[76:79], a[212:215], v[188:191], a[76:79]\n"
"ds_read_b64_tr_b16 a[232:233], v235 offset:512\n"
"v_cvt_pk_bf16_f32 v154, v56, v57\n"
"v_mfma_f32_16x16x32_bf16 a[76:79], a[208:211], v[172:175], a[76:79]\n"
"s_waitcnt lgkmcnt(5)\n"
"ds_read_b64_tr_b16 a[234:235], v235 offset:4608\n"
"v_mfma_f32_16x16x32_bf16 a[80:83], a[216:219], v[160:163], a[80:83]\n"
"v_exp_f32 v58, v58\n"
"v_mfma_f32_16x16x32_bf16 a[80:83], a[220:223], v[176:179], a[80:83]\n"
"ds_read_b64_tr_b16 a[236:237], v235 offset:8704\n"
"v_mfma_f32_16x16x32_bf16 a[84:87], a[220:223], v[180:183], a[84:87]\n"
"ds_read_b64_tr_b16 a[238:239], v235 offset:12800\n"
"v_mfma_f32_16x16x32_bf16 a[84:87], a[216:219], v[164:167], a[84:87]\n"
"v_exp_f32 v59, v59\n"
"v_mfma_f32_16x16x32_bf16 a[88:91], a[216:219], v[168:171], a[88:91]\n"
"v_exp_f32 v44, v44\n"
"v_mfma_f32_16x16x32_bf16 a[88:91], a[220:223], v[184:187], a[88:91]\n"
"v_exp_f32 v45, v45\n"
"v_mfma_f32_16x16x32_bf16 a[92:95], a[220:223], v[188:191], a[92:95]\n"
"v_exp_f32 v46, v46\n"
"v_mfma_f32_16x16x32_bf16 a[92:95], a[216:219], v[172:175], a[92:95]\n"
"s_waitcnt lgkmcnt(0)\n"
"v_exp_f32 v47, v47\n"
"v_mfma_f32_16x16x32_bf16 a[96:99], a[224:227], v[160:163], a[96:99]\n"
"s_waitcnt vmcnt(25)\n"
"s_barrier\n"
"v_exp_f32 v60, v60\n"
"v_mfma_f32_16x16x32_bf16 a[96:99], a[228:231], v[176:179], a[96:99]\n"
"s_add_u32 m0, s85, 0\n"
"v_exp_f32 v61, v61\n"
"v_mfma_f32_16x16x32_bf16 a[100:103], a[228:231], v[180:183], a[100:103]\n"
"ds_read_b128 a[208:211], v240 offset:32768\n"
"buffer_load_dwordx4 v230, s[28:31], s83 offen offset:0 lds\n"
"v_mfma_f32_16x16x32_bf16 a[100:103], a[224:227], v[164:167], a[100:103]\n"
"v_exp_f32 v62, v62\n"
"v_mfma_f32_16x16x32_bf16 a[104:107], a[224:227], v[168:171], a[104:107]\n"
"ds_read_b128 a[212:215], v241 offset:32768\n"
"v_cvt_pk_bf16_f32 v155, v58, v59\n"
"v_mfma_f32_16x16x32_bf16 a[104:107], a[228:231], v[184:187], a[104:107]\n"
"v_exp_f32 v63, v63\n"
"v_mfma_f32_16x16x32_bf16 a[108:111], a[228:231], v[188:191], a[108:111]\n"
"ds_read_b128 a[216:219], v242 offset:32768\n"
"v_cvt_pk_bf16_f32 v156, v44, v45\n"
"v_cvt_pk_bf16_f32 v159, v62, v63\n"
"v_mfma_f32_16x16x32_bf16 a[108:111], a[224:227], v[172:175], a[108:111]\n"
"v_cvt_pk_bf16_f32 v157, v46, v47\n"
"v_cvt_pk_bf16_f32 v158, v60, v61\n"
"v_mfma_f32_16x16x32_bf16 a[112:115], a[232:235], v[160:163], a[112:115]\n"
"ds_read_b128 a[220:223], v243 offset:32768\n"
"v_mfma_f32_16x16x32_bf16 a[112:115], a[236:239], v[176:179], a[112:115]\n"
"v_exp_f32 v64, v64\n"
"v_mfma_f32_16x16x32_bf16 a[116:119], a[236:239], v[180:183], a[116:119]\n"
"buffer_load_dwordx4 v231, s[28:31], s83 offen offset:1024 lds\n"
"v_mfma_f32_16x16x32_bf16 a[116:119], a[232:235], v[164:167], a[116:119]\n"
"v_exp_f32 v65, v65\n"
"v_mfma_f32_16x16x32_bf16 a[120:123], a[232:235], v[168:171], a[120:123]\n"
"v_exp_f32 v66, v66\n"
"v_mfma_f32_16x16x32_bf16 a[120:123], a[236:239], v[184:187], a[120:123]\n"
"v_exp_f32 v67, v67\n"
"v_mfma_f32_16x16x32_bf16 a[124:127], a[236:239], v[188:191], a[124:127]\n"
"v_exp_f32 v80, v80\n"
"v_mfma_f32_16x16x32_bf16 a[124:127], a[232:235], v[172:175], a[124:127]\n"
"v_exp_f32 v81, v81\n"
"v_mfma_f32_16x16x32_bf16 a[128:131], v[208:211], v[160:163], a[128:131]\n"
"v_mfma_f32_16x16x32_bf16 a[128:131], v[208:211], v[176:179], a[128:131]\n"
"v_exp_f32 v82, v82\n"
"v_mfma_f32_16x16x32_bf16 a[132:135], v[208:211], v[164:167], a[132:135]\n"
"buffer_load_dwordx4 v230, s[28:31], s83 offen offset:2048 lds\n"
"v_mfma_f32_16x16x32_bf16 a[132:135], v[208:211], v[180:183], a[132:135]\n"
"v_exp_f32 v83, v83\n"
"v_mfma_f32_16x16x32_bf16 a[136:139], v[208:211], v[168:171], a[136:139]\n"
"v_exp_f32 v68, v68\n"
"v_mfma_f32_16x16x32_bf16 a[136:139], v[208:211], v[184:187], a[136:139]\n"
"v_exp_f32 v69, v69\n"
"v_mfma_f32_16x16x32_bf16 a[140:143], v[208:211], v[172:175], a[140:143]\n"
"v_exp_f32 v70, v70\n"
"v_mfma_f32_16x16x32_bf16 a[140:143], v[208:211], v[188:191], a[140:143]\n"
"s_waitcnt lgkmcnt(2)\n"
"v_exp_f32 v71, v71\n"
"v_mfma_f32_16x16x32_bf16 v[0:3], a[208:211], a[144:147], v[192:195]\n"
"ds_read_b128 a[224:227], v240 offset:36864\n"
"v_cvt_pk_bf16_f32 v160, v64, v65\n"
"v_mfma_f32_16x16x32_bf16 v[0:3], a[212:215], a[148:151], v[0:3]\n"
"v_exp_f32 v84, v84\n"
"v_mfma_f32_16x16x32_bf16 v[4:7], a[212:215], a[164:167], v[196:199]\n"
"ds_read_b128 a[228:231], v241 offset:36864\n"
"buffer_load_dwordx4 v231, s[28:31], s83 offen offset:3072 lds\n"
"v_mfma_f32_16x16x32_bf16 v[4:7], a[208:211], a[160:163], v[4:7]\n"
"s_add_u32 s83, s83, 16384\n"
"v_exp_f32 v85, v85\n"
"v_mfma_f32_16x16x32_bf16 v[8:11], a[208:211], a[176:179], v[200:203]\n"
"s_waitcnt vmcnt(16)\n"
"ds_read_b128 a[232:235], v242 offset:36864\n"
"v_cmp_ne_u32 vcc, s73, v220\n"
"v_mfma_f32_16x16x32_bf16 v[8:11], a[212:215], a[180:183], v[8:11]\n"
"s_cbranch_vccnz .L_fx_retry\n"
".L_fx_ok:\n"
"v_exp_f32 v86, v86\n"
"v_mfma_f32_16x16x32_bf16 v[12:15], a[212:215], a[196:199], v[204:207]\n"
"s_and_b32 s83, s83, 0x1ffff\n"
"s_add_u32 m0, s81, 65536\n"
"ds_read_b128 a[236:239], v243 offset:36864\n"
"v_mfma_f32_16x16x32_bf16 v[12:15], a[208:211], a[192:195], v[12:15]\n"
"s_waitcnt lgkmcnt(4)\n"
"v_exp_f32 v87, v87\n"
"v_mfma_f32_16x16x32_bf16 v[0:3], a[216:219], a[152:155], v[0:3]\n"
"v_cvt_pk_bf16_f32 v161, v66, v67\n"
"v_cvt_pk_bf16_f32 v162, v80, v81\n"
"v_mfma_f32_16x16x32_bf16 v[0:3], a[220:223], a[156:159], v[0:3]\n"
"v_exp_f32 v72, v72\n"
"v_mfma_f32_16x16x32_bf16 v[4:7], a[220:223], a[172:175], v[4:7]\n"
"buffer_load_dwordx4 v252, s[16:19], s80 offen lds\n"
"v_cvt_pk_bf16_f32 v163, v82, v83\n"
"v_mfma_f32_16x16x32_bf16 v[4:7], a[216:219], a[168:171], v[4:7]\n"
"s_add_u32 m0, s81, 66560\n"
"v_exp_f32 v73, v73\n"
"v_mfma_f32_16x16x32_bf16 v[8:11], a[216:219], a[184:187], v[8:11]\n"
"v_exp_f32 v74, v74\n"
"v_mfma_f32_16x16x32_bf16 v[8:11], a[220:223], a[188:191], v[8:11]\n"
"v_exp_f32 v75, v75\n"
"v_mfma_f32_16x16x32_bf16 v[12:15], a[220:223], a[204:207], v[12:15]\n"
"v_exp_f32 v88, v88\n"
"v_mfma_f32_16x16x32_bf16 v[12:15], a[216:219], a[200:203], v[12:15]\n"
"s_waitcnt lgkmcnt(2)\n"
"v_exp_f32 v89, v89\n"
"v_mfma_f32_16x16x32_bf16 v[16:19], a[224:227], a[144:147], v[192:195]\n"
"ds_read_b128 a[208:211], v240 offset:40960\n"
"v_cvt_pk_bf16_f32 v164, v68, v69\n"
"v_mfma_f32_16x16x32_bf16 v[16:19], a[228:231], a[148:151], v[16:19]\n"
"v_exp_f32 v90, v90\n"
"v_mfma_f32_16x16x32_bf16 v[20:23], a[228:231], a[164:167], v[196:199]\n"
"ds_read_b128 a[212:215], v241 offset:40960\n"
"buffer_load_dwordx4 v253, s[16:19], s80 offen lds\n"
"v_mfma_f32_16x16x32_bf16 v[20:23], a[224:227], a[160:163], v[20:23]\n"
"s_add_u32 m0, s81, 73728\n"
"v_exp_f32 v91, v91\n"
"v_mfma_f32_16x16x32_bf16 v[24:27], a[224:227], a[176:179], v[200:203]\n"
"ds_read_b128 a[216:219], v242 offset:40960\n"
"v_cvt_pk_bf16_f32 v165, v70, v71\n"
"v_mfma_f32_16x16x32_bf16 v[24:27], a[228:231], a[180:183], v[24:27]\n"
"v_exp_f32 v76, v76\n"
"v_mfma_f32_16x16x32_bf16 v[28:31], a[228:231], a[196:199], v[204:207]\n"
"ds_read_b128 a[220:223], v243 offset:40960\n"
"v_cvt_pk_bf16_f32 v166, v84, v85\n"
"v_mfma_f32_16x16x32_bf16 v[28:31], a[224:227], a[192:195], v[28:31]\n"
"s_waitcnt lgkmcnt(4)\n"
"v_exp_f32 v77, v77\n"
"v_mfma_f32_16x16x32_bf16 v[16:19], a[232:235], a[152:155], v[16:19]\n"
"v_cvt_pk_bf16_f32 v167, v86, v87\n"
"v_cvt_pk_bf16_f32 v168, v72, v73\n"
"v_mfma_f32_16x16x32_bf16 v[16:19], a[236:239], a[156:159], v[16:19]\n"
"v_exp_f32 v78, v78\n"
"v_mfma_f32_16x16x32_bf16 v[20:23], a[236:239], a[172:175], v[20:23]\n"
"buffer_load_dwordx4 v252, s[20:23], s80 offen lds\n"
"v_cvt_pk_bf16_f32 v169, v74, v75\n"
"v_mfma_f32_16x16x32_bf16 v[20:23], a[232:235], a[168:171], v[20:23]\n"
"s_add_u32 m0, s81, 74752\n"
"v_exp_f32 v79, v79\n"
"v_mfma_f32_16x16x32_bf16 v[24:27], a[232:235], a[184:187], v[24:27]\n"
"v_exp_f32 v92, v92\n"
"v_mfma_f32_16x16x32_bf16 v[24:27], a[236:239], a[188:191], v[24:27]\n"
".L_pad7:\n"
"s_cbranch_execz .L_padret0\n"
"v_exp_f32 v93, v93\n"
"v_mfma_f32_16x16x32_bf16 v[28:31], a[236:239], a[204:207], v[28:31]\n"
"v_exp_f32 v94, v94\n"
"v_mfma_f32_16x16x32_bf16 v[28:31], a[232:235], a[200:203], v[28:31]\n"
"s_waitcnt lgkmcnt(2)\n"
"v_exp_f32 v95, v95\n"
"v_mfma_f32_16x16x32_bf16 v[32:35], a[208:211], a[144:147], v[192:195]\n"
"ds_read_b128 a[224:227], v240 offset:45056\n"
"v_cvt_pk_bf16_f32 v170, v88, v89\n"
"v_mfma_f32_16x16x32_bf16 v[32:35], a[212:215], a[148:151], v[32:35]\n"
"v_exp_f32 v96, v96\n"
"v_mfma_f32_16x16x32_bf16 v[36:39], a[212:215], a[164:167], v[196:199]\n"
"ds_read_b128 a[228:231], v241 offset:45056\n"
"buffer_load_dwordx4 v253, s[20:23], s80 offen lds\n"
"v_mfma_f32_16x16x32_bf16 v[36:39], a[208:211], a[160:163], v[36:39]\n"
"s_add_u32 s80, s80, 221184\n"
"v_exp_f32 v97, v97\n"
"v_mfma_f32_16x16x32_bf16 v[40:43], a[208:211], a[176:179], v[200:203]\n"
"s_sub_u32 s86, s80, 2211840\n"
"s_min_u32 s80, s80, s86\n"
"ds_read_b128 a[232:235], v242 offset:45056\n"
"v_mfma_f32_16x16x32_bf16 v[40:43], a[212:215], a[180:183], v[40:43]\n"
"s_add_u32 m0, s81, 81920\n"
"v_exp_f32 v98, v98\n"
"v_mfma_f32_16x16x32_bf16 v[44:47], a[212:215], a[196:199], v[204:207]\n"
"ds_read_b128 a[236:239], v243 offset:45056\n"
"v_cvt_pk_bf16_f32 v171, v90, v91\n"
"v_mfma_f32_16x16x32_bf16 v[44:47], a[208:211], a[192:195], v[44:47]\n"
"s_waitcnt lgkmcnt(4)\n"
"v_exp_f32 v99, v99\n"
"v_mfma_f32_16x16x32_bf16 v[32:35], a[216:219], a[152:155], v[32:35]\n"
"v_add_u32 v254, 20480, v255\n"
"v_add_u32 v226, 40960, v255\n"
"v_mfma_f32_16x16x32_bf16 v[32:35], a[220:223], a[156:159], v[32:35]\n"
"v_add_u32 v217, 61440, v255\n"
"v_cvt_pk_bf16_f32 v172, v76, v77\n"
"v_mfma_f32_16x16x32_bf16 v[36:39], a[220:223], a[172:175], v[36:39]\n"
"s_add_u32 s76, s79, s58\n"
"s_add_u32 s58, s58, 2048\n"
"buffer_load_dwordx4 v252, s[16:19], s80 offen lds\n"
"v_mfma_f32_16x16x32_bf16 v[36:39], a[216:219], a[168:171], v[36:39]\n"
"s_add_u32 m0, s81, 82944\n"
"v_exp_f32 v112, v112\n"
"v_mfma_f32_16x16x32_bf16 v[40:43], a[216:219], a[184:187], v[40:43]\n"
"s_sub_u32 s98, s58, 20480\n"
"s_min_u32 s58, s58, s98\n"
"s_add_u32 s77, s79, s58\n"
"v_mfma_f32_16x16x32_bf16 v[40:43], a[220:223], a[188:191], v[40:43]\n"
"s_add_u32 s58, s58, 2048\n"
"v_exp_f32 v113, v113\n"
"v_mfma_f32_16x16x32_bf16 v[44:47], a[220:223], a[204:207], v[44:47]\n"
"s_sub_u32 s98, s58, 20480\n"
"s_min_u32 s58, s58, s98\n"
"s_add_u32 s78, s79, s58\n"
"v_mfma_f32_16x16x32_bf16 v[44:47], a[216:219], a[200:203], v[44:47]\n"
"s_waitcnt lgkmcnt(2)\n"
"v_exp_f32 v114, v114\n"
"v_mfma_f32_16x16x32_bf16 v[48:51], a[224:227], a[144:147], v[192:195]\n"
"s_add_u32 s58, s58, 2048\n"
"s_sub_u32 s98, s58, 20480\n"
"ds_read_b64_tr_b16 a[208:209], v232 offset:16384\n"
"v_mfma_f32_16x16x32_bf16 v[48:51], a[228:231], a[148:151], v[48:51]\n"
"s_min_u32 s58, s58, s98\n"
"v_exp_f32 v115, v115\n"
"v_mfma_f32_16x16x32_bf16 v[52:55], a[228:231], a[164:167], v[196:199]\n"
"ds_read_b64_tr_b16 a[210:211], v232 offset:20480\n"
"buffer_load_dwordx4 v253, s[16:19], s80 offen lds\n"
"v_mfma_f32_16x16x32_bf16 v[52:55], a[224:227], a[160:163], v[52:55]\n"
"s_add_u32 m0, s81, 90112\n"
"v_exp_f32 v100, v100\n"
"v_mfma_f32_16x16x32_bf16 v[56:59], a[224:227], a[176:179], v[200:203]\n"
"ds_read_b64_tr_b16 a[212:213], v232 offset:24576\n"
"v_cvt_pk_bf16_f32 v173, v78, v79\n"
"v_mfma_f32_16x16x32_bf16 v[56:59], a[228:231], a[180:183], v[56:59]\n"
"v_exp_f32 v101, v101\n"
"v_mfma_f32_16x16x32_bf16 v[60:63], a[228:231], a[196:199], v[204:207]\n"
"ds_read_b64_tr_b16 a[214:215], v232 offset:28672\n"
"v_cvt_pk_bf16_f32 v174, v92, v93\n"
"v_mfma_f32_16x16x32_bf16 v[60:63], a[224:227], a[192:195], v[60:63]\n"
"s_waitcnt lgkmcnt(4)\n"
"v_exp_f32 v102, v102\n"
"v_mfma_f32_16x16x32_bf16 v[48:51], a[232:235], a[152:155], v[48:51]\n"
"ds_read_b64_tr_b16 a[216:217], v233 offset:16384\n"
"v_cvt_pk_bf16_f32 v175, v94, v95\n"
"v_mfma_f32_16x16x32_bf16 v[48:51], a[236:239], a[156:159], v[48:51]\n"
"v_exp_f32 v103, v103\n"
"v_mfma_f32_16x16x32_bf16 v[52:55], a[236:239], a[172:175], v[52:55]\n"
"ds_read_b64_tr_b16 a[218:219], v233 offset:20480\n"
"buffer_load_dwordx4 v252, s[20:23], s80 offen lds\n"
"v_mfma_f32_16x16x32_bf16 v[52:55], a[232:235], a[168:171], v[52:55]\n"
"s_add_u32 m0, s81, 91136\n"
"v_exp_f32 v116, v116\n"
"v_mfma_f32_16x16x32_bf16 v[56:59], a[232:235], a[184:187], v[56:59]\n"
"ds_read_b64_tr_b16 a[220:221], v233 offset:24576\n"
"v_cvt_pk_bf16_f32 v176, v96, v97\n"
"v_mfma_f32_16x16x32_bf16 v[56:59], a[236:239], a[188:191], v[56:59]\n"
"v_exp_f32 v117, v117\n"
"v_mfma_f32_16x16x32_bf16 v[60:63], a[236:239], a[204:207], v[60:63]\n"
"ds_read_b64_tr_b16 a[222:223], v233 offset:28672\n"
"v_cvt_pk_bf16_f32 v177, v98, v99\n"
"v_mfma_f32_16x16x32_bf16 v[60:63], a[232:235], a[200:203], v[60:63]\n"
"s_waitcnt lgkmcnt(4)\n"
"v_exp_f32 v118, v118\n"
"v_mfma_f32_16x16x32_bf16 a[0:3], a[208:211], v[128:131], a[0:3]\n"
"ds_read_b64_tr_b16 a[224:225], v234 offset:16384\n"
"v_cvt_pk_bf16_f32 v178, v112, v113\n"
"v_mfma_f32_16x16x32_bf16 a[0:3], a[212:215], v[144:147], a[0:3]\n"
"v_exp_f32 v119, v119\n"
"v_mfma_f32_16x16x32_bf16 a[4:7], a[212:215], v[148:151], a[4:7]\n"
"ds_read_b64_tr_b16 a[226:227], v234 offset:20480\n"
"buffer_load_dwordx4 v253, s[20:23], s80 offen lds\n"
"v_mfma_f32_16x16x32_bf16 a[4:7], a[208:211], v[132:135], a[4:7]\n"
"s_add_u32 s80, s80, 221184\n"
"v_exp_f32 v104, v104\n"
"v_mfma_f32_16x16x32_bf16 a[8:11], a[208:211], v[136:139], a[8:11]\n"
"s_sub_u32 s86, s80, 2211840\n"
"s_min_u32 s80, s80, s86\n"
"ds_read_b64_tr_b16 a[228:229], v234 offset:24576\n"
"v_mfma_f32_16x16x32_bf16 a[8:11], a[212:215], v[152:155], a[8:11]\n"
"v_exp_f32 v105, v105\n"
"v_mfma_f32_16x16x32_bf16 a[12:15], a[212:215], v[156:159], a[12:15]\n"
"ds_read_b64_tr_b16 a[230:231], v234 offset:28672\n"
"v_cvt_pk_bf16_f32 v179, v114, v115\n"
"v_mfma_f32_16x16x32_bf16 a[12:15], a[208:211], v[140:143], a[12:15]\n"
"s_waitcnt lgkmcnt(4)\n"
"v_exp_f32 v106, v106\n"
"v_mfma_f32_16x16x32_bf16 a[16:19], a[216:219], v[128:131], a[16:19]\n"
"ds_read_b64_tr_b16 a[232:233], v235 offset:16384\n"
"v_cvt_pk_bf16_f32 v180, v100, v101\n"
"v_mfma_f32_16x16x32_bf16 a[16:19], a[220:223], v[144:147], a[16:19]\n"
"v_exp_f32 v107, v107\n"
"v_mfma_f32_16x16x32_bf16 a[20:23], a[220:223], v[148:151], a[20:23]\n"
"ds_read_b64_tr_b16 a[234:235], v235 offset:20480\n"
"v_cvt_pk_bf16_f32 v181, v102, v103\n"
"v_mfma_f32_16x16x32_bf16 a[20:23], a[216:219], v[132:135], a[20:23]\n"
"v_exp_f32 v120, v120\n"
"v_mfma_f32_16x16x32_bf16 a[24:27], a[216:219], v[136:139], a[24:27]\n"
"ds_read_b64_tr_b16 a[236:237], v235 offset:24576\n"
"v_cvt_pk_bf16_f32 v182, v116, v117\n"
"v_mfma_f32_16x16x32_bf16 a[24:27], a[220:223], v[152:155], a[24:27]\n"
"v_exp_f32 v121, v121\n"
"v_mfma_f32_16x16x32_bf16 a[28:31], a[220:223], v[156:159], a[28:31]\n"
"ds_read_b64_tr_b16 a[238:239], v235 offset:28672\n"
"v_cvt_pk_bf16_f32 v183, v118, v119\n"
"v_mfma_f32_16x16x32_bf16 a[28:31], a[216:219], v[140:143], a[28:31]\n"
"s_waitcnt lgkmcnt(4)\n"
"v_exp_f32 v122, v122\n"
"v_mfma_f32_16x16x32_bf16 a[32:35], a[224:227], v[128:131], a[32:35]\n"
"ds_read_b64_tr_b16 a[208:209], v232 offset:16896\n"
"v_cvt_pk_bf16_f32 v184, v104, v105\n"
"v_mfma_f32_16x16x32_bf16 a[32:35], a[228:231], v[144:147], a[32:35]\n"
"v_exp_f32 v123, v123\n"
"v_mfma_f32_16x16x32_bf16 a[36:39], a[228:231], v[148:151], a[36:39]\n"
"ds_read_b64_tr_b16 a[210:211], v232 offset:20992\n"
"v_cvt_pk_bf16_f32 v185, v106, v107\n"
"v_mfma_f32_16x16x32_bf16 a[36:39], a[224:227], v[132:135], a[36:39]\n"
"v_exp_f32 v108, v108\n"
"v_mfma_f32_16x16x32_bf16 a[40:43], a[224:227], v[136:139], a[40:43]\n"
"ds_read_b64_tr_b16 a[212:213], v232 offset:25088\n"
"v_cvt_pk_bf16_f32 v186, v120, v121\n"
"v_mfma_f32_16x16x32_bf16 a[40:43], a[228:231], v[152:155], a[40:43]\n"
"v_exp_f32 v109, v109\n"
"v_mfma_f32_16x16x32_bf16 a[44:47], a[228:231], v[156:159], a[44:47]\n"
"ds_read_b64_tr_b16 a[214:215], v232 offset:29184\n"
"v_cvt_pk_bf16_f32 v187, v122, v123\n"
"v_mfma_f32_16x16x32_bf16 a[44:47], a[224:227], v[140:143], a[44:47]\n"
"s_waitcnt lgkmcnt(4)\n"
"v_exp_f32 v110, v110\n"
"v_mfma_f32_16x16x32_bf16 a[48:51], a[232:235], v[128:131], a[48:51]\n"
"ds_read_b64_tr_b16 a[216:217], v233 offset:16896\n"
"v_cvt_pk_bf16_f32 v188, v108, v109\n"
"v_mfma_f32_16x16x32_bf16 a[48:51], a[236:239], v[144:147], a[48:51]\n"
"v_exp_f32 v111, v111\n"
"v_mfma_f32_16x16x32_bf16 a[52:55], a[236:239], v[148:151], a[52:55]\n"
"ds_read_b64_tr_b16 a[218:219], v233 offset:20992\n"
"v_cvt_pk_bf16_f32 v189, v110, v111\n"
"v_mfma_f32_16x16x32_bf16 a[52:55], a[232:235], v[132:135], a[52:55]\n"
"v_exp_f32 v124, v124\n"
"v_mfma_f32_16x16x32_bf16 a[56:59], a[232:235], v[136:139], a[56:59]\n"
"ds_read_b64_tr_b16 a[220:221], v233 offset:25088\n"
"v_mfma_f32_16x16x32_bf16 a[56:59], a[236:239], v[152:155], a[56:59]\n"
"v_exp_f32 v125, v125\n"
"v_mfma_f32_16x16x32_bf16 a[60:63], a[236:239], v[156:159], a[60:63]\n"
"ds_read_b64_tr_b16 a[222:223], v233 offset:29184\n"
"v_cvt_pk_bf16_f32 v190, v124, v125\n"
"v_mfma_f32_16x16x32_bf16 a[60:63], a[232:235], v[140:143], a[60:63]\n"
"s_waitcnt lgkmcnt(4)\n"
"v_exp_f32 v126, v126\n"
"v_mfma_f32_16x16x32_bf16 a[64:67], a[208:211], v[128:131], a[64:67]\n"
"ds_read_b64_tr_b16 a[224:225], v234 offset:16896\n"
"v_mfma_f32_16x16x32_bf16 a[64:67], a[212:215], v[144:147], a[64:67]\n"
"ds_read_b64_tr_b16 a[226:227], v234 offset:20992\n"
"v_mfma_f32_16x16x32_bf16 a[68:71], a[212:215], v[148:151], a[68:71]\n"
"v_exp_f32 v0, v0\n"
"v_mfma_f32_16x16x32_bf16 a[68:71], a[208:211], v[132:135], a[68:71]\n"
"ds_read_b64_tr_b16 a[228:229], v234 offset:25088\n"
"v_mfma_f32_16x16x32_bf16 a[72:75], a[208:211], v[136:139], a[72:75]\n"
"ds_read_b64_tr_b16 a[230:231], v234 offset:29184\n"
"v_mfma_f32_16x16x32_bf16 a[72:75], a[212:215], v[152:155], a[72:75]\n"
"v_exp_f32 v127, v127\n"
"v_mfma_f32_16x16x32_bf16 a[76:79], a[212:215], v[156:159], a[76:79]\n"
"ds_read_b64_tr_b16 a[232:233], v235 offset:16896\n"
"v_cvt_pk_bf16_f32 v191, v126, v127\n"
"v_mfma_f32_16x16x32_bf16 a[76:79], a[208:211], v[140:143], a[76:79]\n"
"s_waitcnt lgkmcnt(5)\n"
"ds_read_b64_tr_b16 a[234:235], v235 offset:20992\n"
"v_mfma_f32_16x16x32_bf16 a[80:83], a[216:219], v[128:131], a[80:83]\n"
"v_mfma_f32_16x16x32_bf16 a[80:83], a[220:223], v[144:147], a[80:83]\n"
"ds_read_b64_tr_b16 a[236:237], v235 offset:25088\n"
"v_mfma_f32_16x16x32_bf16 a[84:87], a[220:223], v[148:151], a[84:87]\n"
"ds_read_b64_tr_b16 a[238:239], v235 offset:29184\n"
"v_mfma_f32_16x16x32_bf16 a[84:87], a[216:219], v[132:135], a[84:87]\n"
"v_exp_f32 v1, v1\n"
"v_mfma_f32_16x16x32_bf16 a[88:91], a[216:219], v[136:139], a[88:91]\n"
"v_exp_f32 v2, v2\n"
"v_mfma_f32_16x16x32_bf16 a[88:91], a[220:223], v[152:155], a[88:91]\n"
"v_exp_f32 v3, v3\n"
"v_mfma_f32_16x16x32_bf16 a[92:95], a[220:223], v[156:159], a[92:95]\n"
"v_mfma_f32_16x16x32_bf16 a[92:95], a[216:219], v[140:143], a[92:95]\n"
"s_waitcnt lgkmcnt(0)\n"
"v_exp_f32 v16, v16\n"
"v_mfma_f32_16x16x32_bf16 a[96:99], a[224:227], v[128:131], a[96:99]\n"
"s_waitcnt vmcnt(20)\n"
"s_barrier\n"
"v_mfma_f32_16x16x32_bf16 a[96:99], a[228:231], v[144:147], a[96:99]\n"
"s_add_u32 m0, s85, 16384\n"
"v_exp_f32 v17, v17\n"
"v_mfma_f32_16x16x32_bf16 a[100:103], a[228:231], v[148:151], a[100:103]\n"
"ds_read_b128 a[208:211], v240 offset:16384\n"
"buffer_load_dwordx4 v230, s[28:31], s83 offen offset:0 lds\n"
"v_mfma_f32_16x16x32_bf16 a[100:103], a[224:227], v[132:135], a[100:103]\n"
"v_exp_f32 v18, v18\n"
"v_mfma_f32_16x16x32_bf16 a[104:107], a[224:227], v[136:139], a[104:107]\n"
"ds_read_b128 a[212:215], v241 offset:16384\n"
"v_mfma_f32_16x16x32_bf16 a[104:107], a[228:231], v[152:155], a[104:107]\n"
"v_exp_f32 v19, v19\n"
"v_mfma_f32_16x16x32_bf16 a[108:111], a[228:231], v[156:159], a[108:111]\n"
"ds_read_b128 a[216:219], v242 offset:16384\n"
"v_mfma_f32_16x16x32_bf16 a[108:111], a[224:227], v[140:143], a[108:111]\n"
"v_exp_f32 v4, v4\n"
"v_mfma_f32_16x16x32_bf16 a[112:115], a[232:235], v[128:131], a[112:115]\n"
"ds_read_b128 a[220:223], v243 offset:16384\n"
"v_mfma_f32_16x16x32_bf16 a[112:115], a[236:239], v[144:147], a[112:115]\n"
"v_exp_f32 v5, v5\n"
"v_mfma_f32_16x16x32_bf16 a[116:119], a[236:239], v[148:151], a[116:119]\n"
"buffer_load_dwordx4 v231, s[28:31], s83 offen offset:1024 lds\n"
"v_mfma_f32_16x16x32_bf16 a[116:119], a[232:235], v[132:135], a[116:119]\n"
"v_exp_f32 v6, v6\n"
"v_mfma_f32_16x16x32_bf16 a[120:123], a[232:235], v[136:139], a[120:123]\n"
"v_exp_f32 v7, v7\n"
"v_mfma_f32_16x16x32_bf16 a[120:123], a[236:239], v[152:155], a[120:123]\n"
"v_exp_f32 v20, v20\n"
"v_mfma_f32_16x16x32_bf16 a[124:127], a[236:239], v[156:159], a[124:127]\n"
"v_mfma_f32_16x16x32_bf16 a[124:127], a[232:235], v[140:143], a[124:127]\n"
"v_exp_f32 v21, v21\n"
"v_mfma_f32_16x16x32_bf16 a[128:131], v[208:211], v[128:131], a[128:131]\n"
"v_mfma_f32_16x16x32_bf16 a[128:131], v[208:211], v[144:147], a[128:131]\n"
"v_exp_f32 v22, v22\n"
"v_mfma_f32_16x16x32_bf16 a[132:135], v[208:211], v[132:135], a[132:135]\n"
"buffer_load_dwordx4 v230, s[28:31], s83 offen offset:2048 lds\n"
"v_mfma_f32_16x16x32_bf16 a[132:135], v[208:211], v[148:151], a[132:135]\n"
"v_exp_f32 v23, v23\n"
"v_mfma_f32_16x16x32_bf16 a[136:139], v[208:211], v[136:139], a[136:139]\n"
"v_exp_f32 v8, v8\n"
"v_mfma_f32_16x16x32_bf16 a[136:139], v[208:211], v[152:155], a[136:139]\n"
"v_exp_f32 v9, v9\n"
"v_mfma_f32_16x16x32_bf16 a[140:143], v[208:211], v[140:143], a[140:143]\n"
"v_mfma_f32_16x16x32_bf16 a[140:143], v[208:211], v[156:159], a[140:143]\n"
"s_waitcnt lgkmcnt(2)\n"
"v_exp_f32 v10, v10\n"
"v_mfma_f32_16x16x32_bf16 v[64:67], a[208:211], a[144:147], v[192:195]\n"
"ds_read_b128 a[224:227], v240 offset:20480\n"
"v_cvt_pk_bf16_f32 v128, v0, v1\n"
"v_mfma_f32_16x16x32_bf16 v[64:67], a[212:215], a[148:151], v[64:67]\n"
"v_exp_f32 v11, v11\n"
"v_mfma_f32_16x16x32_bf16 v[68:71], a[212:215], a[164:167], v[196:199]\n"
"ds_read_b128 a[228:231], v241 offset:20480\n"
"buffer_load_dwordx4 v231, s[28:31], s83 offen offset:3072 lds\n"
"v_mfma_f32_16x16x32_bf16 v[68:71], a[208:211], a[160:163], v[68:71]\n"
"s_add_u32 s83, s83, 16384\n"
"v_exp_f32 v24, v24\n"
"v_mfma_f32_16x16x32_bf16 v[72:75], a[208:211], a[176:179], v[200:203]\n"
"s_and_b32 s83, s83, 0x1ffff\n"
"s_add_u32 m0, s81, 114688\n"
"ds_read_b128 a[232:235], v242 offset:20480\n"
"v_mfma_f32_16x16x32_bf16 v[72:75], a[212:215], a[180:183], v[72:75]\n"
"v_exp_f32 v25, v25\n"
"v_mfma_f32_16x16x32_bf16 v[76:79], a[212:215], a[196:199], v[204:207]\n"
"ds_read_b128 a[236:239], v243 offset:20480\n"
"v_cvt_pk_bf16_f32 v129, v2, v3\n"
"v_mfma_f32_16x16x32_bf16 v[76:79], a[208:211], a[192:195], v[76:79]\n"
"s_waitcnt lgkmcnt(4)\n"
"v_exp_f32 v26, v26\n"
"v_mfma_f32_16x16x32_bf16 v[64:67], a[216:219], a[152:155], v[64:67]\n"
"v_exp_f32 v27, v27\n"
"v_mfma_f32_16x16x32_bf16 v[64:67], a[220:223], a[156:159], v[64:67]\n"
"v_exp_f32 v12, v12\n"
"v_mfma_f32_16x16x32_bf16 v[68:71], a[220:223], a[172:175], v[68:71]\n"
"buffer_load_dwordx4 v252, s[16:19], s80 offen lds\n"
"v_cvt_pk_bf16_f32 v130, v16, v17\n"
"v_mfma_f32_16x16x32_bf16 v[68:71], a[216:219], a[168:171], v[68:71]\n"
"s_add_u32 m0, s81, 115712\n"
"v_exp_f32 v13, v13\n"
"v_mfma_f32_16x16x32_bf16 v[72:75], a[216:219], a[184:187], v[72:75]\n"
"v_cvt_pk_bf16_f32 v131, v18, v19\n"
"v_cvt_pk_bf16_f32 v132, v4, v5\n"
"v_mfma_f32_16x16x32_bf16 v[72:75], a[220:223], a[188:191], v[72:75]\n"
"v_exp_f32 v14, v14\n"
"v_mfma_f32_16x16x32_bf16 v[76:79], a[220:223], a[204:207], v[76:79]\n"
"v_exp_f32 v15, v15\n"
"v_mfma_f32_16x16x32_bf16 v[76:79], a[216:219], a[200:203], v[76:79]\n"
"s_waitcnt lgkmcnt(2)\n"
"v_exp_f32 v28, v28\n"
"v_mfma_f32_16x16x32_bf16 v[80:83], a[224:227], a[144:147], v[192:195]\n"
"ds_read_b128 a[208:211], v240 offset:24576\n"
"v_cvt_pk_bf16_f32 v133, v6, v7\n"
"v_mfma_f32_16x16x32_bf16 v[80:83], a[228:231], a[148:151], v[80:83]\n"
"v_exp_f32 v29, v29\n"
"v_mfma_f32_16x16x32_bf16 v[84:87], a[228:231], a[164:167], v[196:199]\n"
"ds_read_b128 a[212:215], v241 offset:24576\n"
"buffer_load_dwordx4 v253, s[16:19], s80 offen lds\n"
"v_mfma_f32_16x16x32_bf16 v[84:87], a[224:227], a[160:163], v[84:87]\n"
"s_add_u32 m0, s81, 122880\n"
"v_exp_f32 v30, v30\n"
"v_mfma_f32_16x16x32_bf16 v[88:91], a[224:227], a[176:179], v[200:203]\n"
"ds_read_b128 a[216:219], v242 offset:24576\n"
"v_cvt_pk_bf16_f32 v134, v20, v21\n"
"v_mfma_f32_16x16x32_bf16 v[88:91], a[228:231], a[180:183], v[88:91]\n"
"v_exp_f32 v31, v31\n"
"v_mfma_f32_16x16x32_bf16 v[92:95], a[228:231], a[196:199], v[204:207]\n"
"ds_read_b128 a[220:223], v243 offset:24576\n"
"v_cvt_pk_bf16_f32 v135, v22, v23\n"
"v_mfma_f32_16x16x32_bf16 v[92:95], a[224:227], a[192:195], v[92:95]\n"
"s_waitcnt lgkmcnt(4)\n"
"v_exp_f32 v32, v32\n"
"v_mfma_f32_16x16x32_bf16 v[80:83], a[232:235], a[152:155], v[80:83]\n"
"v_cvt_pk_bf16_f32 v136, v8, v9\n"
"v_cvt_pk_bf16_f32 v137, v10, v11\n"
"v_mfma_f32_16x16x32_bf16 v[80:83], a[236:239], a[156:159], v[80:83]\n"
"v_exp_f32 v33, v33\n"
"v_mfma_f32_16x16x32_bf16 v[84:87], a[236:239], a[172:175], v[84:87]\n"
"buffer_load_dwordx4 v252, s[20:23], s80 offen lds\n"
"v_cvt_pk_bf16_f32 v138, v24, v25\n"
"v_mfma_f32_16x16x32_bf16 v[84:87], a[232:235], a[168:171], v[84:87]\n"
"s_add_u32 m0, s81, 123904\n"
"v_exp_f32 v34, v34\n"
"v_mfma_f32_16x16x32_bf16 v[88:91], a[232:235], a[184:187], v[88:91]\n"
"v_cvt_pk_bf16_f32 v139, v26, v27\n"
"v_cvt_pk_bf16_f32 v140, v12, v13\n"
"v_mfma_f32_16x16x32_bf16 v[88:91], a[236:239], a[188:191], v[88:91]\n"
"v_exp_f32 v35, v35\n"
"v_mfma_f32_16x16x32_bf16 v[92:95], a[236:239], a[204:207], v[92:95]\n"
"v_exp_f32 v48, v48\n"
"v_mfma_f32_16x16x32_bf16 v[92:95], a[232:235], a[200:203], v[92:95]\n"
"s_waitcnt lgkmcnt(2)\n"
"v_exp_f32 v49, v49\n"
"v_mfma_f32_16x16x32_bf16 v[96:99], a[208:211], a[144:147], v[192:195]\n"
"ds_read_b128 a[224:227], v240 offset:28672\n"
"v_cvt_pk_bf16_f32 v141, v14, v15\n"
"v_mfma_f32_16x16x32_bf16 v[96:99], a[212:215], a[148:151], v[96:99]\n"
"v_exp_f32 v50, v50\n"
"v_mfma_f32_16x16x32_bf16 v[100:103], a[212:215], a[164:167], v[196:199]\n"
"ds_read_b128 a[228:231], v241 offset:28672\n"
"buffer_load_dwordx4 v253, s[20:23], s80 offen lds\n"
"v_mfma_f32_16x16x32_bf16 v[100:103], a[208:211], a[160:163], v[100:103]\n"
"s_add_u32 s80, s80, 221184\n"
"v_exp_f32 v51, v51\n"
"v_mfma_f32_16x16x32_bf16 v[104:107], a[208:211], a[176:179], v[200:203]\n"
"s_sub_u32 s86, s80, 2211840\n"
"s_min_u32 s80, s80, s86\n"
"ds_read_b128 a[232:235], v242 offset:28672\n"
"v_mfma_f32_16x16x32_bf16 v[104:107], a[212:215], a[180:183], v[104:107]\n"
"v_exp_f32 v36, v36\n"
"v_mfma_f32_16x16x32_bf16 v[108:111], a[212:215], a[196:199], v[204:207]\n"
"ds_read_b128 a[236:239], v243 offset:28672\n"
"v_cvt_pk_bf16_f32 v142, v28, v29\n"
"v_mfma_f32_16x16x32_bf16 v[108:111], a[208:211], a[192:195], v[108:111]\n"
"s_waitcnt lgkmcnt(4)\n"
"v_exp_f32 v37, v37\n"
"v_mfma_f32_16x16x32_bf16 v[96:99], a[216:219], a[152:155], v[96:99]\n"
"v_cvt_pk_bf16_f32 v143, v30, v31\n"
"v_cvt_pk_bf16_f32 v144, v32, v33\n"
"v_mfma_f32_16x16x32_bf16 v[96:99], a[220:223], a[156:159], v[96:99]\n"
"v_exp_f32 v38, v38\n"
"v_mfma_f32_16x16x32_bf16 v[100:103], a[220:223], a[172:175], v[100:103]\n"
"v_exp_f32 v39, v39\n"
"v_mfma_f32_16x16x32_bf16 v[100:103], a[216:219], a[168:171], v[100:103]\n"
"v_exp_f32 v52, v52\n"
"v_mfma_f32_16x16x32_bf16 v[104:107], a[216:219], a[184:187], v[104:107]\n"
"v_cvt_pk_bf16_f32 v145, v34, v35\n"
"v_cvt_pk_bf16_f32 v146, v48, v49\n"
"v_mfma_f32_16x16x32_bf16 v[104:107], a[220:223], a[188:191], v[104:107]\n"
"v_exp_f32 v53, v53\n"
"v_mfma_f32_16x16x32_bf16 v[108:111], a[220:223], a[204:207], v[108:111]\n"
"v_cvt_pk_bf16_f32 v147, v50, v51\n"
"v_cvt_pk_bf16_f32 v148, v36, v37\n"
"v_mfma_f32_16x16x32_bf16 v[108:111], a[216:219], a[200:203], v[108:111]\n"
"s_waitcnt lgkmcnt(2)\n"
"v_exp_f32 v54, v54\n"
"v_mfma_f32_16x16x32_bf16 v[112:115], a[224:227], a[144:147], v[192:195]\n"
"ds_read_b64_tr_b16 a[208:209], v244 offset:32768\n"
"v_cvt_pk_bf16_f32 v149, v38, v39\n"
"v_mfma_f32_16x16x32_bf16 v[112:115], a[228:231], a[148:151], v[112:115]\n"
"v_exp_f32 v55, v55\n"
"v_mfma_f32_16x16x32_bf16 v[116:119], a[228:231], a[164:167], v[196:199]\n"
"ds_read_b64_tr_b16 a[210:211], v244 offset:36864\n"
"v_cvt_pk_bf16_f32 v150, v52, v53\n"
"v_mfma_f32_16x16x32_bf16 v[116:119], a[224:227], a[160:163], v[116:119]\n"
"v_exp_f32 v40, v40\n"
"v_mfma_f32_16x16x32_bf16 v[120:123], a[224:227], a[176:179], v[200:203]\n"
"ds_read_b64_tr_b16 a[212:213], v244 offset:40960\n"
"v_cvt_pk_bf16_f32 v151, v54, v55\n"
"v_mfma_f32_16x16x32_bf16 v[120:123], a[228:231], a[180:183], v[120:123]\n"
"v_exp_f32 v41, v41\n"
"v_mfma_f32_16x16x32_bf16 v[124:127], a[228:231], a[196:199], v[204:207]\n"
"ds_read_b64_tr_b16 a[214:215], v244 offset:45056\n"
"v_cvt_pk_bf16_f32 v152, v40, v41\n"
"v_mfma_f32_16x16x32_bf16 v[124:127], a[224:227], a[192:195], v[124:127]\n"
"s_waitcnt lgkmcnt(4)\n"
"v_exp_f32 v42, v42\n"
"v_mfma_f32_16x16x32_bf16 v[112:115], a[232:235], a[152:155], v[112:115]\n"
"ds_read_b64_tr_b16 a[216:217], v245 offset:32768\n"
"v_mfma_f32_16x16x32_bf16 v[112:115], a[236:239], a[156:159], v[112:115]\n"
"v_exp_f32 v43, v43\n"
"v_mfma_f32_16x16x32_bf16 v[116:119], a[236:239], a[172:175], v[116:119]\n"
"ds_read_b64_tr_b16 a[218:219], v245 offset:36864\n"
"v_cvt_pk_bf16_f32 v153, v42, v43\n"
"v_mfma_f32_16x16x32_bf16 v[116:119], a[232:235], a[168:171], v[116:119]\n"
"v_exp_f32 v56, v56\n"
"v_mfma_f32_16x16x32_bf16 v[120:123], a[232:235], a[184:187], v[120:123]\n"
"ds_read_b64_tr_b16 a[220:221], v245 offset:40960\n"
"v_mfma_f32_16x16x32_bf16 v[120:123], a[236:239], a[188:191], v[120:123]\n"
"v_exp_f32 v57, v57\n"
"v_mfma_f32_16x16x32_bf16 v[124:127], a[236:239], a[204:207], v[124:127]\n"
"ds_read_b64_tr_b16 a[222:223], v245 offset:45056\n"
"v_cvt_pk_bf16_f32 v154, v56, v57\n"
"v_mfma_f32_16x16x32_bf16 v[124:127], a[232:235], a[200:203], v[124:127]\n"
"s_waitcnt lgkmcnt(4)\n"
"v_exp_f32 v58, v58\n"
"v_mfma_f32_16x16x32_bf16 a[0:3], a[208:211], v[160:163], a[0:3]\n"
"ds_read_b64_tr_b16 a[224:225], v246 offset:32768\n"
"buffer_load_dwordx4 a[144:147], v255, s[36:39], s76 offen offset:0 sc1\n"
"v_mfma_f32_16x16x32_bf16 a[0:3], a[212:215], v[176:179], a[0:3]\n"
"v_exp_f32 v59, v59\n"
"v_mfma_f32_16x16x32_bf16 a[4:7], a[212:215], v[180:183], a[4:7]\n"
"ds_read_b64_tr_b16 a[226:227], v246 offset:36864\n"
"v_cvt_pk_bf16_f32 v155, v58, v59\n"
"v_mfma_f32_16x16x32_bf16 a[4:7], a[208:211], v[164:167], a[4:7]\n"
"v_exp_f32 v44, v44\n"
"v_mfma_f32_16x16x32_bf16 a[8:11], a[208:211], v[168:171], a[8:11]\n"
"ds_read_b64_tr_b16 a[228:229], v246 offset:40960\n"
"v_mfma_f32_16x16x32_bf16 a[8:11], a[212:215], v[184:187], a[8:11]\n"
"v_exp_f32 v45, v45\n"
"v_mfma_f32_16x16x32_bf16 a[12:15], a[212:215], v[188:191], a[12:15]\n"
"ds_read_b64_tr_b16 a[230:231], v246 offset:45056\n"
"buffer_load_dwordx4 a[148:151], v255, s[36:39], s76 offen offset:1024 sc1\n"
"v_mfma_f32_16x16x32_bf16 a[12:15], a[208:211], v[172:175], a[12:15]\n"
"s_waitcnt lgkmcnt(4)\n"
"v_exp_f32 v46, v46\n"
"v_mfma_f32_16x16x32_bf16 a[16:19], a[216:219], v[160:163], a[16:19]\n"
"ds_read_b64_tr_b16 a[232:233], v247 offset:32768\n"
"v_cvt_pk_bf16_f32 v156, v44, v45\n"
"v_mfma_f32_16x16x32_bf16 a[16:19], a[220:223], v[176:179], a[16:19]\n"
"v_exp_f32 v47, v47\n"
"v_mfma_f32_16x16x32_bf16 a[20:23], a[220:223], v[180:183], a[20:23]\n"
"ds_read_b64_tr_b16 a[234:235], v247 offset:36864\n"
"v_cvt_pk_bf16_f32 v157, v46, v47\n"
"v_mfma_f32_16x16x32_bf16 a[20:23], a[216:219], v[164:167], a[20:23]\n"
"v_exp_f32 v60, v60\n"
"v_mfma_f32_16x16x32_bf16 a[24:27], a[216:219], v[168:171], a[24:27]\n"
"ds_read_b64_tr_b16 a[236:237], v247 offset:40960\n"
"buffer_load_dwordx4 a[152:155], v254, s[36:39], s76 offen offset:0 sc1\n"
"v_mfma_f32_16x16x32_bf16 a[24:27], a[220:223], v[184:187], a[24:27]\n"
"v_exp_f32 v61, v61\n"
"v_mfma_f32_16x16x32_bf16 a[28:31], a[220:223], v[188:191], a[28:31]\n"
"ds_read_b64_tr_b16 a[238:239], v247 offset:45056\n"
"v_cvt_pk_bf16_f32 v158, v60, v61\n"
"v_mfma_f32_16x16x32_bf16 a[28:31], a[216:219], v[172:175], a[28:31]\n"
"s_waitcnt lgkmcnt(4)\n"
"v_exp_f32 v62, v62\n"
"v_mfma_f32_16x16x32_bf16 a[32:35], a[224:227], v[160:163], a[32:35]\n"
"ds_read_b64_tr_b16 a[208:209], v244 offset:33280\n"
"v_mfma_f32_16x16x32_bf16 a[32:35], a[228:231], v[176:179], a[32:35]\n"
"v_exp_f32 v63, v63\n"
"v_mfma_f32_16x16x32_bf16 a[36:39], a[228:231], v[180:183], a[36:39]\n"
"ds_read_b64_tr_b16 a[210:211], v244 offset:37376\n"
"buffer_load_dwordx4 a[156:159], v254, s[36:39], s76 offen offset:1024 sc1\n"
"v_mfma_f32_16x16x32_bf16 a[36:39], a[224:227], v[164:167], a[36:39]\n"
"v_cvt_pk_bf16_f32 v159, v62, v63\n"
"v_mfma_f32_16x16x32_bf16 a[40:43], a[224:227], v[168:171], a[40:43]\n"
"ds_read_b64_tr_b16 a[212:213], v244 offset:41472\n"
"v_mfma_f32_16x16x32_bf16 a[40:43], a[228:231], v[184:187], a[40:43]\n"
"v_exp_f32 v64, v64\n"
"v_mfma_f32_16x16x32_bf16 a[44:47], a[228:231], v[188:191], a[44:47]\n"
"ds_read_b64_tr_b16 a[214:215], v244 offset:45568\n"
"v_mfma_f32_16x16x32_bf16 a[44:47], a[224:227], v[172:175], a[44:47]\n"
"s_waitcnt lgkmcnt(4)\n"
"v_exp_f32 v65, v65\n"
"v_mfma_f32_16x16x32_bf16 a[48:51], a[232:235], v[160:163], a[48:51]\n"
"ds_read_b64_tr_b16 a[216:217], v245 offset:33280\n"
"buffer_load_dwordx4 a[160:163], v226, s[36:39], s76 offen offset:0 sc1\n"
"v_mfma_f32_16x16x32_bf16 a[48:51], a[236:239], v[176:179], a[48:51]\n"
"v_exp_f32 v66, v66\n"
"v_mfma_f32_16x16x32_bf16 a[52:55], a[236:239], v[180:183], a[52:55]\n"
"ds_read_b64_tr_b16 a[218:219], v245 offset:37376\n"
"v_mfma_f32_16x16x32_bf16 a[52:55], a[232:235], v[164:167], a[52:55]\n"
"v_exp_f32 v67, v67\n"
"v_mfma_f32_16x16x32_bf16 a[56:59], a[232:235], v[168:171], a[56:59]\n"
"ds_read_b64_tr_b16 a[220:221], v245 offset:41472\n"
"v_mfma_f32_16x16x32_bf16 a[56:59], a[236:239], v[184:187], a[56:59]\n"
"v_exp_f32 v80, v80\n"
"v_mfma_f32_16x16x32_bf16 a[60:63], a[236:239], v[188:191], a[60:63]\n"
"ds_read_b64_tr_b16 a[222:223], v245 offset:45568\n"
"buffer_load_dwordx4 a[164:167], v226, s[36:39], s76 offen offset:1024 sc1\n"
"v_mfma_f32_16x16x32_bf16 a[60:63], a[232:235], v[172:175], a[60:63]\n"
"s_waitcnt lgkmcnt(4)\n"
"v_exp_f32 v81, v81\n"
"v_mfma_f32_16x16x32_bf16 a[64:67], a[208:211], v[160:163], a[64:67]\n"
"ds_read_b64_tr_b16 a[224:225], v246 offset:33280\n"
"v_mfma_f32_16x16x32_bf16 a[64:67], a[212:215], v[176:179], a[64:67]\n"
"ds_read_b64_tr_b16 a[226:227], v246 offset:37376\n"
"v_mfma_f32_16x16x32_bf16 a[68:71], a[212:215], v[180:183], a[68:71]\n"
"v_exp_f32 v82, v82\n"
"v_mfma_f32_16x16x32_bf16 a[68:71], a[208:211], v[164:167], a[68:71]\n"
"ds_read_b64_tr_b16 a[228:229], v246 offset:41472\n"
"v_mfma_f32_16x16x32_bf16 a[72:75], a[208:211], v[168:171], a[72:75]\n"
"ds_read_b64_tr_b16 a[230:231], v246 offset:45568\n"
"buffer_load_dwordx4 a[168:171], v217, s[36:39], s76 offen offset:0 sc1\n"
"v_mfma_f32_16x16x32_bf16 a[72:75], a[212:215], v[184:187], a[72:75]\n"
"v_exp_f32 v83, v83\n"
"v_mfma_f32_16x16x32_bf16 a[76:79], a[212:215], v[188:191], a[76:79]\n"
"ds_read_b64_tr_b16 a[232:233], v247 offset:33280\n"
"v_mfma_f32_16x16x32_bf16 a[76:79], a[208:211], v[172:175], a[76:79]\n"
"s_waitcnt lgkmcnt(5)\n"
"ds_read_b64_tr_b16 a[234:235], v247 offset:37376\n"
"v_mfma_f32_16x16x32_bf16 a[80:83], a[216:219], v[160:163], a[80:83]\n"
"v_exp_f32 v68, v68\n"
"v_mfma_f32_16x16x32_bf16 a[80:83], a[220:223], v[176:179], a[80:83]\n"
"ds_read_b64_tr_b16 a[236:237], v247 offset:41472\n"
"v_mfma_f32_16x16x32_bf16 a[84:87], a[220:223], v[180:183], a[84:87]\n"
"ds_read_b64_tr_b16 a[238:239], v247 offset:45568\n"
"buffer_load_dwordx4 a[172:175], v217, s[36:39], s76 offen offset:1024 sc1\n"
"v_mfma_f32_16x16x32_bf16 a[84:87], a[216:219], v[164:167], a[84:87]\n"
"v_exp_f32 v69, v69\n"
"v_mfma_f32_16x16x32_bf16 a[88:91], a[216:219], v[168:171], a[88:91]\n"
"v_exp_f32 v70, v70\n"
"v_mfma_f32_16x16x32_bf16 a[88:91], a[220:223], v[184:187], a[88:91]\n"
"v_exp_f32 v71, v71\n"
"v_mfma_f32_16x16x32_bf16 a[92:95], a[220:223], v[188:191], a[92:95]\n"
"v_exp_f32 v84, v84\n"
"v_mfma_f32_16x16x32_bf16 a[92:95], a[216:219], v[172:175], a[92:95]\n"
"s_waitcnt lgkmcnt(0)\n"
"v_exp_f32 v85, v85\n"
"v_mfma_f32_16x16x32_bf16 a[96:99], a[224:227], v[160:163], a[96:99]\n"
"s_waitcnt vmcnt(24)\n"
"s_barrier\n"
"v_mfma_f32_16x16x32_bf16 a[96:99], a[228:231], v[176:179], a[96:99]\n"
"s_lshl_b32 s92, s75, 4\n"
"ds_read_b64_tr_b16 a[208:209], v232 offset:0\n"
"v_mfma_f32_16x16x32_bf16 a[100:103], a[228:231], v[180:183], a[100:103]\n"
"s_add_u32 s92, s92, 4096\n"
"ds_read_b64_tr_b16 a[210:211], v232 offset:4096\n"
"v_mov_b32 v219, s73\n"
"v_mfma_f32_16x16x32_bf16 a[100:103], a[224:227], v[164:167], a[100:103]\n"
"s_add_u32 m0, s81, 98304\n"
"v_exp_f32 v86, v86\n"
"v_mfma_f32_16x16x32_bf16 a[104:107], a[224:227], v[168:171], a[104:107]\n"
"ds_read_b64_tr_b16 a[212:213], v232 offset:8192\n"
"buffer_store_dword v219, v225, s[36:39], s92 offen sc1\n"
"v_mfma_f32_16x16x32_bf16 a[104:107], a[228:231], v[184:187], a[104:107]\n"
"ds_read_b64_tr_b16 a[214:215], v232 offset:12288\n"
"v_mfma_f32_16x16x32_bf16 a[108:111], a[228:231], v[188:191], a[108:111]\n"
"buffer_load_dwordx4 v252, s[16:19], s80 offen lds\n"
"v_mfma_f32_16x16x32_bf16 a[108:111], a[224:227], v[172:175], a[108:111]\n"
"s_add_u32 m0, s81, 99328\n"
"ds_read_b64_tr_b16 a[216:217], v233 offset:0\n"
"v_mfma_f32_16x16x32_bf16 a[112:115], a[232:235], v[160:163], a[112:115]\n"
"ds_read_b64_tr_b16 a[218:219], v233 offset:4096\n"
"v_mfma_f32_16x16x32_bf16 a[112:115], a[236:239], v[176:179], a[112:115]\n"
"v_exp_f32 v87, v87\n"
"v_mfma_f32_16x16x32_bf16 a[116:119], a[236:239], v[180:183], a[116:119]\n"
"ds_read_b64_tr_b16 a[220:221], v233 offset:8192\n"
"v_mfma_f32_16x16x32_bf16 a[116:119], a[232:235], v[164:167], a[116:119]\n"
"ds_read_b64_tr_b16 a[222:223], v233 offset:12288\n"
"v_mfma_f32_16x16x32_bf16 a[120:123], a[232:235], v[168:171], a[120:123]\n"
"buffer_load_dwordx4 v253, s[16:19], s80 offen lds\n"
"v_mfma_f32_16x16x32_bf16 a[120:123], a[236:239], v[184:187], a[120:123]\n"
"s_add_u32 m0, s81, 106496\n"
"v_exp_f32 v72, v72\n"
"v_mfma_f32_16x16x32_bf16 a[124:127], a[236:239], v[188:191], a[124:127]\n"
".L_pad8:\n"
"s_cbranch_execz .L_padret0\n"
"v_exp_f32 v73, v73\n"
"v_mfma_f32_16x16x32_bf16 a[124:127], a[232:235], v[172:175], a[124:127]\n"
"v_exp_f32 v74, v74\n"
"v_mfma_f32_16x16x32_bf16 a[128:131], v[208:211], v[160:163], a[128:131]\n"
"v_exp_f32 v75, v75\n"
"v_mfma_f32_16x16x32_bf16 a[128:131], v[208:211], v[176:179], a[128:131]\n"
"v_exp_f32 v88, v88\n"
"v_mfma_f32_16x16x32_bf16 a[132:135], v[208:211], v[164:167], a[132:135]\n"
"buffer_load_dwordx4 v252, s[20:23], s80 offen lds\n"
"v_mfma_f32_16x16x32_bf16 a[132:135], v[208:211], v[180:183], a[132:135]\n"
"s_add_u32 m0, s81, 107520\n"
"v_exp_f32 v89, v89\n"
"v_mfma_f32_16x16x32_bf16 a[136:139], v[208:211], v[168:171], a[136:139]\n"
"v_exp_f32 v90, v90\n"
"v_mfma_f32_16x16x32_bf16 a[136:139], v[208:211], v[184:187], a[136:139]\n"
"v_exp_f32 v91, v91\n"
"v_mfma_f32_16x16x32_bf16 a[140:143], v[208:211], v[172:175], a[140:143]\n"
"v_exp_f32 v76, v76\n"
"v_mfma_f32_16x16x32_bf16 a[140:143], v[208:211], v[188:191], a[140:143]\n"
"v_exp_f32 v77, v77\n"
"v_mfma_f32_16x16x32_bf16 a[128:131], v[208:211], v[128:131], a[128:131]\n"
"buffer_load_dwordx4 v253, s[20:23], s80 offen lds\n"
"v_cvt_pk_bf16_f32 v160, v64, v65\n"
"v_mfma_f32_16x16x32_bf16 a[128:131], v[208:211], v[144:147], a[128:131]\n"
"s_add_u32 s80, s80, 221184\n"
"v_exp_f32 v78, v78\n"
"v_mfma_f32_16x16x32_bf16 a[132:135], v[208:211], v[132:135], a[132:135]\n"
"s_sub_u32 s86, s80, 2211840\n"
"s_min_u32 s80, s80, s86\n"
"v_exp_f32 v79, v79\n"
"v_mfma_f32_16x16x32_bf16 a[132:135], v[208:211], v[148:151], a[132:135]\n"
"v_exp_f32 v92, v92\n"
"v_mfma_f32_16x16x32_bf16 a[136:139], v[208:211], v[136:139], a[136:139]\n"
"v_exp_f32 v93, v93\n"
"v_mfma_f32_16x16x32_bf16 a[136:139], v[208:211], v[152:155], a[136:139]\n"
"v_exp_f32 v94, v94\n"
"v_mfma_f32_16x16x32_bf16 a[140:143], v[208:211], v[140:143], a[140:143]\n"
"buffer_load_dwordx4 a[176:179], v255, s[36:39], s77 offen offset:0 sc1\n"
"v_cvt_pk_bf16_f32 v161, v66, v67\n"
"v_mfma_f32_16x16x32_bf16 a[140:143], v[208:211], v[156:159], a[140:143]\n"
"s_waitcnt lgkmcnt(4)\n"
"v_exp_f32 v95, v95\n"
"v_mfma_f32_16x16x32_bf16 a[0:3], a[208:211], v[128:131], a[0:3]\n"
"ds_read_b64_tr_b16 a[224:225], v234 offset:0\n"
"v_cvt_pk_bf16_f32 v162, v80, v81\n"
"v_mfma_f32_16x16x32_bf16 a[0:3], a[212:215], v[144:147], a[0:3]\n"
"v_exp_f32 v96, v96\n"
"v_mfma_f32_16x16x32_bf16 a[4:7], a[212:215], v[148:151], a[4:7]\n"
"ds_read_b64_tr_b16 a[226:227], v234 offset:4096\n"
"v_cvt_pk_bf16_f32 v163, v82, v83\n"
"v_mfma_f32_16x16x32_bf16 a[4:7], a[208:211], v[132:135], a[4:7]\n"
"v_exp_f32 v97, v97\n"
"v_mfma_f32_16x16x32_bf16 a[8:11], a[208:211], v[136:139], a[8:11]\n"
"ds_read_b64_tr_b16 a[228:229], v234 offset:8192\n"
"buffer_load_dwordx4 a[180:183], v255, s[36:39], s77 offen offset:1024 sc1\n"
"v_mfma_f32_16x16x32_bf16 a[8:11], a[212:215], v[152:155], a[8:11]\n"
"v_exp_f32 v98, v98\n"
"v_mfma_f32_16x16x32_bf16 a[12:15], a[212:215], v[156:159], a[12:15]\n"
"ds_read_b64_tr_b16 a[230:231], v234 offset:12288\n"
"v_cvt_pk_bf16_f32 v164, v68, v69\n"
"v_mfma_f32_16x16x32_bf16 a[12:15], a[208:211], v[140:143], a[12:15]\n"
"s_waitcnt lgkmcnt(4)\n"
"v_exp_f32 v99, v99\n"
"v_mfma_f32_16x16x32_bf16 a[16:19], a[216:219], v[128:131], a[16:19]\n"
"ds_read_b64_tr_b16 a[232:233], v235 offset:0\n"
"v_cvt_pk_bf16_f32 v165, v70, v71\n"
"v_mfma_f32_16x16x32_bf16 a[16:19], a[220:223], v[144:147], a[16:19]\n"
"v_exp_f32 v112, v112\n"
"v_mfma_f32_16x16x32_bf16 a[20:23], a[220:223], v[148:151], a[20:23]\n"
"ds_read_b64_tr_b16 a[234:235], v235 offset:4096\n"
"buffer_load_dwordx4 a[184:187], v254, s[36:39], s77 offen offset:0 sc1\n"
"v_mfma_f32_16x16x32_bf16 a[20:23], a[216:219], v[132:135], a[20:23]\n"
"v_exp_f32 v113, v113\n"
"v_mfma_f32_16x16x32_bf16 a[24:27], a[216:219], v[136:139], a[24:27]\n"
"ds_read_b64_tr_b16 a[236:237], v235 offset:8192\n"
"v_exp_f32 v114, v114\n"
"v_mfma_f32_16x16x32_bf16 a[24:27], a[220:223], v[152:155], a[24:27]\n"
"v_exp_f32 v115, v115\n"
"v_mfma_f32_16x16x32_bf16 a[28:31], a[220:223], v[156:159], a[28:31]\n"
"ds_read_b64_tr_b16 a[238:239], v235 offset:12288\n"
"v_exp_f32 v100, v100\n"
"v_mfma_f32_16x16x32_bf16 a[28:31], a[216:219], v[140:143], a[28:31]\n"
"s_waitcnt lgkmcnt(4)\n"
"v_exp_f32 v101, v101\n"
"v_mfma_f32_16x16x32_bf16 a[32:35], a[224:227], v[128:131], a[32:35]\n"
"ds_read_b64_tr_b16 a[208:209], v232 offset:512\n"
"buffer_load_dwordx4 a[188:191], v254, s[36:39], s77 offen offset:1024 sc1\n"
"v_mfma_f32_16x16x32_bf16 a[32:35], a[228:231], v[144:147], a[32:35]\n"
"v_exp_f32 v102, v102\n"
"v_mfma_f32_16x16x32_bf16 a[36:39], a[228:231], v[148:151], a[36:39]\n"
"ds_read_b64_tr_b16 a[210:211], v232 offset:4608\n"
"v_cvt_pk_bf16_f32 v166, v84, v85\n"
"v_mfma_f32_16x16x32_bf16 a[36:39], a[224:227], v[132:135], a[36:39]\n"
"v_exp_f32 v103, v103\n"
"v_mfma_f32_16x16x32_bf16 a[40:43], a[224:227], v[136:139], a[40:43]\n"
"ds_read_b64_tr_b16 a[212:213], v232 offset:8704\n"
"v_cvt_pk_bf16_f32 v167, v86, v87\n"
"v_mfma_f32_16x16x32_bf16 a[40:43], a[228:231], v[152:155], a[40:43]\n"
"v_exp_f32 v116, v116\n"
"v_mfma_f32_16x16x32_bf16 a[44:47], a[228:231], v[156:159], a[44:47]\n"
"ds_read_b64_tr_b16 a[214:215], v232 offset:12800\n"
"buffer_load_dwordx4 a[192:195], v226, s[36:39], s77 offen offset:0 sc1\n"
"v_mfma_f32_16x16x32_bf16 a[44:47], a[224:227], v[140:143], a[44:47]\n"
"s_waitcnt lgkmcnt(4)\n"
"v_exp_f32 v117, v117\n"
"v_mfma_f32_16x16x32_bf16 a[48:51], a[232:235], v[128:131], a[48:51]\n"
"ds_read_b64_tr_b16 a[216:217], v233 offset:512\n"
"v_cvt_pk_bf16_f32 v168, v72, v73\n"
"v_mfma_f32_16x16x32_bf16 a[48:51], a[236:239], v[144:147], a[48:51]\n"
"v_exp_f32 v118, v118\n"
"v_mfma_f32_16x16x32_bf16 a[52:55], a[236:239], v[148:151], a[52:55]\n"
"ds_read_b64_tr_b16 a[218:219], v233 offset:4608\n"
"v_cvt_pk_bf16_f32 v169, v74, v75\n"
"v_mfma_f32_16x16x32_bf16 a[52:55], a[232:235], v[132:135], a[52:55]\n"
"v_exp_f32 v119, v119\n"
"v_mfma_f32_16x16x32_bf16 a[56:59], a[232:235], v[136:139], a[56:59]\n"
"ds_read_b64_tr_b16 a[220:221], v233 offset:8704\n"
"buffer_load_dwordx4 a[196:199], v226, s[36:39], s77 offen offset:1024 sc1\n"
"v_mfma_f32_16x16x32_bf16 a[56:59], a[236:239], v[152:155], a[56:59]\n"
"v_exp_f32 v104, v104\n"
"v_mfma_f32_16x16x32_bf16 a[60:63], a[236:239], v[156:159], a[60:63]\n"
"ds_read_b64_tr_b16 a[222:223], v233 offset:12800\n"
"v_cvt_pk_bf16_f32 v170, v88, v89\n"
"v_mfma_f32_16x16x32_bf16 a[60:63], a[232:235], v[140:143], a[60:63]\n"
"s_waitcnt lgkmcnt(4)\n"
"v_exp_f32 v105, v105\n"
"v_mfma_f32_16x16x32_bf16 a[64:67], a[208:211], v[128:131], a[64:67]\n"
"ds_read_b64_tr_b16 a[224:225], v234 offset:512\n"
"v_cvt_pk_bf16_f32 v171, v90, v91\n"
"v_mfma_f32_16x16x32_bf16 a[64:67], a[212:215], v[144:147], a[64:67]\n"
"ds_read_b64_tr_b16 a[226:227], v234 offset:4608\n"
"v_cvt_pk_bf16_f32 v172, v76, v77\n"
"v_mfma_f32_16x16x32_bf16 a[68:71], a[212:215], v[148:151], a[68:71]\n"
"buffer_load_dwordx4 a[200:203], v217, s[36:39], s77 offen offset:0 sc1\n"
"v_cvt_pk_bf16_f32 v173, v78, v79\n"
"v_mfma_f32_16x16x32_bf16 a[68:71], a[208:211], v[132:135], a[68:71]\n"
"ds_read_b64_tr_b16 a[228:229], v234 offset:8704\n"
"v_cvt_pk_bf16_f32 v174, v92, v93\n"
"v_mfma_f32_16x16x32_bf16 a[72:75], a[208:211], v[136:139], a[72:75]\n"
"ds_read_b64_tr_b16 a[230:231], v234 offset:12800\n"
"v_exp_f32 v106, v106\n"
"v_mfma_f32_16x16x32_bf16 a[72:75], a[212:215], v[152:155], a[72:75]\n"
"v_exp_f32 v107, v107\n"
"v_mfma_f32_16x16x32_bf16 a[76:79], a[212:215], v[156:159], a[76:79]\n"
"ds_read_b64_tr_b16 a[232:233], v235 offset:512\n"
"v_exp_f32 v120, v120\n"
"v_mfma_f32_16x16x32_bf16 a[76:79], a[208:211], v[140:143], a[76:79]\n"
"s_waitcnt lgkmcnt(5)\n"
"ds_read_b64_tr_b16 a[234:235], v235 offset:4608\n"
"v_cvt_pk_bf16_f32 v175, v94, v95\n"
"v_mfma_f32_16x16x32_bf16 a[80:83], a[216:219], v[128:131], a[80:83]\n"
"buffer_load_dwordx4 a[204:207], v217, s[36:39], s77 offen offset:1024 sc1\n"
"v_cvt_pk_bf16_f32 v176, v96, v97\n"
"v_mfma_f32_16x16x32_bf16 a[80:83], a[220:223], v[144:147], a[80:83]\n"
"s_add_u32 m0, s81, 32768\n"
"ds_read_b64_tr_b16 a[236:237], v235 offset:8704\n"
"v_cvt_pk_bf16_f32 v177, v98, v99\n"
"v_mfma_f32_16x16x32_bf16 a[84:87], a[220:223], v[148:151], a[84:87]\n"
"ds_read_b64_tr_b16 a[238:239], v235 offset:12800\n"
"v_cvt_pk_bf16_f32 v178, v112, v113\n"
"v_mfma_f32_16x16x32_bf16 a[84:87], a[216:219], v[132:135], a[84:87]\n"
"v_exp_f32 v121, v121\n"
"v_mfma_f32_16x16x32_bf16 a[88:91], a[216:219], v[136:139], a[88:91]\n"
"v_exp_f32 v122, v122\n"
"v_mfma_f32_16x16x32_bf16 a[88:91], a[220:223], v[152:155], a[88:91]\n"
"v_exp_f32 v123, v123\n"
"v_mfma_f32_16x16x32_bf16 a[92:95], a[220:223], v[156:159], a[92:95]\n"
"buffer_load_dwordx4 v252, s[16:19], s80 offen lds\n"
"v_cvt_pk_bf16_f32 v179, v114, v115\n"
"v_mfma_f32_16x16x32_bf16 a[92:95], a[216:219], v[140:143], a[92:95]\n"
"s_waitcnt lgkmcnt(0)\n"
"v_exp_f32 v108, v108\n"
"v_mfma_f32_16x16x32_bf16 a[96:99], a[224:227], v[128:131], a[96:99]\n"
"s_waitcnt vmcnt(26)\n"
"s_barrier\n"
"s_add_u32 m0, s81, 33792\n"
"v_exp_f32 v109, v109\n"
"v_mfma_f32_16x16x32_bf16 a[96:99], a[228:231], v[144:147], a[96:99]\n"
"ds_read_b64_tr_b16 a[208:209], v232 offset:16384\n"
"v_exp_f32 v110, v110\n"
"v_mfma_f32_16x16x32_bf16 a[100:103], a[228:231], v[148:151], a[100:103]\n"
"ds_read_b64_tr_b16 a[210:211], v232 offset:20480\n"
"v_cvt_pk_bf16_f32 v180, v100, v101\n"
"v_mfma_f32_16x16x32_bf16 a[100:103], a[224:227], v[132:135], a[100:103]\n"
"v_exp_f32 v111, v111\n"
"v_mfma_f32_16x16x32_bf16 a[104:107], a[224:227], v[136:139], a[104:107]\n"
"ds_read_b64_tr_b16 a[212:213], v232 offset:24576\n"
"buffer_load_dwordx4 v253, s[16:19], s80 offen lds\n"
"v_cvt_pk_bf16_f32 v189, v110, v111\n"
"v_mfma_f32_16x16x32_bf16 a[104:107], a[228:231], v[152:155], a[104:107]\n"
"s_add_u32 m0, s81, 40960\n"
"ds_read_b64_tr_b16 a[214:215], v232 offset:28672\n"
"v_cvt_pk_bf16_f32 v181, v102, v103\n"
"v_mfma_f32_16x16x32_bf16 a[108:111], a[228:231], v[156:159], a[108:111]\n"
"v_exp_f32 v124, v124\n"
"v_mfma_f32_16x16x32_bf16 a[108:111], a[224:227], v[140:143], a[108:111]\n"
"ds_read_b64_tr_b16 a[216:217], v233 offset:16384\n"
"v_cvt_pk_bf16_f32 v182, v116, v117\n"
"v_mfma_f32_16x16x32_bf16 a[112:115], a[232:235], v[128:131], a[112:115]\n"
"ds_read_b64_tr_b16 a[218:219], v233 offset:20480\n"
"v_cvt_pk_bf16_f32 v183, v118, v119\n"
"v_mfma_f32_16x16x32_bf16 a[112:115], a[236:239], v[144:147], a[112:115]\n"
"v_exp_f32 v125, v125\n"
"v_mfma_f32_16x16x32_bf16 a[116:119], a[236:239], v[148:151], a[116:119]\n"
"ds_read_b64_tr_b16 a[220:221], v233 offset:24576\n"
"buffer_load_dwordx4 v252, s[20:23], s80 offen lds\n"
"v_cvt_pk_bf16_f32 v190, v124, v125\n"
"v_mfma_f32_16x16x32_bf16 a[116:119], a[232:235], v[132:135], a[116:119]\n"
"s_add_u32 m0, s81, 41984\n"
"ds_read_b64_tr_b16 a[222:223], v233 offset:28672\n"
"v_cvt_pk_bf16_f32 v184, v104, v105\n"
"v_mfma_f32_16x16x32_bf16 a[120:123], a[232:235], v[136:139], a[120:123]\n"
"v_exp_f32 v126, v126\n"
"v_mfma_f32_16x16x32_bf16 a[120:123], a[236:239], v[152:155], a[120:123]\n"
"v_exp_f32 v127, v127\n"
"v_mfma_f32_16x16x32_bf16 a[124:127], a[236:239], v[156:159], a[124:127]\n"
"v_cvt_pk_bf16_f32 v185, v106, v107\n"
"v_cvt_pk_bf16_f32 v186, v120, v121\n"
"v_cvt_pk_bf16_f32 v191, v126, v127\n"
"v_mfma_f32_16x16x32_bf16 a[124:127], a[232:235], v[140:143], a[124:127]\n"
"v_cvt_pk_bf16_f32 v187, v122, v123\n"
"v_cvt_pk_bf16_f32 v188, v108, v109\n"
"v_mfma_f32_16x16x32_bf16 a[128:131], v[208:211], v[160:163], a[128:131]\n"
"buffer_load_dwordx4 v253, s[20:23], s80 offen lds\n"
"v_mfma_f32_16x16x32_bf16 a[128:131], v[208:211], v[176:179], a[128:131]\n"
"s_add_u32 s80, s80, 221184\n"
"v_mfma_f32_16x16x32_bf16 a[132:135], v[208:211], v[164:167], a[132:135]\n"
"s_sub_u32 s86, s80, 2211840\n"
"s_min_u32 s80, s80, s86\n"
"v_mfma_f32_16x16x32_bf16 a[132:135], v[208:211], v[180:183], a[132:135]\n"
"v_mfma_f32_16x16x32_bf16 a[136:139], v[208:211], v[168:171], a[136:139]\n"
"buffer_load_dwordx4 v[128:131], v255, s[36:39], s78 offen offset:0 sc1\n"
"v_mfma_f32_16x16x32_bf16 a[136:139], v[208:211], v[184:187], a[136:139]\n"
"v_mfma_f32_16x16x32_bf16 a[140:143], v[208:211], v[172:175], a[140:143]\n"
"v_mfma_f32_16x16x32_bf16 a[140:143], v[208:211], v[188:191], a[140:143]\n"
"s_waitcnt lgkmcnt(4)\n"
"v_mfma_f32_16x16x32_bf16 a[0:3], a[208:211], v[160:163], a[0:3]\n"
"ds_read_b64_tr_b16 a[224:225], v234 offset:16384\n"
"buffer_load_dwordx4 v[132:135], v255, s[36:39], s78 offen offset:1024 sc1\n"
"v_mfma_f32_16x16x32_bf16 a[0:3], a[212:215], v[176:179], a[0:3]\n"
"v_accvgpr_read_b32 v212, a128\n"
"v_accvgpr_read_b32 v213, a132\n"
"v_mfma_f32_16x16x32_bf16 a[4:7], a[212:215], v[180:183], a[4:7]\n"
"ds_read_b64_tr_b16 a[226:227], v234 offset:20480\n"
"v_accvgpr_read_b32 v214, a136\n"
"v_mfma_f32_16x16x32_bf16 a[4:7], a[208:211], v[164:167], a[4:7]\n"
"v_accvgpr_read_b32 v215, a140\n"
"v_subrev_u32 v216, 0x1f800000, v212\n"
"v_mfma_f32_16x16x32_bf16 a[8:11], a[208:211], v[168:171], a[8:11]\n"
"ds_read_b64_tr_b16 a[228:229], v234 offset:24576\n"
"buffer_load_dwordx4 v[136:139], v254, s[36:39], s78 offen offset:0 sc1\n"
"v_mfma_f32_16x16x32_bf16 a[8:11], a[212:215], v[184:187], a[8:11]\n"
"v_subrev_u32 v218, 0x1f800000, v213\n"
"v_max_u32 v216, v216, v218\n"
"v_mfma_f32_16x16x32_bf16 a[12:15], a[212:215], v[188:191], a[12:15]\n"
"ds_read_b64_tr_b16 a[230:231], v234 offset:28672\n"
"v_subrev_u32 v218, 0x1f800000, v214\n"
"v_mfma_f32_16x16x32_bf16 a[12:15], a[208:211], v[172:175], a[12:15]\n"
"s_waitcnt lgkmcnt(4)\n"
"v_max_u32 v216, v216, v218\n"
"v_subrev_u32 v218, 0x1f800000, v215\n"
"v_mfma_f32_16x16x32_bf16 a[16:19], a[216:219], v[160:163], a[16:19]\n"
"ds_read_b64_tr_b16 a[232:233], v235 offset:16384\n"
"buffer_load_dwordx4 v[140:143], v254, s[36:39], s78 offen offset:1024 sc1\n"
"v_mfma_f32_16x16x32_bf16 a[16:19], a[220:223], v[176:179], a[16:19]\n"
"v_max_u32 v216, v216, v218\n"
"v_cmp_lt_u32 s[10:11], 2.0, v216\n"
"v_mfma_f32_16x16x32_bf16 a[20:23], a[220:223], v[180:183], a[20:23]\n"
"s_or_b32 s10, s10, s11\n"
"s_lshl2_add_u32 s97, s68, 131072\n"
"ds_read_b64_tr_b16 a[234:235], v235 offset:20480\n"
"v_mfma_f32_16x16x32_bf16 a[20:23], a[216:219], v[164:167], a[20:23]\n"
"v_mov_b32 v216, s10\n"
"v_mov_b32 v218, s97\n"
"v_mfma_f32_16x16x32_bf16 a[24:27], a[216:219], v[168:171], a[24:27]\n"
"ds_read_b64_tr_b16 a[236:237], v235 offset:24576\n"
"buffer_load_dwordx4 v[144:147], v226, s[36:39], s78 offen offset:0 sc1\n"
"v_mfma_f32_16x16x32_bf16 a[24:27], a[220:223], v[184:187], a[24:27]\n"
"ds_write_b32 v218, v216\n"
"v_mov_b32 v218, 131072\n"
"v_mfma_f32_16x16x32_bf16 a[28:31], a[220:223], v[188:191], a[28:31]\n"
"ds_read_b64_tr_b16 a[238:239], v235 offset:28672\n"
"v_mfma_f32_16x16x32_bf16 a[28:31], a[216:219], v[172:175], a[28:31]\n"
"s_waitcnt lgkmcnt(5)\n"
"v_rcp_f32 v212, v212\n"
"v_mfma_f32_16x16x32_bf16 a[32:35], a[224:227], v[160:163], a[32:35]\n"
"ds_read_b64_tr_b16 a[208:209], v232 offset:16896\n"
"buffer_load_dwordx4 v[148:151], v226, s[36:39], s78 offen offset:1024 sc1\n"
"v_mfma_f32_16x16x32_bf16 a[32:35], a[228:231], v[176:179], a[32:35]\n"
"v_rcp_f32 v213, v213\n"
"v_mfma_f32_16x16x32_bf16 a[36:39], a[228:231], v[180:183], a[36:39]\n"
"ds_read_b64_tr_b16 a[210:211], v232 offset:20992\n"
"v_mfma_f32_16x16x32_bf16 a[36:39], a[224:227], v[164:167], a[36:39]\n"
"v_rcp_f32 v214, v214\n"
"v_mfma_f32_16x16x32_bf16 a[40:43], a[224:227], v[168:171], a[40:43]\n"
"ds_read_b64_tr_b16 a[212:213], v232 offset:25088\n"
"buffer_load_dwordx4 v[152:155], v217, s[36:39], s78 offen offset:0 sc1\n"
"v_mfma_f32_16x16x32_bf16 a[40:43], a[228:231], v[184:187], a[40:43]\n"
"v_rcp_f32 v215, v215\n"
"v_mfma_f32_16x16x32_bf16 a[44:47], a[228:231], v[188:191], a[44:47]\n"
"s_lshr_b32 s90, s68, 1\n"
"s_lshl1_add_u32 s90, s69, s90\n"
"ds_read_b64_tr_b16 a[214:215], v232 offset:29184\n"
"v_mfma_f32_16x16x32_bf16 a[44:47], a[224:227], v[172:175], a[44:47]\n"
"s_waitcnt lgkmcnt(4)\n"
"v_mfma_f32_16x16x32_bf16 a[48:51], a[232:235], v[160:163], a[48:51]\n"
"s_lshl_b32 s90, s90, 20\n"
"ds_read_b64_tr_b16 a[216:217], v233 offset:16896\n"
"buffer_load_dwordx4 v[156:159], v217, s[36:39], s78 offen offset:1024 sc1\n"
"v_mfma_f32_16x16x32_bf16 a[48:51], a[236:239], v[176:179], a[48:51]\n"
"s_lshl_b32 s91, s70, 15\n"
"v_mfma_f32_16x16x32_bf16 a[52:55], a[236:239], v[180:183], a[52:55]\n"
"s_add_u32 s90, s90, s91\n"
"s_and_b32 s91, s68, 1\n"
"ds_read_b64_tr_b16 a[218:219], v233 offset:20992\n"
"v_mfma_f32_16x16x32_bf16 a[52:55], a[232:235], v[164:167], a[52:55]\n"
"s_lshl_b32 s91, s91, 12\n"
"v_mfma_f32_16x16x32_bf16 a[56:59], a[232:235], v[168:171], a[56:59]\n"
"s_add_u32 s90, s90, s91\n"
"s_add_u32 s44, s90, 41943040\n"
"ds_read_b64_tr_b16 a[220:221], v233 offset:25088\n"
"v_mfma_f32_16x16x32_bf16 a[56:59], a[236:239], v[184:187], a[56:59]\n"
"v_accvgpr_read_b32 v192, a0\n"
"v_accvgpr_read_b32 v193, a1\n"
"v_mfma_f32_16x16x32_bf16 a[60:63], a[236:239], v[188:191], a[60:63]\n"
"ds_read_b64_tr_b16 a[222:223], v233 offset:29184\n"
"v_accvgpr_read_b32 v194, a2\n"
"v_mfma_f32_16x16x32_bf16 a[60:63], a[232:235], v[172:175], a[60:63]\n"
"s_waitcnt lgkmcnt(4)\n"
"v_accvgpr_read_b32 v195, a3\n"
"v_accvgpr_read_b32 v196, a16\n"
"v_mfma_f32_16x16x32_bf16 a[64:67], a[208:211], v[160:163], a[64:67]\n"
"ds_read_b64_tr_b16 a[224:225], v234 offset:16896\n"
"v_accvgpr_read_b32 v197, a17\n"
"v_mfma_f32_16x16x32_bf16 a[64:67], a[212:215], v[176:179], a[64:67]\n"
"ds_read_b64_tr_b16 a[226:227], v234 offset:20992\n"
"v_accvgpr_read_b32 v198, a18\n"
"v_mfma_f32_16x16x32_bf16 a[68:71], a[212:215], v[180:183], a[68:71]\n"
"v_accvgpr_read_b32 v199, a19\n"
"v_mul_f32 v192, v212, v192\n"
"v_mfma_f32_16x16x32_bf16 a[68:71], a[208:211], v[164:167], a[68:71]\n"
"ds_read_b64_tr_b16 a[228:229], v234 offset:25088\n"
"v_mul_f32 v193, v212, v193\n"
"v_mfma_f32_16x16x32_bf16 a[72:75], a[208:211], v[168:171], a[72:75]\n"
"ds_read_b64_tr_b16 a[230:231], v234 offset:29184\n"
"v_mul_f32 v194, v212, v194\n"
"v_mfma_f32_16x16x32_bf16 a[72:75], a[212:215], v[184:187], a[72:75]\n"
"v_mul_f32 v195, v212, v195\n"
"v_mul_f32 v196, v212, v196\n"
"v_mfma_f32_16x16x32_bf16 a[76:79], a[212:215], v[188:191], a[76:79]\n"
"ds_read_b64_tr_b16 a[232:233], v235 offset:16896\n"
"v_mul_f32 v197, v212, v197\n"
"v_mfma_f32_16x16x32_bf16 a[76:79], a[208:211], v[172:175], a[76:79]\n"
"s_waitcnt lgkmcnt(5)\n"
"ds_read_b64_tr_b16 a[234:235], v235 offset:20992\n"
"v_mul_f32 v198, v212, v198\n"
"v_mfma_f32_16x16x32_bf16 a[80:83], a[216:219], v[160:163], a[80:83]\n"
"v_mul_f32 v199, v212, v199\n"
"v_cvt_pk_bf16_f32 v200, v192, v193\n"
"v_mfma_f32_16x16x32_bf16 a[80:83], a[220:223], v[176:179], a[80:83]\n"
"ds_read_b64_tr_b16 a[236:237], v235 offset:25088\n"
"v_cvt_pk_bf16_f32 v201, v194, v195\n"
"v_mfma_f32_16x16x32_bf16 a[84:87], a[220:223], v[180:183], a[84:87]\n"
"ds_read_b64_tr_b16 a[238:239], v235 offset:29184\n"
"v_cvt_pk_bf16_f32 v202, v196, v197\n"
"v_mfma_f32_16x16x32_bf16 a[84:87], a[216:219], v[164:167], a[84:87]\n"
"v_cvt_pk_bf16_f32 v203, v198, v199\n"
"v_accvgpr_read_b32 v192, a4\n"
"v_mfma_f32_16x16x32_bf16 a[88:91], a[216:219], v[168:171], a[88:91]\n"
"buffer_store_dwordx4 v[200:203], v255, s[36:39], s44 offen offset:0 sc1\n"
"v_accvgpr_read_b32 v193, a5\n"
"v_mfma_f32_16x16x32_bf16 a[88:91], a[220:223], v[184:187], a[88:91]\n"
"v_accvgpr_read_b32 v194, a6\n"
"v_accvgpr_read_b32 v195, a7\n"
"v_mfma_f32_16x16x32_bf16 a[92:95], a[220:223], v[188:191], a[92:95]\n"
"v_accvgpr_read_b32 v196, a20\n"
"v_accvgpr_read_b32 v197, a21\n"
"v_mfma_f32_16x16x32_bf16 a[92:95], a[216:219], v[172:175], a[92:95]\n"
"s_waitcnt lgkmcnt(0)\n"
"v_accvgpr_read_b32 v198, a22\n"
"v_accvgpr_read_b32 v199, a23\n"
"v_mfma_f32_16x16x32_bf16 a[96:99], a[224:227], v[160:163], a[96:99]\n"
"s_nop 0\n"
"s_barrier\n"
"v_mul_f32 v192, v213, v192\n"
"v_mfma_f32_16x16x32_bf16 a[96:99], a[228:231], v[176:179], a[96:99]\n"
"ds_read_b128 v[220:223], v218\n"
"v_mul_f32 v193, v213, v193\n"
"v_mfma_f32_16x16x32_bf16 a[100:103], a[228:231], v[180:183], a[100:103]\n"
"ds_read_b64_tr_b16 a[208:209], v244 offset:0\n"
"v_mul_f32 v194, v213, v194\n"
"v_mfma_f32_16x16x32_bf16 a[100:103], a[224:227], v[164:167], a[100:103]\n"
"ds_read_b64_tr_b16 a[210:211], v244 offset:512\n"
"v_mul_f32 v195, v213, v195\n"
"v_mfma_f32_16x16x32_bf16 a[104:107], a[224:227], v[168:171], a[104:107]\n"
"ds_read_b64_tr_b16 a[212:213], v244 offset:4096\n"
"v_mul_f32 v196, v213, v196\n"
"v_mfma_f32_16x16x32_bf16 a[104:107], a[228:231], v[184:187], a[104:107]\n"
"ds_read_b64_tr_b16 a[214:215], v244 offset:4608\n"
"v_mul_f32 v197, v213, v197\n"
"v_mfma_f32_16x16x32_bf16 a[108:111], a[228:231], v[188:191], a[108:111]\n"
"ds_read_b64_tr_b16 a[216:217], v244 offset:8192\n"
"v_mul_f32 v198, v213, v198\n"
"v_mfma_f32_16x16x32_bf16 a[108:111], a[224:227], v[172:175], a[108:111]\n"
"ds_read_b64_tr_b16 a[218:219], v244 offset:8704\n"
"v_mul_f32 v199, v213, v199\n"
"v_mfma_f32_16x16x32_bf16 a[112:115], a[232:235], v[160:163], a[112:115]\n"
"s_waitcnt lgkmcnt(6)\n"
"ds_read_b64_tr_b16 a[220:221], v244 offset:12288\n"
"v_or3_b32 v220, v220, v221, v222\n"
"v_mfma_f32_16x16x32_bf16 a[112:115], a[236:239], v[176:179], a[112:115]\n"
"ds_read_b64_tr_b16 a[222:223], v244 offset:12800\n"
"v_or_b32 v220, v220, v223\n"
"v_mfma_f32_16x16x32_bf16 a[116:119], a[236:239], v[180:183], a[116:119]\n"
"v_readfirstlane_b32 s97, v220\n"
"v_cvt_pk_bf16_f32 v204, v192, v193\n"
"v_mfma_f32_16x16x32_bf16 a[116:119], a[232:235], v[164:167], a[116:119]\n"
"v_cvt_pk_bf16_f32 v205, v194, v195\n"
"v_cvt_pk_bf16_f32 v206, v196, v197\n"
"v_mfma_f32_16x16x32_bf16 a[120:123], a[232:235], v[168:171], a[120:123]\n"
"s_andn2_b32 s97, s97, s89\n"
"s_cbranch_scc1 .L_fallback\n"
"v_cvt_pk_bf16_f32 v207, v198, v199\n"
"v_mfma_f32_16x16x32_bf16 a[120:123], a[236:239], v[184:187], a[120:123]\n"
"v_mfma_f32_16x16x32_bf16 a[124:127], a[236:239], v[188:191], a[124:127]\n"
"buffer_store_dwordx4 v[204:207], v255, s[36:39], s44 offen offset:1024 sc1\n"
"v_mfma_f32_16x16x32_bf16 a[124:127], a[232:235], v[172:175], a[124:127]\n"
"s_cmp_eq_u32 s88, 0\n"
"s_cbranch_scc1 .L_nogu\n"
"s_waitcnt vmcnt(27) lgkmcnt(0)\n"
"v_mfma_f32_16x16x32_bf16 v[0:3], a[208:211], a[144:147], 0\n"
"s_add_u32 s76, s79, s58\n"
"s_add_u32 s58, s58, 2048\n"
"ds_read_b64_tr_b16 a[224:225], v245 offset:0\n"
"v_mfma_f32_16x16x32_bf16 v[0:3], a[212:215], a[148:151], v[0:3]\n"
"s_sub_u32 s98, s58, 20480\n"
"s_min_u32 s58, s58, s98\n"
"ds_read_b64_tr_b16 a[226:227], v245 offset:512\n"
"v_mfma_f32_16x16x32_bf16 v[4:7], a[212:215], a[156:159], 0\n"
"buffer_load_dwordx4 v[160:163], v255, s[36:39], s76 offen offset:0 sc1\n"
"v_mfma_f32_16x16x32_bf16 v[4:7], a[208:211], a[152:155], v[4:7]\n"
"ds_read_b64_tr_b16 a[228:229], v245 offset:4096\n"
"v_mfma_f32_16x16x32_bf16 v[8:11], a[208:211], a[160:163], 0\n"
"ds_read_b64_tr_b16 a[230:231], v245 offset:4608\n"
"v_mfma_f32_16x16x32_bf16 v[8:11], a[212:215], a[164:167], v[8:11]\n"
"v_mfma_f32_16x16x32_bf16 v[12:15], a[212:215], a[172:175], 0\n"
"ds_read_b64_tr_b16 a[232:233], v245 offset:8192\n"
"v_mfma_f32_16x16x32_bf16 v[12:15], a[208:211], a[168:171], v[12:15]\n"
"v_mfma_f32_16x16x32_bf16 v[64:67], a[216:219], a[144:147], 0\n"
"ds_read_b64_tr_b16 a[234:235], v245 offset:8704\n"
"v_accvgpr_read_b32 v192, a8\n"
"v_mfma_f32_16x16x32_bf16 v[64:67], a[220:223], a[148:151], v[64:67]\n"
"ds_read_b64_tr_b16 a[236:237], v245 offset:12288\n"
"v_accvgpr_read_b32 v193, a9\n"
"v_mfma_f32_16x16x32_bf16 v[68:71], a[220:223], a[156:159], 0\n"
"buffer_load_dwordx4 v[164:167], v255, s[36:39], s76 offen offset:1024 sc1\n"
"v_accvgpr_read_b32 v194, a10\n"
"v_mfma_f32_16x16x32_bf16 v[68:71], a[216:219], a[152:155], v[68:71]\n"
"ds_read_b64_tr_b16 a[238:239], v245 offset:12800\n"
"v_accvgpr_read_b32 v195, a11\n"
"v_mfma_f32_16x16x32_bf16 v[72:75], a[216:219], a[160:163], 0\n"
"v_accvgpr_read_b32 v196, a24\n"
"v_mfma_f32_16x16x32_bf16 v[72:75], a[220:223], a[164:167], v[72:75]\n"
"v_accvgpr_read_b32 v197, a25\n"
"v_mfma_f32_16x16x32_bf16 v[76:79], a[220:223], a[172:175], 0\n"
"v_accvgpr_read_b32 v198, a26\n"
"v_mfma_f32_16x16x32_bf16 v[76:79], a[216:219], a[168:171], v[76:79]\n"
"s_waitcnt lgkmcnt(4)\n"
"v_accvgpr_read_b32 v199, a27\n"
"v_mfma_f32_16x16x32_bf16 v[16:19], a[224:227], a[144:147], 0\n"
"ds_read_b64_tr_b16 a[208:209], v246 offset:0\n"
"v_mul_f32 v192, v214, v192\n"
"v_mfma_f32_16x16x32_bf16 v[16:19], a[228:231], a[148:151], v[16:19]\n"
"ds_read_b64_tr_b16 a[210:211], v246 offset:512\n"
"v_mfma_f32_16x16x32_bf16 v[20:23], a[228:231], a[156:159], 0\n"
"buffer_load_dwordx4 v[168:171], v254, s[36:39], s76 offen offset:0 sc1\n"
"v_mul_f32 v193, v214, v193\n"
"v_mfma_f32_16x16x32_bf16 v[20:23], a[224:227], a[152:155], v[20:23]\n"
"ds_read_b64_tr_b16 a[212:213], v246 offset:4096\n"
"v_mul_f32 v194, v214, v194\n"
"v_mfma_f32_16x16x32_bf16 v[24:27], a[224:227], a[160:163], 0\n"
"ds_read_b64_tr_b16 a[214:215], v246 offset:4608\n"
"v_mul_f32 v195, v214, v195\n"
"v_mfma_f32_16x16x32_bf16 v[24:27], a[228:231], a[164:167], v[24:27]\n"
"v_mul_f32 v196, v214, v196\n"
"v_mfma_f32_16x16x32_bf16 v[28:31], a[228:231], a[172:175], 0\n"
"ds_read_b64_tr_b16 a[216:217], v246 offset:8192\n"
"v_mfma_f32_16x16x32_bf16 v[28:31], a[224:227], a[168:171], v[28:31]\n"
"s_waitcnt lgkmcnt(5)\n"
"v_mul_f32 v197, v214, v197\n"
"v_mfma_f32_16x16x32_bf16 v[80:83], a[232:235], a[144:147], 0\n"
"ds_read_b64_tr_b16 a[218:219], v246 offset:8704\n"
"v_mul_f32 v198, v214, v198\n"
"v_mfma_f32_16x16x32_bf16 v[80:83], a[236:239], a[148:151], v[80:83]\n"
"ds_read_b64_tr_b16 a[220:221], v246 offset:12288\n"
"v_mul_f32 v199, v214, v199\n"
"v_mfma_f32_16x16x32_bf16 v[84:87], a[236:239], a[156:159], 0\n"
"buffer_load_dwordx4 v[172:175], v254, s[36:39], s76 offen offset:1024 sc1\n"
"v_cvt_pk_bf16_f32 v208, v192, v193\n"
"v_mfma_f32_16x16x32_bf16 v[84:87], a[232:235], a[152:155], v[84:87]\n"
"ds_read_b64_tr_b16 a[222:223], v246 offset:12800\n"
"v_mfma_f32_16x16x32_bf16 v[88:91], a[232:235], a[160:163], 0\n"
"v_cvt_pk_bf16_f32 v209, v194, v195\n"
"v_mfma_f32_16x16x32_bf16 v[88:91], a[236:239], a[164:167], v[88:91]\n"
"v_cvt_pk_bf16_f32 v210, v196, v197\n"
"v_mfma_f32_16x16x32_bf16 v[92:95], a[236:239], a[172:175], 0\n"
"v_cvt_pk_bf16_f32 v211, v198, v199\n"
"v_mfma_f32_16x16x32_bf16 v[92:95], a[232:235], a[168:171], v[92:95]\n"
"s_waitcnt lgkmcnt(4)\n"
"buffer_store_dwordx4 v[208:211], v255, s[36:39], s44 offen offset:2048 sc1\n"
"v_mfma_f32_16x16x32_bf16 v[32:35], a[208:211], a[144:147], 0\n"
"ds_read_b64_tr_b16 a[224:225], v247 offset:0\n"
"v_mfma_f32_16x16x32_bf16 v[32:35], a[212:215], a[148:151], v[32:35]\n"
"ds_read_b64_tr_b16 a[226:227], v247 offset:512\n"
"v_accvgpr_read_b32 v192, a12\n"
"v_mfma_f32_16x16x32_bf16 v[36:39], a[212:215], a[156:159], 0\n"
"ds_read_b64_tr_b16 a[228:229], v247 offset:4096\n"
"buffer_load_dwordx4 v[176:179], v226, s[36:39], s76 offen offset:0 sc1\n"
"v_mfma_f32_16x16x32_bf16 v[36:39], a[208:211], a[152:155], v[36:39]\n"
"ds_read_b64_tr_b16 a[230:231], v247 offset:4608\n"
"v_accvgpr_read_b32 v193, a13\n"
"v_mfma_f32_16x16x32_bf16 v[40:43], a[208:211], a[160:163], 0\n"
"ds_read_b64_tr_b16 a[232:233], v247 offset:8192\n"
"v_accvgpr_read_b32 v194, a14\n"
"v_mfma_f32_16x16x32_bf16 v[40:43], a[212:215], a[164:167], v[40:43]\n"
"ds_read_b64_tr_b16 a[234:235], v247 offset:8704\n"
"v_accvgpr_read_b32 v195, a15\n"
"v_mfma_f32_16x16x32_bf16 v[44:47], a[212:215], a[172:175], 0\n"
"ds_read_b64_tr_b16 a[236:237], v247 offset:12288\n"
"v_accvgpr_read_b32 v196, a28\n"
"v_mfma_f32_16x16x32_bf16 v[44:47], a[208:211], a[168:171], v[44:47]\n"
"s_waitcnt lgkmcnt(7)\n"
"ds_read_b64_tr_b16 a[238:239], v247 offset:12800\n"
"v_accvgpr_read_b32 v197, a29\n"
"v_mfma_f32_16x16x32_bf16 v[96:99], a[216:219], a[144:147], 0\n"
"v_accvgpr_read_b32 v198, a30\n"
"v_mfma_f32_16x16x32_bf16 v[96:99], a[220:223], a[148:151], v[96:99]\n"
"v_accvgpr_read_b32 v199, a31\n"
"v_mfma_f32_16x16x32_bf16 v[100:103], a[220:223], a[156:159], 0\n"
"buffer_load_dwordx4 v[180:183], v226, s[36:39], s76 offen offset:1024 sc1\n"
"v_mfma_f32_16x16x32_bf16 v[100:103], a[216:219], a[152:155], v[100:103]\n"
"v_mul_f32 v192, v215, v192\n"
"v_mfma_f32_16x16x32_bf16 v[104:107], a[216:219], a[160:163], 0\n"
"v_mul_f32 v193, v215, v193\n"
"v_mfma_f32_16x16x32_bf16 v[104:107], a[220:223], a[164:167], v[104:107]\n"
"v_mul_f32 v194, v215, v194\n"
"v_mfma_f32_16x16x32_bf16 v[108:111], a[220:223], a[172:175], 0\n"
"v_mul_f32 v195, v215, v195\n"
"v_mfma_f32_16x16x32_bf16 v[108:111], a[216:219], a[168:171], v[108:111]\n"
"s_waitcnt lgkmcnt(0)\n"
"v_mfma_f32_16x16x32_bf16 v[48:51], a[224:227], a[144:147], 0\n"
"s_nop 0\n"
"s_barrier\n"
"v_mul_f32 v196, v215, v196\n"
"v_mfma_f32_16x16x32_bf16 v[48:51], a[228:231], a[148:151], v[48:51]\n"
"ds_read_b64_tr_b16 a[208:209], v244 offset:16384\n"
"v_mul_f32 v197, v215, v197\n"
"v_mfma_f32_16x16x32_bf16 v[52:55], a[228:231], a[156:159], 0\n"
"ds_read_b64_tr_b16 a[210:211], v244 offset:16896\n"
"buffer_load_dwordx4 v[184:187], v217, s[36:39], s76 offen offset:0 sc1\n"
"v_mfma_f32_16x16x32_bf16 v[52:55], a[224:227], a[152:155], v[52:55]\n"
"v_mul_f32 v198, v215, v198\n"
"v_mul_f32 v199, v215, v199\n"
"v_mfma_f32_16x16x32_bf16 v[56:59], a[224:227], a[160:163], 0\n"
"ds_read_b64_tr_b16 a[212:213], v244 offset:20480\n"
"v_mfma_f32_16x16x32_bf16 v[56:59], a[228:231], a[164:167], v[56:59]\n"
"ds_read_b64_tr_b16 a[214:215], v244 offset:20992\n"
"v_cvt_pk_bf16_f32 v200, v192, v193\n"
"v_mfma_f32_16x16x32_bf16 v[60:63], a[228:231], a[172:175], 0\n"
"v_cvt_pk_bf16_f32 v201, v194, v195\n"
"v_mfma_f32_16x16x32_bf16 v[60:63], a[224:227], a[168:171], v[60:63]\n"
"ds_read_b64_tr_b16 a[216:217], v244 offset:24576\n"
"v_cvt_pk_bf16_f32 v202, v196, v197\n"
"v_mfma_f32_16x16x32_bf16 v[112:115], a[232:235], a[144:147], 0\n"
"ds_read_b64_tr_b16 a[218:219], v244 offset:25088\n"
"v_cvt_pk_bf16_f32 v203, v198, v199\n"
"v_mfma_f32_16x16x32_bf16 v[112:115], a[236:239], a[148:151], v[112:115]\n"
"v_mfma_f32_16x16x32_bf16 v[116:119], a[236:239], a[156:159], 0\n"
".L_pad9:\n"
"s_cbranch_execz .L_pad13\n"
"ds_read_b64_tr_b16 a[220:221], v244 offset:28672\n"
"buffer_load_dwordx4 v[188:191], v217, s[36:39], s76 offen offset:1024 sc1\n"
"v_mfma_f32_16x16x32_bf16 v[116:119], a[232:235], a[152:155], v[116:119]\n"
"ds_read_b64_tr_b16 a[222:223], v244 offset:29184\n"
"buffer_store_dwordx4 v[200:203], v255, s[36:39], s44 offen offset:3072 sc1\n"
"v_mfma_f32_16x16x32_bf16 v[120:123], a[232:235], a[160:163], 0\n"
"s_add_u32 s44, s44, 8192\n"
"v_accvgpr_read_b32 v192, a32\n"
"v_accvgpr_read_b32 v193, a33\n"
"v_mfma_f32_16x16x32_bf16 v[120:123], a[236:239], a[164:167], v[120:123]\n"
"v_mfma_f32_16x16x32_bf16 v[124:127], a[236:239], a[172:175], 0\n"
"s_waitcnt vmcnt(24)\n"
"v_accvgpr_read_b32 v194, a34\n"
"v_mfma_f32_16x16x32_bf16 v[124:127], a[232:235], a[168:171], v[124:127]\n"
"s_waitcnt lgkmcnt(4)\n"
"v_accvgpr_read_b32 v195, a35\n"
"v_mfma_f32_16x16x32_bf16 v[0:3], a[208:211], a[176:179], v[0:3]\n"
"ds_read_b64_tr_b16 a[224:225], v245 offset:16384\n"
"v_accvgpr_read_b32 v196, a48\n"
"v_mfma_f32_16x16x32_bf16 v[0:3], a[212:215], a[180:183], v[0:3]\n"
"ds_read_b64_tr_b16 a[226:227], v245 offset:16896\n"
"v_accvgpr_read_b32 v197, a49\n"
"v_mfma_f32_16x16x32_bf16 v[4:7], a[212:215], a[188:191], v[4:7]\n"
"v_mfma_f32_16x16x32_bf16 v[4:7], a[208:211], a[184:187], v[4:7]\n"
"ds_read_b64_tr_b16 a[228:229], v245 offset:20480\n"
"v_accvgpr_read_b32 v198, a50\n"
"v_mfma_f32_16x16x32_bf16 v[8:11], a[208:211], a[192:195], v[8:11]\n"
"ds_read_b64_tr_b16 a[230:231], v245 offset:20992\n"
"v_accvgpr_read_b32 v199, a51\n"
"v_mfma_f32_16x16x32_bf16 v[8:11], a[212:215], a[196:199], v[8:11]\n"
"v_mul_f32 v192, v212, v192\n"
"v_mfma_f32_16x16x32_bf16 v[12:15], a[212:215], a[204:207], v[12:15]\n"
"ds_read_b64_tr_b16 a[232:233], v245 offset:24576\n"
"v_mul_f32 v193, v212, v193\n"
"v_mfma_f32_16x16x32_bf16 v[12:15], a[208:211], a[200:203], v[12:15]\n"
"s_waitcnt lgkmcnt(5)\n"
"v_mfma_f32_16x16x32_bf16 v[64:67], a[216:219], a[176:179], v[64:67]\n"
"s_add_u32 s76, s79, s58\n"
"s_add_u32 s58, s58, 2048\n"
"ds_read_b64_tr_b16 a[234:235], v245 offset:25088\n"
"v_mfma_f32_16x16x32_bf16 v[64:67], a[220:223], a[180:183], v[64:67]\n"
"s_sub_u32 s98, s58, 20480\n"
"s_min_u32 s58, s58, s98\n"
"ds_read_b64_tr_b16 a[236:237], v245 offset:28672\n"
"v_mfma_f32_16x16x32_bf16 v[68:71], a[220:223], a[188:191], v[68:71]\n"
"buffer_load_dwordx4 a[0:3], v255, s[36:39], s76 offen offset:0 sc1\n"
"v_mul_f32 v194, v212, v194\n"
"v_mfma_f32_16x16x32_bf16 v[68:71], a[216:219], a[184:187], v[68:71]\n"
"ds_read_b64_tr_b16 a[238:239], v245 offset:29184\n"
"v_mul_f32 v195, v212, v195\n"
"v_mfma_f32_16x16x32_bf16 v[72:75], a[216:219], a[192:195], v[72:75]\n"
"v_mul_f32 v196, v212, v196\n"
"v_mul_f32 v197, v212, v197\n"
"v_mfma_f32_16x16x32_bf16 v[72:75], a[220:223], a[196:199], v[72:75]\n"
"v_mul_f32 v198, v212, v198\n"
"v_mfma_f32_16x16x32_bf16 v[76:79], a[220:223], a[204:207], v[76:79]\n"
"v_mul_f32 v199, v212, v199\n"
"v_mfma_f32_16x16x32_bf16 v[76:79], a[216:219], a[200:203], v[76:79]\n"
"s_waitcnt lgkmcnt(4)\n"
"v_cvt_pk_bf16_f32 v204, v192, v193\n"
"v_mfma_f32_16x16x32_bf16 v[16:19], a[224:227], a[176:179], v[16:19]\n"
"ds_read_b64_tr_b16 a[208:209], v246 offset:16384\n"
"v_cvt_pk_bf16_f32 v205, v194, v195\n"
"v_mfma_f32_16x16x32_bf16 v[16:19], a[228:231], a[180:183], v[16:19]\n"
"ds_read_b64_tr_b16 a[210:211], v246 offset:16896\n"
"v_mfma_f32_16x16x32_bf16 v[20:23], a[228:231], a[188:191], v[20:23]\n"
"buffer_load_dwordx4 a[4:7], v255, s[36:39], s76 offen offset:1024 sc1\n"
"v_cvt_pk_bf16_f32 v206, v196, v197\n"
"v_mfma_f32_16x16x32_bf16 v[20:23], a[224:227], a[184:187], v[20:23]\n"
"ds_read_b64_tr_b16 a[212:213], v246 offset:20480\n"
"v_cvt_pk_bf16_f32 v207, v198, v199\n"
"v_mfma_f32_16x16x32_bf16 v[24:27], a[224:227], a[192:195], v[24:27]\n"
"ds_read_b64_tr_b16 a[214:215], v246 offset:20992\n"
"buffer_store_dwordx4 v[204:207], v255, s[36:39], s44 offen offset:0 sc1\n"
"v_mfma_f32_16x16x32_bf16 v[24:27], a[228:231], a[196:199], v[24:27]\n"
"v_accvgpr_read_b32 v192, a36\n"
"v_mfma_f32_16x16x32_bf16 v[28:31], a[228:231], a[204:207], v[28:31]\n"
"ds_read_b64_tr_b16 a[216:217], v246 offset:24576\n"
"v_mfma_f32_16x16x32_bf16 v[28:31], a[224:227], a[200:203], v[28:31]\n"
"s_waitcnt lgkmcnt(5)\n"
"v_accvgpr_read_b32 v193, a37\n"
"v_mfma_f32_16x16x32_bf16 v[80:83], a[232:235], a[176:179], v[80:83]\n"
"ds_read_b64_tr_b16 a[218:219], v246 offset:25088\n"
"v_accvgpr_read_b32 v194, a38\n"
"v_mfma_f32_16x16x32_bf16 v[80:83], a[236:239], a[180:183], v[80:83]\n"
"ds_read_b64_tr_b16 a[220:221], v246 offset:28672\n"
"v_accvgpr_read_b32 v195, a39\n"
"v_mfma_f32_16x16x32_bf16 v[84:87], a[236:239], a[188:191], v[84:87]\n"
"buffer_load_dwordx4 a[8:11], v254, s[36:39], s76 offen offset:0 sc1\n"
"v_accvgpr_read_b32 v196, a52\n"
"v_mfma_f32_16x16x32_bf16 v[84:87], a[232:235], a[184:187], v[84:87]\n"
"ds_read_b64_tr_b16 a[222:223], v246 offset:29184\n"
"v_mfma_f32_16x16x32_bf16 v[88:91], a[232:235], a[192:195], v[88:91]\n"
"v_accvgpr_read_b32 v197, a53\n"
"v_mfma_f32_16x16x32_bf16 v[88:91], a[236:239], a[196:199], v[88:91]\n"
"v_accvgpr_read_b32 v198, a54\n"
"v_mfma_f32_16x16x32_bf16 v[92:95], a[236:239], a[204:207], v[92:95]\n"
"v_accvgpr_read_b32 v199, a55\n"
"v_mfma_f32_16x16x32_bf16 v[92:95], a[232:235], a[200:203], v[92:95]\n"
"s_waitcnt lgkmcnt(4)\n"
"v_mul_f32 v192, v213, v192\n"
"v_mfma_f32_16x16x32_bf16 v[32:35], a[208:211], a[176:179], v[32:35]\n"
"ds_read_b64_tr_b16 a[224:225], v247 offset:16384\n"
"v_mfma_f32_16x16x32_bf16 v[32:35], a[212:215], a[180:183], v[32:35]\n"
"ds_read_b64_tr_b16 a[226:227], v247 offset:16896\n"
"v_mul_f32 v193, v213, v193\n"
"v_mfma_f32_16x16x32_bf16 v[36:39], a[212:215], a[188:191], v[36:39]\n"
"ds_read_b64_tr_b16 a[228:229], v247 offset:20480\n"
"buffer_load_dwordx4 a[12:15], v254, s[36:39], s76 offen offset:1024 sc1\n"
"v_mfma_f32_16x16x32_bf16 v[36:39], a[208:211], a[184:187], v[36:39]\n"
"ds_read_b64_tr_b16 a[230:231], v247 offset:20992\n"
"v_mul_f32 v194, v213, v194\n"
"v_mfma_f32_16x16x32_bf16 v[40:43], a[208:211], a[192:195], v[40:43]\n"
"ds_read_b64_tr_b16 a[232:233], v247 offset:24576\n"
"v_mul_f32 v195, v213, v195\n"
"v_mfma_f32_16x16x32_bf16 v[40:43], a[212:215], a[196:199], v[40:43]\n"
"ds_read_b64_tr_b16 a[234:235], v247 offset:25088\n"
"v_mul_f32 v196, v213, v196\n"
"v_mfma_f32_16x16x32_bf16 v[44:47], a[212:215], a[204:207], v[44:47]\n"
"ds_read_b64_tr_b16 a[236:237], v247 offset:28672\n"
"v_mul_f32 v197, v213, v197\n"
"v_mfma_f32_16x16x32_bf16 v[44:47], a[208:211], a[200:203], v[44:47]\n"
"s_waitcnt lgkmcnt(7)\n"
"ds_read_b64_tr_b16 a[238:239], v247 offset:29184\n"
"v_mul_f32 v198, v213, v198\n"
"v_mfma_f32_16x16x32_bf16 v[96:99], a[216:219], a[176:179], v[96:99]\n"
"v_mul_f32 v199, v213, v199\n"
"v_mfma_f32_16x16x32_bf16 v[96:99], a[220:223], a[180:183], v[96:99]\n"
"v_cvt_pk_bf16_f32 v208, v192, v193\n"
"v_mfma_f32_16x16x32_bf16 v[100:103], a[220:223], a[188:191], v[100:103]\n"
"buffer_load_dwordx4 a[16:19], v226, s[36:39], s76 offen offset:0 sc1\n"
"v_mfma_f32_16x16x32_bf16 v[100:103], a[216:219], a[184:187], v[100:103]\n"
"v_cvt_pk_bf16_f32 v209, v194, v195\n"
"v_mfma_f32_16x16x32_bf16 v[104:107], a[216:219], a[192:195], v[104:107]\n"
"v_cvt_pk_bf16_f32 v210, v196, v197\n"
"v_mfma_f32_16x16x32_bf16 v[104:107], a[220:223], a[196:199], v[104:107]\n"
"v_cvt_pk_bf16_f32 v211, v198, v199\n"
"v_mfma_f32_16x16x32_bf16 v[108:111], a[220:223], a[204:207], v[108:111]\n"
"buffer_store_dwordx4 v[208:211], v255, s[36:39], s44 offen offset:1024 sc1\n"
"v_mfma_f32_16x16x32_bf16 v[108:111], a[216:219], a[200:203], v[108:111]\n"
"s_waitcnt lgkmcnt(0)\n"
"v_mfma_f32_16x16x32_bf16 v[48:51], a[224:227], a[176:179], v[48:51]\n"
"s_nop 0\n"
"s_barrier\n"
"v_accvgpr_read_b32 v192, a40\n"
"v_mfma_f32_16x16x32_bf16 v[48:51], a[228:231], a[180:183], v[48:51]\n"
"ds_read_b64_tr_b16 a[208:209], v244 offset:49152\n"
"v_accvgpr_read_b32 v193, a41\n"
"v_mfma_f32_16x16x32_bf16 v[52:55], a[228:231], a[188:191], v[52:55]\n"
"ds_read_b64_tr_b16 a[210:211], v244 offset:49664\n"
"buffer_load_dwordx4 a[20:23], v226, s[36:39], s76 offen offset:1024 sc1\n"
"v_mfma_f32_16x16x32_bf16 v[52:55], a[224:227], a[184:187], v[52:55]\n"
"v_accvgpr_read_b32 v194, a42\n"
"v_accvgpr_read_b32 v195, a43\n"
"v_mfma_f32_16x16x32_bf16 v[56:59], a[224:227], a[192:195], v[56:59]\n"
"ds_read_b64_tr_b16 a[212:213], v244 offset:53248\n"
"v_mfma_f32_16x16x32_bf16 v[56:59], a[228:231], a[196:199], v[56:59]\n"
"ds_read_b64_tr_b16 a[214:215], v244 offset:53760\n"
"v_accvgpr_read_b32 v196, a56\n"
"v_mfma_f32_16x16x32_bf16 v[60:63], a[228:231], a[204:207], v[60:63]\n"
"v_accvgpr_read_b32 v197, a57\n"
"v_mfma_f32_16x16x32_bf16 v[60:63], a[224:227], a[200:203], v[60:63]\n"
"ds_read_b64_tr_b16 a[216:217], v244 offset:57344\n"
"v_accvgpr_read_b32 v198, a58\n"
"v_mfma_f32_16x16x32_bf16 v[112:115], a[232:235], a[176:179], v[112:115]\n"
"ds_read_b64_tr_b16 a[218:219], v244 offset:57856\n"
"v_accvgpr_read_b32 v199, a59\n"
"v_mfma_f32_16x16x32_bf16 v[112:115], a[236:239], a[180:183], v[112:115]\n"
"v_mfma_f32_16x16x32_bf16 v[116:119], a[236:239], a[188:191], v[116:119]\n"
"ds_read_b64_tr_b16 a[220:221], v244 offset:61440\n"
"buffer_load_dwordx4 a[24:27], v217, s[36:39], s76 offen offset:0 sc1\n"
"v_mfma_f32_16x16x32_bf16 v[116:119], a[232:235], a[184:187], v[116:119]\n"
"ds_read_b64_tr_b16 a[222:223], v244 offset:61952\n"
"v_mul_f32 v192, v214, v192\n"
"v_mfma_f32_16x16x32_bf16 v[120:123], a[232:235], a[192:195], v[120:123]\n"
"v_mul_f32 v193, v214, v193\n"
"v_mul_f32 v194, v214, v194\n"
"v_mfma_f32_16x16x32_bf16 v[120:123], a[236:239], a[196:199], v[120:123]\n"
"v_mul_f32 v195, v214, v195\n"
"v_mfma_f32_16x16x32_bf16 v[124:127], a[236:239], a[204:207], v[124:127]\n"
"s_waitcnt vmcnt(21)\n"
"v_mfma_f32_16x16x32_bf16 v[124:127], a[232:235], a[200:203], v[124:127]\n"
"s_waitcnt lgkmcnt(4)\n"
"v_mul_f32 v196, v214, v196\n"
"v_mfma_f32_16x16x32_bf16 v[0:3], a[208:211], v[128:131], v[0:3]\n"
"ds_read_b64_tr_b16 a[224:225], v245 offset:49152\n"
"v_mul_f32 v197, v214, v197\n"
"v_mfma_f32_16x16x32_bf16 v[0:3], a[212:215], v[132:135], v[0:3]\n"
"ds_read_b64_tr_b16 a[226:227], v245 offset:49664\n"
"v_mul_f32 v198, v214, v198\n"
"v_mfma_f32_16x16x32_bf16 v[4:7], a[212:215], v[140:143], v[4:7]\n"
"buffer_load_dwordx4 a[28:31], v217, s[36:39], s76 offen offset:1024 sc1\n"
"v_mul_f32 v199, v214, v199\n"
"v_mfma_f32_16x16x32_bf16 v[4:7], a[208:211], v[136:139], v[4:7]\n"
"s_add_u32 s76, s79, s58\n"
"s_add_u32 s58, s58, 2048\n"
"ds_read_b64_tr_b16 a[228:229], v245 offset:53248\n"
"v_mfma_f32_16x16x32_bf16 v[8:11], a[208:211], v[144:147], v[8:11]\n"
"s_sub_u32 s98, s58, 20480\n"
"s_min_u32 s58, s58, s98\n"
"ds_read_b64_tr_b16 a[230:231], v245 offset:53760\n"
"v_mfma_f32_16x16x32_bf16 v[8:11], a[212:215], v[148:151], v[8:11]\n"
"v_cvt_pk_bf16_f32 v200, v192, v193\n"
"v_cvt_pk_bf16_f32 v201, v194, v195\n"
"v_mfma_f32_16x16x32_bf16 v[12:15], a[212:215], v[156:159], v[12:15]\n"
"ds_read_b64_tr_b16 a[232:233], v245 offset:57344\n"
"v_cvt_pk_bf16_f32 v202, v196, v197\n"
"v_mfma_f32_16x16x32_bf16 v[12:15], a[208:211], v[152:155], v[12:15]\n"
"s_waitcnt lgkmcnt(5)\n"
"v_cvt_pk_bf16_f32 v203, v198, v199\n"
"v_mfma_f32_16x16x32_bf16 v[64:67], a[216:219], v[128:131], v[64:67]\n"
"ds_read_b64_tr_b16 a[234:235], v245 offset:57856\n"
"v_mfma_f32_16x16x32_bf16 v[64:67], a[220:223], v[132:135], v[64:67]\n"
"ds_read_b64_tr_b16 a[236:237], v245 offset:61440\n"
"buffer_store_dwordx4 v[200:203], v255, s[36:39], s44 offen offset:2048 sc1\n"
"v_mfma_f32_16x16x32_bf16 v[68:71], a[220:223], v[140:143], v[68:71]\n"
"buffer_load_dwordx4 a[144:147], v255, s[36:39], s76 offen offset:0 sc1\n"
"v_accvgpr_read_b32 v192, a44\n"
"v_mfma_f32_16x16x32_bf16 v[68:71], a[216:219], v[136:139], v[68:71]\n"
"ds_read_b64_tr_b16 a[238:239], v245 offset:61952\n"
"v_accvgpr_read_b32 v193, a45\n"
"v_mfma_f32_16x16x32_bf16 v[72:75], a[216:219], v[144:147], v[72:75]\n"
"v_accvgpr_read_b32 v194, a46\n"
"v_mfma_f32_16x16x32_bf16 v[72:75], a[220:223], v[148:151], v[72:75]\n"
"v_mfma_f32_16x16x32_bf16 v[76:79], a[220:223], v[156:159], v[76:79]\n"
"v_accvgpr_read_b32 v195, a47\n"
"v_mfma_f32_16x16x32_bf16 v[76:79], a[216:219], v[152:155], v[76:79]\n"
"s_waitcnt lgkmcnt(4)\n"
"v_accvgpr_read_b32 v196, a60\n"
"v_mfma_f32_16x16x32_bf16 v[16:19], a[224:227], v[128:131], v[16:19]\n"
"ds_read_b64_tr_b16 a[208:209], v246 offset:49152\n"
"v_accvgpr_read_b32 v197, a61\n"
"v_mfma_f32_16x16x32_bf16 v[16:19], a[228:231], v[132:135], v[16:19]\n"
"ds_read_b64_tr_b16 a[210:211], v246 offset:49664\n"
"v_accvgpr_read_b32 v198, a62\n"
"v_mfma_f32_16x16x32_bf16 v[20:23], a[228:231], v[140:143], v[20:23]\n"
"buffer_load_dwordx4 a[148:151], v255, s[36:39], s76 offen offset:1024 sc1\n"
"v_mfma_f32_16x16x32_bf16 v[20:23], a[224:227], v[136:139], v[20:23]\n"
"ds_read_b64_tr_b16 a[212:213], v246 offset:53248\n"
"v_accvgpr_read_b32 v199, a63\n"
"v_mfma_f32_16x16x32_bf16 v[24:27], a[224:227], v[144:147], v[24:27]\n"
"ds_read_b64_tr_b16 a[214:215], v246 offset:53760\n"
"v_mul_f32 v192, v215, v192\n"
"v_mfma_f32_16x16x32_bf16 v[24:27], a[228:231], v[148:151], v[24:27]\n"
"v_mul_f32 v193, v215, v193\n"
"v_mfma_f32_16x16x32_bf16 v[28:31], a[228:231], v[156:159], v[28:31]\n"
"ds_read_b64_tr_b16 a[216:217], v246 offset:57344\n"
"v_mul_f32 v194, v215, v194\n"
"v_mfma_f32_16x16x32_bf16 v[28:31], a[224:227], v[152:155], v[28:31]\n"
"s_waitcnt lgkmcnt(5)\n"
"v_mfma_f32_16x16x32_bf16 v[80:83], a[232:235], v[128:131], v[80:83]\n"
"ds_read_b64_tr_b16 a[218:219], v246 offset:57856\n"
"v_mul_f32 v195, v215, v195\n"
"v_mfma_f32_16x16x32_bf16 v[80:83], a[236:239], v[132:135], v[80:83]\n"
"ds_read_b64_tr_b16 a[220:221], v246 offset:61440\n"
"v_mul_f32 v196, v215, v196\n"
"v_mfma_f32_16x16x32_bf16 v[84:87], a[236:239], v[140:143], v[84:87]\n"
"buffer_load_dwordx4 a[152:155], v254, s[36:39], s76 offen offset:0 sc1\n"
"v_mul_f32 v197, v215, v197\n"
"v_mfma_f32_16x16x32_bf16 v[84:87], a[232:235], v[136:139], v[84:87]\n"
"ds_read_b64_tr_b16 a[222:223], v246 offset:61952\n"
"v_mul_f32 v198, v215, v198\n"
"v_mfma_f32_16x16x32_bf16 v[88:91], a[232:235], v[144:147], v[88:91]\n"
"v_mfma_f32_16x16x32_bf16 v[88:91], a[236:239], v[148:151], v[88:91]\n"
"v_mul_f32 v199, v215, v199\n"
"v_mfma_f32_16x16x32_bf16 v[92:95], a[236:239], v[156:159], v[92:95]\n"
"v_cvt_pk_bf16_f32 v204, v192, v193\n"
"v_mfma_f32_16x16x32_bf16 v[92:95], a[232:235], v[152:155], v[92:95]\n"
"s_waitcnt lgkmcnt(4)\n"
"v_cvt_pk_bf16_f32 v205, v194, v195\n"
"v_mfma_f32_16x16x32_bf16 v[32:35], a[208:211], v[128:131], v[32:35]\n"
"ds_read_b64_tr_b16 a[224:225], v247 offset:49152\n"
"v_cvt_pk_bf16_f32 v206, v196, v197\n"
"v_mfma_f32_16x16x32_bf16 v[32:35], a[212:215], v[132:135], v[32:35]\n"
"ds_read_b64_tr_b16 a[226:227], v247 offset:49664\n"
"v_mfma_f32_16x16x32_bf16 v[36:39], a[212:215], v[140:143], v[36:39]\n"
"ds_read_b64_tr_b16 a[228:229], v247 offset:53248\n"
"buffer_load_dwordx4 a[156:159], v254, s[36:39], s76 offen offset:1024 sc1\n"
"v_mfma_f32_16x16x32_bf16 v[36:39], a[208:211], v[136:139], v[36:39]\n"
"ds_read_b64_tr_b16 a[230:231], v247 offset:53760\n"
"v_cvt_pk_bf16_f32 v207, v198, v199\n"
"v_mfma_f32_16x16x32_bf16 v[40:43], a[208:211], v[144:147], v[40:43]\n"
"ds_read_b64_tr_b16 a[232:233], v247 offset:57344\n"
"buffer_store_dwordx4 v[204:207], v255, s[36:39], s44 offen offset:3072 sc1\n"
"v_mfma_f32_16x16x32_bf16 v[40:43], a[212:215], v[148:151], v[40:43]\n"
"s_add_u32 s44, s44, 8192\n"
"ds_read_b64_tr_b16 a[234:235], v247 offset:57856\n"
"v_accvgpr_read_b32 v192, a64\n"
"v_mfma_f32_16x16x32_bf16 v[44:47], a[212:215], v[156:159], v[44:47]\n"
"ds_read_b64_tr_b16 a[236:237], v247 offset:61440\n"
"v_accvgpr_read_b32 v193, a65\n"
"v_mfma_f32_16x16x32_bf16 v[44:47], a[208:211], v[152:155], v[44:47]\n"
"s_waitcnt lgkmcnt(7)\n"
"ds_read_b64_tr_b16 a[238:239], v247 offset:61952\n"
"v_accvgpr_read_b32 v194, a66\n"
"v_mfma_f32_16x16x32_bf16 v[96:99], a[216:219], v[128:131], v[96:99]\n"
"v_accvgpr_read_b32 v195, a67\n"
"v_mfma_f32_16x16x32_bf16 v[96:99], a[220:223], v[132:135], v[96:99]\n"
"v_accvgpr_read_b32 v196, a80\n"
"v_mfma_f32_16x16x32_bf16 v[100:103], a[220:223], v[140:143], v[100:103]\n"
"buffer_load_dwordx4 a[160:163], v226, s[36:39], s76 offen offset:0 sc1\n"
"v_mfma_f32_16x16x32_bf16 v[100:103], a[216:219], v[136:139], v[100:103]\n"
"v_accvgpr_read_b32 v197, a81\n"
"v_mfma_f32_16x16x32_bf16 v[104:107], a[216:219], v[144:147], v[104:107]\n"
"v_accvgpr_read_b32 v198, a82\n"
"v_mfma_f32_16x16x32_bf16 v[104:107], a[220:223], v[148:151], v[104:107]\n"
"v_accvgpr_read_b32 v199, a83\n"
"v_mfma_f32_16x16x32_bf16 v[108:111], a[220:223], v[156:159], v[108:111]\n"
"v_mul_f32 v192, v212, v192\n"
"v_mfma_f32_16x16x32_bf16 v[108:111], a[216:219], v[152:155], v[108:111]\n"
"s_waitcnt lgkmcnt(0)\n"
"v_mfma_f32_16x16x32_bf16 v[48:51], a[224:227], v[128:131], v[48:51]\n"
"s_nop 0\n"
"s_barrier\n"
"v_mul_f32 v193, v212, v193\n"
"v_mfma_f32_16x16x32_bf16 v[48:51], a[228:231], v[132:135], v[48:51]\n"
"ds_read_b64_tr_b16 a[208:209], v248 offset:0\n"
"v_mul_f32 v194, v212, v194\n"
"v_mfma_f32_16x16x32_bf16 v[52:55], a[228:231], v[140:143], v[52:55]\n"
"ds_read_b64_tr_b16 a[210:211], v248 offset:512\n"
"buffer_load_dwordx4 a[164:167], v226, s[36:39], s76 offen offset:1024 sc1\n"
"v_mfma_f32_16x16x32_bf16 v[52:55], a[224:227], v[136:139], v[52:55]\n"
"v_mul_f32 v195, v212, v195\n"
"v_mul_f32 v196, v212, v196\n"
"v_mfma_f32_16x16x32_bf16 v[56:59], a[224:227], v[144:147], v[56:59]\n"
"ds_read_b64_tr_b16 a[212:213], v248 offset:4096\n"
"v_mfma_f32_16x16x32_bf16 v[56:59], a[228:231], v[148:151], v[56:59]\n"
"ds_read_b64_tr_b16 a[214:215], v248 offset:4608\n"
"v_mul_f32 v197, v212, v197\n"
"v_mfma_f32_16x16x32_bf16 v[60:63], a[228:231], v[156:159], v[60:63]\n"
"v_mul_f32 v198, v212, v198\n"
"v_mfma_f32_16x16x32_bf16 v[60:63], a[224:227], v[152:155], v[60:63]\n"
"ds_read_b64_tr_b16 a[216:217], v248 offset:8192\n"
"v_mul_f32 v199, v212, v199\n"
"v_mfma_f32_16x16x32_bf16 v[112:115], a[232:235], v[128:131], v[112:115]\n"
"ds_read_b64_tr_b16 a[218:219], v248 offset:8704\n"
"v_cvt_pk_bf16_f32 v208, v192, v193\n"
"v_mfma_f32_16x16x32_bf16 v[112:115], a[236:239], v[132:135], v[112:115]\n"
"v_mfma_f32_16x16x32_bf16 v[116:119], a[236:239], v[140:143], v[116:119]\n"
"ds_read_b64_tr_b16 a[220:221], v248 offset:12288\n"
"buffer_load_dwordx4 a[168:171], v217, s[36:39], s76 offen offset:0 sc1\n"
"v_mfma_f32_16x16x32_bf16 v[116:119], a[232:235], v[136:139], v[116:119]\n"
"ds_read_b64_tr_b16 a[222:223], v248 offset:12800\n"
"v_cvt_pk_bf16_f32 v209, v194, v195\n"
"v_mfma_f32_16x16x32_bf16 v[120:123], a[232:235], v[144:147], v[120:123]\n"
"v_cvt_pk_bf16_f32 v210, v196, v197\n"
"v_cvt_pk_bf16_f32 v211, v198, v199\n"
"v_mfma_f32_16x16x32_bf16 v[120:123], a[236:239], v[148:151], v[120:123]\n"
"buffer_store_dwordx4 v[208:211], v255, s[36:39], s44 offen offset:0 sc1\n"
"v_mfma_f32_16x16x32_bf16 v[124:127], a[236:239], v[156:159], v[124:127]\n"
"s_waitcnt vmcnt(21)\n"
"v_mfma_f32_16x16x32_bf16 v[124:127], a[232:235], v[152:155], v[124:127]\n"
"s_waitcnt lgkmcnt(4)\n"
"v_accvgpr_read_b32 v192, a68\n"
"v_mfma_f32_16x16x32_bf16 v[0:3], a[208:211], v[160:163], v[0:3]\n"
"s_lshl_b32 s95, s68, 10\n"
"s_add_u32 s95, s95, 4096\n"
"ds_read_b64_tr_b16 a[224:225], v249 offset:0\n"
"v_mfma_f32_16x16x32_bf16 v[0:3], a[212:215], v[164:167], v[0:3]\n"
"ds_read_b64_tr_b16 a[226:227], v249 offset:512\n"
"buffer_load_dword v220, v255, s[36:39], s95 offen sc1\n"
"v_mfma_f32_16x16x32_bf16 v[4:7], a[212:215], v[172:175], v[4:7]\n"
"buffer_load_dwordx4 a[172:175], v217, s[36:39], s76 offen offset:1024 sc1\n"
"v_accvgpr_read_b32 v193, a69\n"
"v_mfma_f32_16x16x32_bf16 v[4:7], a[208:211], v[168:171], v[4:7]\n"
"s_add_u32 s76, s79, s58\n"
"s_add_u32 s58, s58, 2048\n"
"ds_read_b64_tr_b16 a[228:229], v249 offset:4096\n"
"v_mfma_f32_16x16x32_bf16 v[8:11], a[208:211], v[176:179], v[8:11]\n"
"s_sub_u32 s98, s58, 20480\n"
"s_min_u32 s58, s58, s98\n"
"ds_read_b64_tr_b16 a[230:231], v249 offset:4608\n"
"v_mfma_f32_16x16x32_bf16 v[8:11], a[212:215], v[180:183], v[8:11]\n"
"v_accvgpr_read_b32 v194, a70\n"
"v_accvgpr_read_b32 v195, a71\n"
"v_mfma_f32_16x16x32_bf16 v[12:15], a[212:215], v[188:191], v[12:15]\n"
"ds_read_b64_tr_b16 a[232:233], v249 offset:8192\n"
"v_accvgpr_read_b32 v196, a84\n"
"v_mfma_f32_16x16x32_bf16 v[12:15], a[208:211], v[184:187], v[12:15]\n"
"s_waitcnt lgkmcnt(5)\n"
"v_accvgpr_read_b32 v197, a85\n"
"v_accvgpr_read_b32 v198, a86\n"
"v_mfma_f32_16x16x32_bf16 v[64:67], a[216:219], v[160:163], v[64:67]\n"
"ds_read_b64_tr_b16 a[234:235], v249 offset:8704\n"
"v_accvgpr_read_b32 v199, a87\n"
"v_mfma_f32_16x16x32_bf16 v[64:67], a[220:223], v[164:167], v[64:67]\n"
"ds_read_b64_tr_b16 a[236:237], v249 offset:12288\n"
"v_mul_f32 v192, v213, v192\n"
"v_mfma_f32_16x16x32_bf16 v[68:71], a[220:223], v[172:175], v[68:71]\n"
"buffer_load_dwordx4 a[176:179], v255, s[36:39], s76 offen offset:0 sc1\n"
"v_mul_f32 v193, v213, v193\n"
"v_mfma_f32_16x16x32_bf16 v[68:71], a[216:219], v[168:171], v[68:71]\n"
"ds_read_b64_tr_b16 a[238:239], v249 offset:12800\n"
"v_mul_f32 v194, v213, v194\n"
"v_mfma_f32_16x16x32_bf16 v[72:75], a[216:219], v[176:179], v[72:75]\n"
"v_mfma_f32_16x16x32_bf16 v[72:75], a[220:223], v[180:183], v[72:75]\n"
"v_mul_f32 v195, v213, v195\n"
"v_mfma_f32_16x16x32_bf16 v[76:79], a[220:223], v[188:191], v[76:79]\n"
"v_mul_f32 v196, v213, v196\n"
"v_mfma_f32_16x16x32_bf16 v[76:79], a[216:219], v[184:187], v[76:79]\n"
"s_waitcnt lgkmcnt(4)\n"
"v_mul_f32 v197, v213, v197\n"
"v_mfma_f32_16x16x32_bf16 v[16:19], a[224:227], v[160:163], v[16:19]\n"
"ds_read_b64_tr_b16 a[208:209], v250 offset:0\n"
"v_mul_f32 v198, v213, v198\n"
"v_mfma_f32_16x16x32_bf16 v[16:19], a[228:231], v[164:167], v[16:19]\n"
"ds_read_b64_tr_b16 a[210:211], v250 offset:512\n"
"v_mfma_f32_16x16x32_bf16 v[20:23], a[228:231], v[172:175], v[20:23]\n"
"buffer_load_dwordx4 a[180:183], v255, s[36:39], s76 offen offset:1024 sc1\n"
"v_mul_f32 v199, v213, v199\n"
"v_mfma_f32_16x16x32_bf16 v[20:23], a[224:227], v[168:171], v[20:23]\n"
"ds_read_b64_tr_b16 a[212:213], v250 offset:4096\n"
"v_cvt_pk_bf16_f32 v200, v192, v193\n"
"v_mfma_f32_16x16x32_bf16 v[24:27], a[224:227], v[176:179], v[24:27]\n"
"ds_read_b64_tr_b16 a[214:215], v250 offset:4608\n"
"v_cvt_pk_bf16_f32 v201, v194, v195\n"
"v_mfma_f32_16x16x32_bf16 v[24:27], a[228:231], v[180:183], v[24:27]\n"
"v_cvt_pk_bf16_f32 v202, v196, v197\n"
"v_mfma_f32_16x16x32_bf16 v[28:31], a[228:231], v[188:191], v[28:31]\n"
"ds_read_b64_tr_b16 a[216:217], v250 offset:8192\n"
"v_mfma_f32_16x16x32_bf16 v[28:31], a[224:227], v[184:187], v[28:31]\n"
"s_waitcnt lgkmcnt(5)\n"
"v_cvt_pk_bf16_f32 v203, v198, v199\n"
"v_mfma_f32_16x16x32_bf16 v[80:83], a[232:235], v[160:163], v[80:83]\n"
"ds_read_b64_tr_b16 a[218:219], v250 offset:8704\n"
"buffer_store_dwordx4 v[200:203], v255, s[36:39], s44 offen offset:1024 sc1\n"
"v_mfma_f32_16x16x32_bf16 v[80:83], a[236:239], v[164:167], v[80:83]\n"
"ds_read_b64_tr_b16 a[220:221], v250 offset:12288\n"
"v_accvgpr_read_b32 v192, a72\n"
"v_mfma_f32_16x16x32_bf16 v[84:87], a[236:239], v[172:175], v[84:87]\n"
"buffer_load_dwordx4 a[184:187], v254, s[36:39], s76 offen offset:0 sc1\n"
"v_accvgpr_read_b32 v193, a73\n"
"v_mfma_f32_16x16x32_bf16 v[84:87], a[232:235], v[168:171], v[84:87]\n"
"ds_read_b64_tr_b16 a[222:223], v250 offset:12800\n"
"v_mfma_f32_16x16x32_bf16 v[88:91], a[232:235], v[176:179], v[88:91]\n"
"v_accvgpr_read_b32 v194, a74\n"
"v_mfma_f32_16x16x32_bf16 v[88:91], a[236:239], v[180:183], v[88:91]\n"
"v_accvgpr_read_b32 v195, a75\n"
"v_mfma_f32_16x16x32_bf16 v[92:95], a[236:239], v[188:191], v[92:95]\n"
"v_accvgpr_read_b32 v196, a88\n"
"v_mfma_f32_16x16x32_bf16 v[92:95], a[232:235], v[184:187], v[92:95]\n"
"s_waitcnt lgkmcnt(4)\n"
"v_accvgpr_read_b32 v197, a89\n"
"v_mfma_f32_16x16x32_bf16 v[32:35], a[208:211], v[160:163], v[32:35]\n"
"ds_read_b64_tr_b16 a[224:225], v251 offset:0\n"
"v_mfma_f32_16x16x32_bf16 v[32:35], a[212:215], v[164:167], v[32:35]\n"
"ds_read_b64_tr_b16 a[226:227], v251 offset:512\n"
"v_accvgpr_read_b32 v198, a90\n"
"v_mfma_f32_16x16x32_bf16 v[36:39], a[212:215], v[172:175], v[36:39]\n"
"ds_read_b64_tr_b16 a[228:229], v251 offset:4096\n"
"buffer_load_dwordx4 a[188:191], v254, s[36:39], s76 offen offset:1024 sc1\n"
"v_mfma_f32_16x16x32_bf16 v[36:39], a[208:211], v[168:171], v[36:39]\n"
"ds_read_b64_tr_b16 a[230:231], v251 offset:4608\n"
"v_accvgpr_read_b32 v199, a91\n"
"v_mfma_f32_16x16x32_bf16 v[40:43], a[208:211], v[176:179], v[40:43]\n"
"ds_read_b64_tr_b16 a[232:233], v251 offset:8192\n"
"v_mul_f32 v192, v214, v192\n"
"v_mfma_f32_16x16x32_bf16 v[40:43], a[212:215], v[180:183], v[40:43]\n"
"ds_read_b64_tr_b16 a[234:235], v251 offset:8704\n"
"v_mul_f32 v193, v214, v193\n"
"v_mfma_f32_16x16x32_bf16 v[44:47], a[212:215], v[188:191], v[44:47]\n"
"ds_read_b64_tr_b16 a[236:237], v251 offset:12288\n"
"v_mul_f32 v194, v214, v194\n"
"v_mfma_f32_16x16x32_bf16 v[44:47], a[208:211], v[184:187], v[44:47]\n"
"s_waitcnt lgkmcnt(7)\n"
"ds_read_b64_tr_b16 a[238:239], v251 offset:12800\n"
"v_mul_f32 v195, v214, v195\n"
"v_mfma_f32_16x16x32_bf16 v[96:99], a[216:219], v[160:163], v[96:99]\n"
"v_mul_f32 v196, v214, v196\n"
"v_mfma_f32_16x16x32_bf16 v[96:99], a[220:223], v[164:167], v[96:99]\n"
"v_mul_f32 v197, v214, v197\n"
"v_mfma_f32_16x16x32_bf16 v[100:103], a[220:223], v[172:175], v[100:103]\n"
"buffer_load_dwordx4 a[192:195], v226, s[36:39], s76 offen offset:0 sc1\n"
"v_mfma_f32_16x16x32_bf16 v[100:103], a[216:219], v[168:171], v[100:103]\n"
"v_mul_f32 v198, v214, v198\n"
"v_mfma_f32_16x16x32_bf16 v[104:107], a[216:219], v[176:179], v[104:107]\n"
"v_mul_f32 v199, v214, v199\n"
"v_mfma_f32_16x16x32_bf16 v[104:107], a[220:223], v[180:183], v[104:107]\n"
"v_cvt_pk_bf16_f32 v204, v192, v193\n"
"v_mfma_f32_16x16x32_bf16 v[108:111], a[220:223], v[188:191], v[108:111]\n"
"v_cvt_pk_bf16_f32 v205, v194, v195\n"
"v_mfma_f32_16x16x32_bf16 v[108:111], a[216:219], v[184:187], v[108:111]\n"
"s_waitcnt lgkmcnt(0)\n"
"v_mfma_f32_16x16x32_bf16 v[48:51], a[224:227], v[160:163], v[48:51]\n"
"s_nop 0\n"
"s_barrier\n"
"v_cvt_pk_bf16_f32 v206, v196, v197\n"
"v_mfma_f32_16x16x32_bf16 v[48:51], a[228:231], v[164:167], v[48:51]\n"
"ds_read_b64_tr_b16 a[208:209], v248 offset:16384\n"
"v_cvt_pk_bf16_f32 v207, v198, v199\n"
"v_mfma_f32_16x16x32_bf16 v[52:55], a[228:231], v[172:175], v[52:55]\n"
"ds_read_b64_tr_b16 a[210:211], v248 offset:16896\n"
"buffer_load_dwordx4 a[196:199], v226, s[36:39], s76 offen offset:1024 sc1\n"
"v_mfma_f32_16x16x32_bf16 v[52:55], a[224:227], v[168:171], v[52:55]\n"
"buffer_store_dwordx4 v[204:207], v255, s[36:39], s44 offen offset:2048 sc1\n"
"v_accvgpr_read_b32 v192, a76\n"
"v_mfma_f32_16x16x32_bf16 v[56:59], a[224:227], v[176:179], v[56:59]\n"
"ds_read_b64_tr_b16 a[212:213], v248 offset:20480\n"
"v_mfma_f32_16x16x32_bf16 v[56:59], a[228:231], v[180:183], v[56:59]\n"
"ds_read_b64_tr_b16 a[214:215], v248 offset:20992\n"
"v_accvgpr_read_b32 v193, a77\n"
"v_mfma_f32_16x16x32_bf16 v[60:63], a[228:231], v[188:191], v[60:63]\n"
"v_accvgpr_read_b32 v194, a78\n"
"v_mfma_f32_16x16x32_bf16 v[60:63], a[224:227], v[184:187], v[60:63]\n"
"ds_read_b64_tr_b16 a[216:217], v248 offset:24576\n"
"v_accvgpr_read_b32 v195, a79\n"
"v_mfma_f32_16x16x32_bf16 v[112:115], a[232:235], v[160:163], v[112:115]\n"
"ds_read_b64_tr_b16 a[218:219], v248 offset:25088\n"
"v_accvgpr_read_b32 v196, a92\n"
"v_mfma_f32_16x16x32_bf16 v[112:115], a[236:239], v[164:167], v[112:115]\n"
"v_mfma_f32_16x16x32_bf16 v[116:119], a[236:239], v[172:175], v[116:119]\n"
"ds_read_b64_tr_b16 a[220:221], v248 offset:28672\n"
"buffer_load_dwordx4 a[200:203], v217, s[36:39], s76 offen offset:0 sc1\n"
"v_mfma_f32_16x16x32_bf16 v[116:119], a[232:235], v[168:171], v[116:119]\n"
"ds_read_b64_tr_b16 a[222:223], v248 offset:29184\n"
"v_accvgpr_read_b32 v197, a93\n"
"v_mfma_f32_16x16x32_bf16 v[120:123], a[232:235], v[176:179], v[120:123]\n"
"v_accvgpr_read_b32 v198, a94\n"
"v_accvgpr_read_b32 v199, a95\n"
"v_mfma_f32_16x16x32_bf16 v[120:123], a[236:239], v[180:183], v[120:123]\n"
"v_mul_f32 v192, v215, v192\n"
"v_mfma_f32_16x16x32_bf16 v[124:127], a[236:239], v[188:191], v[124:127]\n"
"s_waitcnt vmcnt(21)\n"
"v_mfma_f32_16x16x32_bf16 v[124:127], a[232:235], v[184:187], v[124:127]\n"
"s_waitcnt lgkmcnt(4)\n"
"v_mul_f32 v193, v215, v193\n"
"v_mfma_f32_16x16x32_bf16 v[0:3], a[208:211], a[0:3], v[0:3]\n"
"ds_read_b64_tr_b16 a[224:225], v249 offset:16384\n"
"v_mul_f32 v194, v215, v194\n"
"v_mfma_f32_16x16x32_bf16 v[0:3], a[212:215], a[4:7], v[0:3]\n"
"ds_read_b64_tr_b16 a[226:227], v249 offset:16896\n"
"v_mul_f32 v195, v215, v195\n"
"v_mfma_f32_16x16x32_bf16 v[4:7], a[212:215], a[12:15], v[4:7]\n"
"buffer_load_dwordx4 a[204:207], v217, s[36:39], s76 offen offset:1024 sc1\n"
"v_mul_f32 v196, v215, v196\n"
"v_mfma_f32_16x16x32_bf16 v[4:7], a[208:211], a[8:11], v[4:7]\n"
"s_add_u32 s76, s79, s58\n"
"s_add_u32 s58, s58, 2048\n"
"ds_read_b64_tr_b16 a[228:229], v249 offset:20480\n"
"v_mfma_f32_16x16x32_bf16 v[8:11], a[208:211], a[16:19], v[8:11]\n"
"s_sub_u32 s98, s58, 20480\n"
"s_min_u32 s58, s58, s98\n"
"ds_read_b64_tr_b16 a[230:231], v249 offset:20992\n"
"v_mfma_f32_16x16x32_bf16 v[8:11], a[212:215], a[20:23], v[8:11]\n"
"v_mul_f32 v197, v215, v197\n"
"v_mul_f32 v198, v215, v198\n"
"v_mfma_f32_16x16x32_bf16 v[12:15], a[212:215], a[28:31], v[12:15]\n"
"ds_read_b64_tr_b16 a[232:233], v249 offset:24576\n"
"v_mul_f32 v199, v215, v199\n"
"v_mfma_f32_16x16x32_bf16 v[12:15], a[208:211], a[24:27], v[12:15]\n"
"s_waitcnt lgkmcnt(5)\n"
"v_cvt_pk_bf16_f32 v208, v192, v193\n"
"v_mfma_f32_16x16x32_bf16 v[64:67], a[216:219], a[0:3], v[64:67]\n"
"ds_read_b64_tr_b16 a[234:235], v249 offset:25088\n"
"v_mfma_f32_16x16x32_bf16 v[64:67], a[220:223], a[4:7], v[64:67]\n"
"ds_read_b64_tr_b16 a[236:237], v249 offset:28672\n"
"v_cvt_pk_bf16_f32 v209, v194, v195\n"
"v_mfma_f32_16x16x32_bf16 v[68:71], a[220:223], a[12:15], v[68:71]\n"
"buffer_load_dwordx4 v[128:131], v255, s[36:39], s76 offen offset:0 sc1\n"
"v_cvt_pk_bf16_f32 v210, v196, v197\n"
"v_mfma_f32_16x16x32_bf16 v[68:71], a[216:219], a[8:11], v[68:71]\n"
"ds_read_b64_tr_b16 a[238:239], v249 offset:29184\n"
"v_cvt_pk_bf16_f32 v211, v198, v199\n"
"v_mfma_f32_16x16x32_bf16 v[72:75], a[216:219], a[16:19], v[72:75]\n"
"buffer_store_dwordx4 v[208:211], v255, s[36:39], s44 offen offset:3072 sc1\n"
"v_mfma_f32_16x16x32_bf16 v[72:75], a[220:223], a[20:23], v[72:75]\n"
"s_add_u32 s44, s44, 8192\n"
"v_mfma_f32_16x16x32_bf16 v[76:79], a[220:223], a[28:31], v[76:79]\n"
".L_pad10:\n"
"s_cbranch_execz .L_pad14\n"
"v_accvgpr_read_b32 v192, a96\n"
"v_mfma_f32_16x16x32_bf16 v[76:79], a[216:219], a[24:27], v[76:79]\n"
"s_waitcnt lgkmcnt(4)\n"
"v_accvgpr_read_b32 v193, a97\n"
"v_mfma_f32_16x16x32_bf16 v[16:19], a[224:227], a[0:3], v[16:19]\n"
"ds_read_b64_tr_b16 a[208:209], v250 offset:16384\n"
"v_accvgpr_read_b32 v194, a98\n"
"v_mfma_f32_16x16x32_bf16 v[16:19], a[228:231], a[4:7], v[16:19]\n"
"ds_read_b64_tr_b16 a[210:211], v250 offset:16896\n"
"v_mfma_f32_16x16x32_bf16 v[20:23], a[228:231], a[12:15], v[20:23]\n"
"buffer_load_dwordx4 v[132:135], v255, s[36:39], s76 offen offset:1024 sc1\n"
"v_accvgpr_read_b32 v195, a99\n"
"v_mfma_f32_16x16x32_bf16 v[20:23], a[224:227], a[8:11], v[20:23]\n"
"ds_read_b64_tr_b16 a[212:213], v250 offset:20480\n"
"v_accvgpr_read_b32 v196, a112\n"
"v_mfma_f32_16x16x32_bf16 v[24:27], a[224:227], a[16:19], v[24:27]\n"
"ds_read_b64_tr_b16 a[214:215], v250 offset:20992\n"
"v_accvgpr_read_b32 v197, a113\n"
"v_mfma_f32_16x16x32_bf16 v[24:27], a[228:231], a[20:23], v[24:27]\n"
"v_accvgpr_read_b32 v198, a114\n"
"v_mfma_f32_16x16x32_bf16 v[28:31], a[228:231], a[28:31], v[28:31]\n"
"ds_read_b64_tr_b16 a[216:217], v250 offset:24576\n"
"v_mfma_f32_16x16x32_bf16 v[28:31], a[224:227], a[24:27], v[28:31]\n"
"s_waitcnt lgkmcnt(5)\n"
"v_accvgpr_read_b32 v199, a115\n"
"v_mfma_f32_16x16x32_bf16 v[80:83], a[232:235], a[0:3], v[80:83]\n"
"ds_read_b64_tr_b16 a[218:219], v250 offset:25088\n"
"v_mul_f32 v192, v212, v192\n"
"v_mfma_f32_16x16x32_bf16 v[80:83], a[236:239], a[4:7], v[80:83]\n"
"ds_read_b64_tr_b16 a[220:221], v250 offset:28672\n"
"v_mul_f32 v193, v212, v193\n"
"v_mfma_f32_16x16x32_bf16 v[84:87], a[236:239], a[12:15], v[84:87]\n"
"buffer_load_dwordx4 v[136:139], v254, s[36:39], s76 offen offset:0 sc1\n"
"v_mul_f32 v194, v212, v194\n"
"v_mfma_f32_16x16x32_bf16 v[84:87], a[232:235], a[8:11], v[84:87]\n"
"ds_read_b64_tr_b16 a[222:223], v250 offset:29184\n"
"v_mfma_f32_16x16x32_bf16 v[88:91], a[232:235], a[16:19], v[88:91]\n"
"v_mul_f32 v195, v212, v195\n"
"v_mfma_f32_16x16x32_bf16 v[88:91], a[236:239], a[20:23], v[88:91]\n"
"v_mul_f32 v196, v212, v196\n"
"v_mfma_f32_16x16x32_bf16 v[92:95], a[236:239], a[28:31], v[92:95]\n"
"v_mul_f32 v197, v212, v197\n"
"v_mfma_f32_16x16x32_bf16 v[92:95], a[232:235], a[24:27], v[92:95]\n"
"s_waitcnt lgkmcnt(4)\n"
"v_mul_f32 v198, v212, v198\n"
"v_mfma_f32_16x16x32_bf16 v[32:35], a[208:211], a[0:3], v[32:35]\n"
"ds_read_b64_tr_b16 a[224:225], v251 offset:16384\n"
"v_mfma_f32_16x16x32_bf16 v[32:35], a[212:215], a[4:7], v[32:35]\n"
"ds_read_b64_tr_b16 a[226:227], v251 offset:16896\n"
"v_mul_f32 v199, v212, v199\n"
"v_mfma_f32_16x16x32_bf16 v[36:39], a[212:215], a[12:15], v[36:39]\n"
"ds_read_b64_tr_b16 a[228:229], v251 offset:20480\n"
"buffer_load_dwordx4 v[140:143], v254, s[36:39], s76 offen offset:1024 sc1\n"
"v_mfma_f32_16x16x32_bf16 v[36:39], a[208:211], a[8:11], v[36:39]\n"
"ds_read_b64_tr_b16 a[230:231], v251 offset:20992\n"
"v_cvt_pk_bf16_f32 v200, v192, v193\n"
"v_mfma_f32_16x16x32_bf16 v[40:43], a[208:211], a[16:19], v[40:43]\n"
"ds_read_b64_tr_b16 a[232:233], v251 offset:24576\n"
"v_cvt_pk_bf16_f32 v201, v194, v195\n"
"v_mfma_f32_16x16x32_bf16 v[40:43], a[212:215], a[20:23], v[40:43]\n"
"ds_read_b64_tr_b16 a[234:235], v251 offset:25088\n"
"v_cvt_pk_bf16_f32 v202, v196, v197\n"
"v_mfma_f32_16x16x32_bf16 v[44:47], a[212:215], a[28:31], v[44:47]\n"
"ds_read_b64_tr_b16 a[236:237], v251 offset:28672\n"
"v_cvt_pk_bf16_f32 v203, v198, v199\n"
"v_mfma_f32_16x16x32_bf16 v[44:47], a[208:211], a[24:27], v[44:47]\n"
"s_waitcnt lgkmcnt(7)\n"
"ds_read_b64_tr_b16 a[238:239], v251 offset:29184\n"
"buffer_store_dwordx4 v[200:203], v255, s[36:39], s44 offen offset:0 sc1\n"
"v_mfma_f32_16x16x32_bf16 v[96:99], a[216:219], a[0:3], v[96:99]\n"
"v_accvgpr_read_b32 v192, a100\n"
"v_mfma_f32_16x16x32_bf16 v[96:99], a[220:223], a[4:7], v[96:99]\n"
"v_accvgpr_read_b32 v193, a101\n"
"v_mfma_f32_16x16x32_bf16 v[100:103], a[220:223], a[12:15], v[100:103]\n"
"buffer_load_dwordx4 v[144:147], v226, s[36:39], s76 offen offset:0 sc1\n"
"v_mfma_f32_16x16x32_bf16 v[100:103], a[216:219], a[8:11], v[100:103]\n"
"v_accvgpr_read_b32 v194, a102\n"
"v_mfma_f32_16x16x32_bf16 v[104:107], a[216:219], a[16:19], v[104:107]\n"
"v_accvgpr_read_b32 v195, a103\n"
"v_mfma_f32_16x16x32_bf16 v[104:107], a[220:223], a[20:23], v[104:107]\n"
"v_accvgpr_read_b32 v196, a116\n"
"v_mfma_f32_16x16x32_bf16 v[108:111], a[220:223], a[28:31], v[108:111]\n"
"v_accvgpr_read_b32 v197, a117\n"
"v_mfma_f32_16x16x32_bf16 v[108:111], a[216:219], a[24:27], v[108:111]\n"
"s_waitcnt lgkmcnt(0)\n"
"v_mfma_f32_16x16x32_bf16 v[48:51], a[224:227], a[0:3], v[48:51]\n"
"s_nop 0\n"
"s_barrier\n"
"v_accvgpr_read_b32 v198, a118\n"
"v_mfma_f32_16x16x32_bf16 v[48:51], a[228:231], a[4:7], v[48:51]\n"
"ds_read_b64_tr_b16 a[208:209], v248 offset:49152\n"
"v_accvgpr_read_b32 v199, a119\n"
"v_mfma_f32_16x16x32_bf16 v[52:55], a[228:231], a[12:15], v[52:55]\n"
"ds_read_b64_tr_b16 a[210:211], v248 offset:49664\n"
"buffer_load_dwordx4 v[148:151], v226, s[36:39], s76 offen offset:1024 sc1\n"
"v_mfma_f32_16x16x32_bf16 v[52:55], a[224:227], a[8:11], v[52:55]\n"
"v_mul_f32 v192, v213, v192\n"
"v_mul_f32 v193, v213, v193\n"
"v_mfma_f32_16x16x32_bf16 v[56:59], a[224:227], a[16:19], v[56:59]\n"
"ds_read_b64_tr_b16 a[212:213], v248 offset:53248\n"
"v_mfma_f32_16x16x32_bf16 v[56:59], a[228:231], a[20:23], v[56:59]\n"
"ds_read_b64_tr_b16 a[214:215], v248 offset:53760\n"
"v_mul_f32 v194, v213, v194\n"
"v_mfma_f32_16x16x32_bf16 v[60:63], a[228:231], a[28:31], v[60:63]\n"
"s_lshl1_add_u32 s90, s69, s45\n"
"v_mul_f32 v195, v213, v195\n"
"v_mfma_f32_16x16x32_bf16 v[60:63], a[224:227], a[24:27], v[60:63]\n"
"s_lshl_b32 s90, s90, 20\n"
"ds_read_b64_tr_b16 a[216:217], v248 offset:57344\n"
"v_mul_f32 v196, v213, v196\n"
"v_mfma_f32_16x16x32_bf16 v[112:115], a[232:235], a[0:3], v[112:115]\n"
"s_lshl_b32 s91, s71, 18\n"
"ds_read_b64_tr_b16 a[218:219], v248 offset:57856\n"
"v_mul_f32 v197, v213, v197\n"
"v_mfma_f32_16x16x32_bf16 v[112:115], a[236:239], a[4:7], v[112:115]\n"
"s_add_u32 s90, s90, s91\n"
"v_mfma_f32_16x16x32_bf16 v[116:119], a[236:239], a[12:15], v[116:119]\n"
"s_add_u32 s40, s90, 41943040\n"
"ds_read_b64_tr_b16 a[220:221], v248 offset:61440\n"
"buffer_load_dwordx4 v[152:155], v217, s[36:39], s76 offen offset:0 sc1\n"
"v_mfma_f32_16x16x32_bf16 v[116:119], a[232:235], a[8:11], v[116:119]\n"
"s_mov_b32 s82, 0\n"
"ds_read_b64_tr_b16 a[222:223], v248 offset:61952\n"
"v_mul_f32 v198, v213, v198\n"
"v_mfma_f32_16x16x32_bf16 v[120:123], a[232:235], a[16:19], v[120:123]\n"
"v_mul_f32 v199, v213, v199\n"
"v_cvt_pk_bf16_f32 v204, v192, v193\n"
"v_mfma_f32_16x16x32_bf16 v[120:123], a[236:239], a[20:23], v[120:123]\n"
"s_lshr_b32 s91, s71, 1\n"
"v_cvt_pk_bf16_f32 v205, v194, v195\n"
"v_mfma_f32_16x16x32_bf16 v[124:127], a[236:239], a[28:31], v[124:127]\n"
"s_waitcnt vmcnt(19)\n"
"s_lshl2_add_u32 s91, s91, s68\n"
"v_mfma_f32_16x16x32_bf16 v[124:127], a[232:235], a[24:27], v[124:127]\n"
"s_waitcnt lgkmcnt(4)\n"
"s_lshl_b32 s91, s91, 10\n"
"v_cvt_pk_bf16_f32 v206, v196, v197\n"
"v_mfma_f32_16x16x32_bf16 v[0:3], a[208:211], a[144:147], v[0:3]\n"
"ds_read_b64_tr_b16 a[224:225], v249 offset:49152\n"
"v_add_u32 v240, s91, v255\n"
"v_mfma_f32_16x16x32_bf16 v[0:3], a[212:215], a[148:151], v[0:3]\n"
"ds_read_b64_tr_b16 a[226:227], v249 offset:49664\n"
"v_cvt_pk_bf16_f32 v207, v198, v199\n"
"v_mfma_f32_16x16x32_bf16 v[4:7], a[212:215], a[156:159], v[4:7]\n"
"buffer_load_dwordx4 v[156:159], v217, s[36:39], s76 offen offset:1024 sc1\n"
"buffer_store_dwordx4 v[204:207], v255, s[36:39], s44 offen offset:1024 sc1\n"
"v_mfma_f32_16x16x32_bf16 v[4:7], a[208:211], a[152:155], v[4:7]\n"
"ds_read_b64_tr_b16 a[228:229], v249 offset:53248\n"
"v_accvgpr_read_b32 v192, a104\n"
"v_mfma_f32_16x16x32_bf16 v[8:11], a[208:211], a[160:163], v[8:11]\n"
"ds_read_b64_tr_b16 a[230:231], v249 offset:53760\n"
"v_accvgpr_read_b32 v193, a105\n"
"v_mfma_f32_16x16x32_bf16 v[8:11], a[212:215], a[164:167], v[8:11]\n"
"v_xor_b32 v241, 4096, v240\n"
"v_accvgpr_read_b32 v194, a106\n"
"v_mfma_f32_16x16x32_bf16 v[12:15], a[212:215], a[172:175], v[12:15]\n"
"ds_read_b64_tr_b16 a[232:233], v249 offset:57344\n"
"v_accvgpr_read_b32 v195, a107\n"
"v_mfma_f32_16x16x32_bf16 v[12:15], a[208:211], a[168:171], v[12:15]\n"
"s_waitcnt lgkmcnt(5)\n"
"v_accvgpr_read_b32 v196, a120\n"
"v_mfma_f32_16x16x32_bf16 v[64:67], a[216:219], a[144:147], v[64:67]\n"
"ds_read_b64_tr_b16 a[234:235], v249 offset:57856\n"
"v_mfma_f32_16x16x32_bf16 v[64:67], a[220:223], a[148:151], v[64:67]\n"
"s_and_b32 s91, s71, 1\n"
"ds_read_b64_tr_b16 a[236:237], v249 offset:61440\n"
"v_accvgpr_read_b32 v197, a121\n"
"v_mfma_f32_16x16x32_bf16 v[68:71], a[220:223], a[156:159], v[68:71]\n"
"s_add_u32 m0, s81, 131072\n"
"v_accvgpr_read_b32 v198, a122\n"
"v_mfma_f32_16x16x32_bf16 v[68:71], a[216:219], a[152:155], v[68:71]\n"
"s_lshl1_add_u32 s91, s46, s91\n"
"ds_read_b64_tr_b16 a[238:239], v249 offset:61952\n"
"buffer_load_dwordx4 v252, s[16:19], s80 offen lds\n"
"v_mfma_f32_16x16x32_bf16 v[72:75], a[216:219], a[160:163], v[72:75]\n"
"s_lshl3_add_u32 s91, s68, s91\n"
"s_add_u32 m0, s81, 132096\n"
"v_accvgpr_read_b32 v199, a123\n"
"v_mfma_f32_16x16x32_bf16 v[72:75], a[220:223], a[164:167], v[72:75]\n"
"s_mul_i32 s83, s91, 5120\n"
"v_mul_f32 v192, v214, v192\n"
"v_mfma_f32_16x16x32_bf16 v[76:79], a[220:223], a[172:175], v[76:79]\n"
"s_lshr_b32 s90, s68, 1\n"
"v_mul_f32 v193, v214, v193\n"
"v_mfma_f32_16x16x32_bf16 v[76:79], a[216:219], a[168:171], v[76:79]\n"
"s_waitcnt lgkmcnt(4)\n"
"s_mul_i32 s91, s90, 40960\n"
"buffer_load_dwordx4 v253, s[16:19], s80 offen lds\n"
"v_mfma_f32_16x16x32_bf16 v[16:19], a[224:227], a[144:147], v[16:19]\n"
"s_xor_b32 s92, s68, s71\n"
"s_add_u32 m0, s81, 139264\n"
"ds_read_b64_tr_b16 a[208:209], v250 offset:49152\n"
"v_mfma_f32_16x16x32_bf16 v[16:19], a[228:231], a[148:151], v[16:19]\n"
"ds_read_b64_tr_b16 a[210:211], v250 offset:49664\n"
"v_mul_f32 v194, v214, v194\n"
"v_mfma_f32_16x16x32_bf16 v[20:23], a[228:231], a[156:159], v[20:23]\n"
"s_and_b32 s92, s92, 1\n"
"v_mul_f32 v195, v214, v195\n"
"v_mul_f32 v196, v214, v196\n"
"v_mfma_f32_16x16x32_bf16 v[20:23], a[224:227], a[152:155], v[20:23]\n"
"s_lshl1_add_u32 s93, s46, s92\n"
"ds_read_b64_tr_b16 a[212:213], v250 offset:53248\n"
"buffer_load_dwordx4 v252, s[20:23], s80 offen lds\n"
"v_mfma_f32_16x16x32_bf16 v[24:27], a[224:227], a[160:163], v[24:27]\n"
"s_mul_i32 s93, s93, 5120\n"
"s_nop 0\n"
"ds_read_b64_tr_b16 a[214:215], v250 offset:53760\n"
"v_mfma_f32_16x16x32_bf16 v[24:27], a[228:231], a[164:167], v[24:27]\n"
"s_add_u32 s91, s91, s93\n"
"s_add_u32 m0, s81, 140288\n"
"v_cmp_ne_u32 vcc, s73, v220\n"
"v_mfma_f32_16x16x32_bf16 v[28:31], a[228:231], a[172:175], v[28:31]\n"
"s_mul_i32 s93, s71, 1310720\n"
"s_cbranch_vccnz .L_f1_retry\n"
".L_f1_ok:\n"
"ds_read_b64_tr_b16 a[216:217], v250 offset:57344\n"
"v_mfma_f32_16x16x32_bf16 v[28:31], a[224:227], a[168:171], v[28:31]\n"
"s_waitcnt lgkmcnt(5)\n"
"s_add_u32 s93, s93, s91\n"
"buffer_load_dwordx4 v253, s[20:23], s80 offen lds\n"
"v_mfma_f32_16x16x32_bf16 v[80:83], a[232:235], a[144:147], v[80:83]\n"
"s_add_u32 s80, s80, 221184\n"
"s_sub_u32 s86, s80, 2211840\n"
"ds_read_b64_tr_b16 a[218:219], v250 offset:57856\n"
"v_mfma_f32_16x16x32_bf16 v[80:83], a[236:239], a[148:151], v[80:83]\n"
"s_add_u32 s41, s93, 3145728\n"
"s_min_u32 s80, s80, s86\n"
"ds_read_b64_tr_b16 a[220:221], v250 offset:61440\n"
"v_mfma_f32_16x16x32_bf16 v[84:87], a[236:239], a[156:159], v[84:87]\n"
"s_mul_i32 s43, s68, 5120\n"
"v_mul_f32 v197, v214, v197\n"
"v_mul_f32 v198, v214, v198\n"
"v_mfma_f32_16x16x32_bf16 v[84:87], a[232:235], a[152:155], v[84:87]\n"
"ds_read_b64_tr_b16 a[222:223], v250 offset:61952\n"
"v_add_u32 v236, 4096, v255\n"
"v_mfma_f32_16x16x32_bf16 v[88:91], a[232:235], a[160:163], v[88:91]\n"
"v_mul_f32 v199, v214, v199\n"
"v_cvt_pk_bf16_f32 v208, v192, v193\n"
"v_mfma_f32_16x16x32_bf16 v[88:91], a[236:239], a[164:167], v[88:91]\n"
"v_cvt_pk_bf16_f32 v209, v194, v195\n"
"v_cvt_pk_bf16_f32 v210, v196, v197\n"
"v_mfma_f32_16x16x32_bf16 v[92:95], a[236:239], a[172:175], v[92:95]\n"
"v_cvt_pk_bf16_f32 v211, v198, v199\n"
"v_accvgpr_read_b32 v192, a108\n"
"v_mfma_f32_16x16x32_bf16 v[92:95], a[232:235], a[168:171], v[92:95]\n"
"s_waitcnt lgkmcnt(4)\n"
"buffer_store_dwordx4 v[208:211], v255, s[36:39], s44 offen offset:2048 sc1\n"
"v_accvgpr_read_b32 v193, a109\n"
"v_mfma_f32_16x16x32_bf16 v[32:35], a[208:211], a[144:147], v[32:35]\n"
"ds_read_b64_tr_b16 a[224:225], v251 offset:49152\n"
"v_lshrrev_b32 v237, 1, v255\n"
"v_mfma_f32_16x16x32_bf16 v[32:35], a[212:215], a[148:151], v[32:35]\n"
"ds_read_b64_tr_b16 a[226:227], v251 offset:49664\n"
"v_accvgpr_read_b32 v194, a110\n"
"v_mfma_f32_16x16x32_bf16 v[36:39], a[212:215], a[156:159], v[36:39]\n"
"ds_read_b64_tr_b16 a[228:229], v251 offset:53248\n"
"v_accvgpr_read_b32 v195, a111\n"
"v_mfma_f32_16x16x32_bf16 v[36:39], a[208:211], a[152:155], v[36:39]\n"
"ds_read_b64_tr_b16 a[230:231], v251 offset:53760\n"
"v_accvgpr_read_b32 v196, a124\n"
"v_mfma_f32_16x16x32_bf16 v[40:43], a[208:211], a[160:163], v[40:43]\n"
"ds_read_b64_tr_b16 a[232:233], v251 offset:57344\n"
"v_add_u32 v242, 61440, v255\n"
"v_mfma_f32_16x16x32_bf16 v[40:43], a[212:215], a[164:167], v[40:43]\n"
"ds_read_b64_tr_b16 a[234:235], v251 offset:57856\n"
"v_accvgpr_read_b32 v197, a125\n"
"v_mfma_f32_16x16x32_bf16 v[44:47], a[212:215], a[172:175], v[44:47]\n"
"ds_read_b64_tr_b16 a[236:237], v251 offset:61440\n"
"v_accvgpr_read_b32 v198, a126\n"
"v_mfma_f32_16x16x32_bf16 v[44:47], a[208:211], a[168:171], v[44:47]\n"
"s_waitcnt lgkmcnt(7)\n"
"ds_read_b64_tr_b16 a[238:239], v251 offset:61952\n"
"v_accvgpr_read_b32 v199, a127\n"
"v_mfma_f32_16x16x32_bf16 v[96:99], a[216:219], a[144:147], v[96:99]\n"
"v_mul_f32 v192, v215, v192\n"
"v_mul_f32 v193, v215, v193\n"
"v_mfma_f32_16x16x32_bf16 v[96:99], a[220:223], a[148:151], v[96:99]\n"
"v_add_u32 v243, 122880, v255\n"
"v_mul_f32 v194, v215, v194\n"
"v_mfma_f32_16x16x32_bf16 v[100:103], a[220:223], a[156:159], v[100:103]\n"
"v_mul_f32 v195, v215, v195\n"
"v_mfma_f32_16x16x32_bf16 v[100:103], a[216:219], a[152:155], v[100:103]\n"
"v_mfma_f32_16x16x32_bf16 v[104:107], a[216:219], a[160:163], v[104:107]\n"
"v_mul_f32 v196, v215, v196\n"
"v_mfma_f32_16x16x32_bf16 v[104:107], a[220:223], a[164:167], v[104:107]\n"
"v_mul_f32 v197, v215, v197\n"
"v_mfma_f32_16x16x32_bf16 v[108:111], a[220:223], a[172:175], v[108:111]\n"
"v_mul_f32 v198, v215, v198\n"
"v_mfma_f32_16x16x32_bf16 v[108:111], a[216:219], a[168:171], v[108:111]\n"
"s_waitcnt lgkmcnt(0)\n"
"v_mul_f32 v199, v215, v199\n"
"v_mfma_f32_16x16x32_bf16 v[48:51], a[224:227], a[144:147], v[48:51]\n"
"s_nop 0\n"
"s_barrier\n"
"v_mfma_f32_16x16x32_bf16 v[48:51], a[228:231], a[148:151], v[48:51]\n"
"s_add_u32 m0, s43, 0\n"
"ds_read_b64_tr_b16 a[208:209], v248 offset:32768\n"
"v_cvt_pk_bf16_f32 v200, v192, v193\n"
"v_mfma_f32_16x16x32_bf16 v[52:55], a[228:231], a[156:159], v[52:55]\n"
"ds_read_b64_tr_b16 a[210:211], v248 offset:33280\n"
"buffer_load_dwordx4 v255, s[36:39], s41 offen offset:0 sc1 lds\n"
"v_mfma_f32_16x16x32_bf16 v[52:55], a[224:227], a[152:155], v[52:55]\n"
"v_cvt_pk_bf16_f32 v201, v194, v195\n"
"v_cvt_pk_bf16_f32 v202, v196, v197\n"
"v_mfma_f32_16x16x32_bf16 v[56:59], a[224:227], a[160:163], v[56:59]\n"
"ds_read_b64_tr_b16 a[212:213], v248 offset:36864\n"
"v_cvt_pk_bf16_f32 v203, v198, v199\n"
"v_mfma_f32_16x16x32_bf16 v[56:59], a[228:231], a[164:167], v[56:59]\n"
"ds_read_b64_tr_b16 a[214:215], v248 offset:37376\n"
"v_mfma_f32_16x16x32_bf16 v[60:63], a[228:231], a[172:175], v[60:63]\n"
"buffer_load_dwordx4 v255, s[36:39], s41 offen offset:1024 sc1 lds\n"
"buffer_store_dwordx4 v[200:203], v255, s[36:39], s44 offen offset:3072 sc1\n"
"v_mfma_f32_16x16x32_bf16 v[60:63], a[224:227], a[168:171], v[60:63]\n"
"s_add_u32 s44, s44, 8192\n"
"ds_read_b64_tr_b16 a[216:217], v248 offset:40960\n"
"v_mfma_f32_16x16x32_bf16 v[112:115], a[232:235], a[144:147], v[112:115]\n"
"ds_read_b64_tr_b16 a[218:219], v248 offset:41472\n"
"v_mfma_f32_16x16x32_bf16 v[112:115], a[236:239], a[148:151], v[112:115]\n"
"v_mfma_f32_16x16x32_bf16 v[116:119], a[236:239], a[156:159], v[116:119]\n"
"ds_read_b64_tr_b16 a[220:221], v248 offset:45056\n"
"buffer_load_dwordx4 v255, s[36:39], s41 offen offset:2048 sc1 lds\n"
"v_mfma_f32_16x16x32_bf16 v[116:119], a[232:235], a[152:155], v[116:119]\n"
"ds_read_b64_tr_b16 a[222:223], v248 offset:45568\n"
"v_mfma_f32_16x16x32_bf16 v[120:123], a[232:235], a[160:163], v[120:123]\n"
"v_mfma_f32_16x16x32_bf16 v[120:123], a[236:239], a[164:167], v[120:123]\n"
"v_mfma_f32_16x16x32_bf16 v[124:127], a[236:239], a[172:175], v[124:127]\n"
"s_waitcnt vmcnt(20)\n"
"buffer_load_dwordx4 v255, s[36:39], s41 offen offset:3072 sc1 lds\n"
"v_mfma_f32_16x16x32_bf16 v[124:127], a[232:235], a[168:171], v[124:127]\n"
"s_waitcnt lgkmcnt(4)\n"
"s_add_u32 m0, s43, 4096\n"
"v_mfma_f32_16x16x32_bf16 v[0:3], a[208:211], a[176:179], v[0:3]\n"
"ds_read_b64_tr_b16 a[224:225], v249 offset:32768\n"
"v_mfma_f32_16x16x32_bf16 v[0:3], a[212:215], a[180:183], v[0:3]\n"
"ds_read_b64_tr_b16 a[226:227], v249 offset:33280\n"
"v_mfma_f32_16x16x32_bf16 v[4:7], a[212:215], a[188:191], v[4:7]\n"
"buffer_load_dwordx4 v236, s[36:39], s41 offen sc1 lds\n"
"v_mfma_f32_16x16x32_bf16 v[4:7], a[208:211], a[184:187], v[4:7]\n"
"s_add_u32 s41, s41, 81920\n"
"s_add_u32 s76, s79, s58\n"
"ds_read_b64_tr_b16 a[228:229], v249 offset:36864\n"
"v_mfma_f32_16x16x32_bf16 v[8:11], a[208:211], a[192:195], v[8:11]\n"
"s_add_u32 s58, s58, 2048\n"
"s_sub_u32 s98, s58, 20480\n"
"ds_read_b64_tr_b16 a[230:231], v249 offset:37376\n"
"v_mfma_f32_16x16x32_bf16 v[8:11], a[212:215], a[196:199], v[8:11]\n"
"s_min_u32 s58, s58, s98\n"
"v_mfma_f32_16x16x32_bf16 v[12:15], a[212:215], a[204:207], v[12:15]\n"
"ds_read_b64_tr_b16 a[232:233], v249 offset:40960\n"
"buffer_load_dwordx4 v[160:163], v255, s[36:39], s76 offen offset:0 sc1\n"
"v_mfma_f32_16x16x32_bf16 v[12:15], a[208:211], a[200:203], v[12:15]\n"
"s_waitcnt lgkmcnt(5)\n"
"v_mfma_f32_16x16x32_bf16 v[64:67], a[216:219], a[176:179], v[64:67]\n"
"ds_read_b64_tr_b16 a[234:235], v249 offset:41472\n"
"v_mfma_f32_16x16x32_bf16 v[64:67], a[220:223], a[180:183], v[64:67]\n"
"ds_read_b64_tr_b16 a[236:237], v249 offset:45056\n"
"v_mfma_f32_16x16x32_bf16 v[68:71], a[220:223], a[188:191], v[68:71]\n"
"buffer_load_dwordx4 v[164:167], v255, s[36:39], s76 offen offset:1024 sc1\n"
"v_mfma_f32_16x16x32_bf16 v[68:71], a[216:219], a[184:187], v[68:71]\n"
"ds_read_b64_tr_b16 a[238:239], v249 offset:45568\n"
"v_mfma_f32_16x16x32_bf16 v[72:75], a[216:219], a[192:195], v[72:75]\n"
"v_mfma_f32_16x16x32_bf16 v[72:75], a[220:223], a[196:199], v[72:75]\n"
"v_mfma_f32_16x16x32_bf16 v[76:79], a[220:223], a[204:207], v[76:79]\n"
"buffer_load_dwordx4 v[168:171], v254, s[36:39], s76 offen offset:0 sc1\n"
"v_mfma_f32_16x16x32_bf16 v[76:79], a[216:219], a[200:203], v[76:79]\n"
"s_waitcnt lgkmcnt(4)\n"
"v_mfma_f32_16x16x32_bf16 v[16:19], a[224:227], a[176:179], v[16:19]\n"
"ds_read_b64_tr_b16 a[208:209], v250 offset:32768\n"
"v_mfma_f32_16x16x32_bf16 v[16:19], a[228:231], a[180:183], v[16:19]\n"
"ds_read_b64_tr_b16 a[210:211], v250 offset:33280\n"
"v_mfma_f32_16x16x32_bf16 v[20:23], a[228:231], a[188:191], v[20:23]\n"
"buffer_load_dwordx4 v[172:175], v254, s[36:39], s76 offen offset:1024 sc1\n"
"v_mfma_f32_16x16x32_bf16 v[20:23], a[224:227], a[184:187], v[20:23]\n"
"ds_read_b64_tr_b16 a[212:213], v250 offset:36864\n"
"v_mfma_f32_16x16x32_bf16 v[24:27], a[224:227], a[192:195], v[24:27]\n"
"ds_read_b64_tr_b16 a[214:215], v250 offset:37376\n"
"v_mfma_f32_16x16x32_bf16 v[24:27], a[228:231], a[196:199], v[24:27]\n"
"v_mfma_f32_16x16x32_bf16 v[28:31], a[228:231], a[204:207], v[28:31]\n"
"ds_read_b64_tr_b16 a[216:217], v250 offset:40960\n"
"buffer_load_dwordx4 v[176:179], v226, s[36:39], s76 offen offset:0 sc1\n"
"v_mfma_f32_16x16x32_bf16 v[28:31], a[224:227], a[200:203], v[28:31]\n"
"s_waitcnt lgkmcnt(5)\n"
"v_mfma_f32_16x16x32_bf16 v[80:83], a[232:235], a[176:179], v[80:83]\n"
"ds_read_b64_tr_b16 a[218:219], v250 offset:41472\n"
"v_mfma_f32_16x16x32_bf16 v[80:83], a[236:239], a[180:183], v[80:83]\n"
"ds_read_b64_tr_b16 a[220:221], v250 offset:45056\n"
"v_mfma_f32_16x16x32_bf16 v[84:87], a[236:239], a[188:191], v[84:87]\n"
"buffer_load_dwordx4 v[180:183], v226, s[36:39], s76 offen offset:1024 sc1\n"
"v_mfma_f32_16x16x32_bf16 v[84:87], a[232:235], a[184:187], v[84:87]\n"
"ds_read_b64_tr_b16 a[222:223], v250 offset:45568\n"
"v_mfma_f32_16x16x32_bf16 v[88:91], a[232:235], a[192:195], v[88:91]\n"
"v_mfma_f32_16x16x32_bf16 v[88:91], a[236:239], a[196:199], v[88:91]\n"
"v_mfma_f32_16x16x32_bf16 v[92:95], a[236:239], a[204:207], v[92:95]\n"
"buffer_load_dwordx4 v[184:187], v217, s[36:39], s76 offen offset:0 sc1\n"
"v_mfma_f32_16x16x32_bf16 v[92:95], a[232:235], a[200:203], v[92:95]\n"
"s_waitcnt lgkmcnt(4)\n"
"v_mfma_f32_16x16x32_bf16 v[32:35], a[208:211], a[176:179], v[32:35]\n"
"ds_read_b64_tr_b16 a[224:225], v251 offset:32768\n"
"v_mfma_f32_16x16x32_bf16 v[32:35], a[212:215], a[180:183], v[32:35]\n"
"ds_read_b64_tr_b16 a[226:227], v251 offset:33280\n"
"v_mfma_f32_16x16x32_bf16 v[36:39], a[212:215], a[188:191], v[36:39]\n"
"ds_read_b64_tr_b16 a[228:229], v251 offset:36864\n"
"buffer_load_dwordx4 v[188:191], v217, s[36:39], s76 offen offset:1024 sc1\n"
"v_mfma_f32_16x16x32_bf16 v[36:39], a[208:211], a[184:187], v[36:39]\n"
"s_add_u32 m0, s81, 147456\n"
"ds_read_b64_tr_b16 a[230:231], v251 offset:37376\n"
"v_mfma_f32_16x16x32_bf16 v[40:43], a[208:211], a[192:195], v[40:43]\n"
"ds_read_b64_tr_b16 a[232:233], v251 offset:40960\n"
"v_mfma_f32_16x16x32_bf16 v[40:43], a[212:215], a[196:199], v[40:43]\n"
"ds_read_b64_tr_b16 a[234:235], v251 offset:41472\n"
"v_mfma_f32_16x16x32_bf16 v[44:47], a[212:215], a[204:207], v[44:47]\n"
"ds_read_b64_tr_b16 a[236:237], v251 offset:45056\n"
"buffer_load_dwordx4 v252, s[16:19], s80 offen lds\n"
"v_mfma_f32_16x16x32_bf16 v[44:47], a[208:211], a[200:203], v[44:47]\n"
"s_waitcnt lgkmcnt(7)\n"
"s_add_u32 m0, s81, 148480\n"
"ds_read_b64_tr_b16 a[238:239], v251 offset:45568\n"
"v_mfma_f32_16x16x32_bf16 v[96:99], a[216:219], a[176:179], v[96:99]\n"
"v_mfma_f32_16x16x32_bf16 v[96:99], a[220:223], a[180:183], v[96:99]\n"
"v_mfma_f32_16x16x32_bf16 v[100:103], a[220:223], a[188:191], v[100:103]\n"
"buffer_load_dwordx4 v253, s[16:19], s80 offen lds\n"
"v_mfma_f32_16x16x32_bf16 v[100:103], a[216:219], a[184:187], v[100:103]\n"
"s_add_u32 m0, s81, 155648\n"
"v_mfma_f32_16x16x32_bf16 v[104:107], a[216:219], a[192:195], v[104:107]\n"
"v_mfma_f32_16x16x32_bf16 v[104:107], a[220:223], a[196:199], v[104:107]\n"
"v_mfma_f32_16x16x32_bf16 v[108:111], a[220:223], a[204:207], v[108:111]\n"
"buffer_load_dwordx4 v252, s[20:23], s80 offen lds\n"
"v_mfma_f32_16x16x32_bf16 v[108:111], a[216:219], a[200:203], v[108:111]\n"
"s_waitcnt lgkmcnt(0)\n"
"s_add_u32 m0, s81, 156672\n"
"v_mfma_f32_16x16x32_bf16 v[48:51], a[224:227], a[176:179], v[48:51]\n"
"s_waitcnt vmcnt(14)\n"
"s_barrier\n"
"v_mfma_f32_16x16x32_bf16 v[48:51], a[228:231], a[180:183], v[48:51]\n"
"s_lshl_b32 s90, s75, 6\n"
"s_add_u32 s90, s90, 2686976\n"
"ds_read_b64_tr_b16 a[208:209], v244 offset:32768\n"
"v_mfma_f32_16x16x32_bf16 v[52:55], a[228:231], a[188:191], v[52:55]\n"
"ds_read_b64_tr_b16 a[210:211], v244 offset:33280\n"
"v_mov_b32 v218, s73\n"
"v_mfma_f32_16x16x32_bf16 v[52:55], a[224:227], a[184:187], v[52:55]\n"
"buffer_store_dword v218, v225, s[36:39], s90 offen sc1\n"
"buffer_load_dwordx4 v253, s[20:23], s80 offen lds\n"
"v_mfma_f32_16x16x32_bf16 v[56:59], a[224:227], a[192:195], v[56:59]\n"
"s_add_u32 s80, s80, 221184\n"
"s_sub_u32 s86, s80, 2211840\n"
"ds_read_b64_tr_b16 a[212:213], v244 offset:36864\n"
"v_mfma_f32_16x16x32_bf16 v[56:59], a[228:231], a[196:199], v[56:59]\n"
"s_min_u32 s80, s80, s86\n"
"s_add_u32 s76, s79, s58\n"
"ds_read_b64_tr_b16 a[214:215], v244 offset:37376\n"
"v_mfma_f32_16x16x32_bf16 v[60:63], a[228:231], a[204:207], v[60:63]\n"
"s_add_u32 s58, s58, 2048\n"
"s_sub_u32 s98, s58, 20480\n"
"s_min_u32 s58, s58, s98\n"
"v_mfma_f32_16x16x32_bf16 v[60:63], a[224:227], a[200:203], v[60:63]\n"
"ds_read_b64_tr_b16 a[216:217], v244 offset:40960\n"
"buffer_load_dwordx4 a[0:3], v255, s[36:39], s76 offen offset:0 sc1\n"
"v_mfma_f32_16x16x32_bf16 v[112:115], a[232:235], a[176:179], v[112:115]\n"
"ds_read_b64_tr_b16 a[218:219], v244 offset:41472\n"
"v_mfma_f32_16x16x32_bf16 v[112:115], a[236:239], a[180:183], v[112:115]\n"
"v_mfma_f32_16x16x32_bf16 v[116:119], a[236:239], a[188:191], v[116:119]\n"
"ds_read_b64_tr_b16 a[220:221], v244 offset:45056\n"
"v_mfma_f32_16x16x32_bf16 v[116:119], a[232:235], a[184:187], v[116:119]\n"
"ds_read_b64_tr_b16 a[222:223], v244 offset:45568\n"
"buffer_load_dwordx4 a[4:7], v255, s[36:39], s76 offen offset:1024 sc1\n"
"v_mfma_f32_16x16x32_bf16 v[120:123], a[232:235], a[192:195], v[120:123]\n"
"v_mfma_f32_16x16x32_bf16 v[120:123], a[236:239], a[196:199], v[120:123]\n"
"v_mfma_f32_16x16x32_bf16 v[124:127], a[236:239], a[204:207], v[124:127]\n"
"s_nop 0\n"
"v_mfma_f32_16x16x32_bf16 v[124:127], a[232:235], a[200:203], v[124:127]\n"
"s_waitcnt lgkmcnt(4)\n"
"buffer_load_dwordx4 a[8:11], v254, s[36:39], s76 offen offset:0 sc1\n"
"v_mfma_f32_16x16x32_bf16 v[0:3], a[208:211], v[128:131], v[0:3]\n"
"ds_read_b64_tr_b16 a[224:225], v245 offset:32768\n"
"v_mfma_f32_16x16x32_bf16 v[0:3], a[212:215], v[132:135], v[0:3]\n"
"ds_read_b64_tr_b16 a[226:227], v245 offset:33280\n"
"v_mfma_f32_16x16x32_bf16 v[4:7], a[212:215], v[140:143], v[4:7]\n"
"v_mfma_f32_16x16x32_bf16 v[4:7], a[208:211], v[136:139], v[4:7]\n"
"ds_read_b64_tr_b16 a[228:229], v245 offset:36864\n"
"buffer_load_dwordx4 a[12:15], v254, s[36:39], s76 offen offset:1024 sc1\n"
"v_mfma_f32_16x16x32_bf16 v[8:11], a[208:211], v[144:147], v[8:11]\n"
"ds_read_b64_tr_b16 a[230:231], v245 offset:37376\n"
"v_mfma_f32_16x16x32_bf16 v[8:11], a[212:215], v[148:151], v[8:11]\n"
"v_mfma_f32_16x16x32_bf16 v[12:15], a[212:215], v[156:159], v[12:15]\n"
"ds_read_b64_tr_b16 a[232:233], v245 offset:40960\n"
"v_mfma_f32_16x16x32_bf16 v[12:15], a[208:211], v[152:155], v[12:15]\n"
"s_waitcnt lgkmcnt(5)\n"
"buffer_load_dwordx4 a[16:19], v226, s[36:39], s76 offen offset:0 sc1\n"
"v_mfma_f32_16x16x32_bf16 v[64:67], a[216:219], v[128:131], v[64:67]\n"
"ds_read_b64_tr_b16 a[234:235], v245 offset:41472\n"
"v_mfma_f32_16x16x32_bf16 v[64:67], a[220:223], v[132:135], v[64:67]\n"
"ds_read_b64_tr_b16 a[236:237], v245 offset:45056\n"
"v_mfma_f32_16x16x32_bf16 v[68:71], a[220:223], v[140:143], v[68:71]\n"
"v_mfma_f32_16x16x32_bf16 v[68:71], a[216:219], v[136:139], v[68:71]\n"
"ds_read_b64_tr_b16 a[238:239], v245 offset:45568\n"
"buffer_load_dwordx4 a[20:23], v226, s[36:39], s76 offen offset:1024 sc1\n"
"v_mfma_f32_16x16x32_bf16 v[72:75], a[216:219], v[144:147], v[72:75]\n"
"v_mfma_f32_16x16x32_bf16 v[72:75], a[220:223], v[148:151], v[72:75]\n"
"v_mfma_f32_16x16x32_bf16 v[76:79], a[220:223], v[156:159], v[76:79]\n"
"v_mfma_f32_16x16x32_bf16 v[76:79], a[216:219], v[152:155], v[76:79]\n"
"s_waitcnt lgkmcnt(4)\n"
"buffer_load_dwordx4 a[24:27], v217, s[36:39], s76 offen offset:0 sc1\n"
"v_mfma_f32_16x16x32_bf16 v[16:19], a[224:227], v[128:131], v[16:19]\n"
"ds_read_b64_tr_b16 a[208:209], v246 offset:32768\n"
"v_mfma_f32_16x16x32_bf16 v[16:19], a[228:231], v[132:135], v[16:19]\n"
"ds_read_b64_tr_b16 a[210:211], v246 offset:33280\n"
"v_mfma_f32_16x16x32_bf16 v[20:23], a[228:231], v[140:143], v[20:23]\n"
"v_mfma_f32_16x16x32_bf16 v[20:23], a[224:227], v[136:139], v[20:23]\n"
"ds_read_b64_tr_b16 a[212:213], v246 offset:36864\n"
"buffer_load_dwordx4 a[28:31], v217, s[36:39], s76 offen offset:1024 sc1\n"
"v_mfma_f32_16x16x32_bf16 v[24:27], a[224:227], v[144:147], v[24:27]\n"
"s_add_u32 m0, s43, 61440\n"
"ds_read_b64_tr_b16 a[214:215], v246 offset:37376\n"
"v_mfma_f32_16x16x32_bf16 v[24:27], a[228:231], v[148:151], v[24:27]\n"
"v_mfma_f32_16x16x32_bf16 v[28:31], a[228:231], v[156:159], v[28:31]\n"
"ds_read_b64_tr_b16 a[216:217], v246 offset:40960\n"
"v_mfma_f32_16x16x32_bf16 v[28:31], a[224:227], v[152:155], v[28:31]\n"
"s_waitcnt lgkmcnt(5)\n"
"buffer_load_dwordx4 v255, s[36:39], s41 offen offset:0 sc1 lds\n"
"v_mfma_f32_16x16x32_bf16 v[80:83], a[232:235], v[128:131], v[80:83]\n"
"ds_read_b64_tr_b16 a[218:219], v246 offset:41472\n"
"v_mfma_f32_16x16x32_bf16 v[80:83], a[236:239], v[132:135], v[80:83]\n"
"ds_read_b64_tr_b16 a[220:221], v246 offset:45056\n"
"v_mfma_f32_16x16x32_bf16 v[84:87], a[236:239], v[140:143], v[84:87]\n"
"v_mfma_f32_16x16x32_bf16 v[84:87], a[232:235], v[136:139], v[84:87]\n"
"ds_read_b64_tr_b16 a[222:223], v246 offset:45568\n"
"buffer_load_dwordx4 v255, s[36:39], s41 offen offset:1024 sc1 lds\n"
"v_mfma_f32_16x16x32_bf16 v[88:91], a[232:235], v[144:147], v[88:91]\n"
"v_mfma_f32_16x16x32_bf16 v[88:91], a[236:239], v[148:151], v[88:91]\n"
"v_mfma_f32_16x16x32_bf16 v[92:95], a[236:239], v[156:159], v[92:95]\n"
"v_mfma_f32_16x16x32_bf16 v[92:95], a[232:235], v[152:155], v[92:95]\n"
"s_waitcnt lgkmcnt(4)\n"
"buffer_load_dwordx4 v255, s[36:39], s41 offen offset:2048 sc1 lds\n"
"v_mfma_f32_16x16x32_bf16 v[32:35], a[208:211], v[128:131], v[32:35]\n"
"ds_read_b64_tr_b16 a[224:225], v247 offset:32768\n"
"v_mfma_f32_16x16x32_bf16 v[32:35], a[212:215], v[132:135], v[32:35]\n"
"ds_read_b64_tr_b16 a[226:227], v247 offset:33280\n"
"v_mfma_f32_16x16x32_bf16 v[36:39], a[212:215], v[140:143], v[36:39]\n"
"ds_read_b64_tr_b16 a[228:229], v247 offset:36864\n"
"v_mfma_f32_16x16x32_bf16 v[36:39], a[208:211], v[136:139], v[36:39]\n"
"ds_read_b64_tr_b16 a[230:231], v247 offset:37376\n"
"buffer_load_dwordx4 v255, s[36:39], s41 offen offset:3072 sc1 lds\n"
"v_mfma_f32_16x16x32_bf16 v[40:43], a[208:211], v[144:147], v[40:43]\n"
"s_add_u32 m0, s43, 65536\n"
"ds_read_b64_tr_b16 a[232:233], v247 offset:40960\n"
"v_mfma_f32_16x16x32_bf16 v[40:43], a[212:215], v[148:151], v[40:43]\n"
"ds_read_b64_tr_b16 a[234:235], v247 offset:41472\n"
"v_mfma_f32_16x16x32_bf16 v[44:47], a[212:215], v[156:159], v[44:47]\n"
"ds_read_b64_tr_b16 a[236:237], v247 offset:45056\n"
"v_mfma_f32_16x16x32_bf16 v[44:47], a[208:211], v[152:155], v[44:47]\n"
"s_waitcnt lgkmcnt(7)\n"
"ds_read_b64_tr_b16 a[238:239], v247 offset:45568\n"
"buffer_load_dwordx4 v236, s[36:39], s41 offen sc1 lds\n"
"v_mfma_f32_16x16x32_bf16 v[96:99], a[216:219], v[128:131], v[96:99]\n"
"s_add_u32 s41, s41, 81920\n"
"s_add_u32 m0, s43, 81920\n"
"v_mfma_f32_16x16x32_bf16 v[96:99], a[220:223], v[132:135], v[96:99]\n"
".L_pad11:\n"
"s_cbranch_execz .L_pad15\n"
"v_mfma_f32_16x16x32_bf16 v[100:103], a[220:223], v[140:143], v[100:103]\n"
"v_mfma_f32_16x16x32_bf16 v[100:103], a[216:219], v[136:139], v[100:103]\n"
"buffer_load_dwordx4 v255, s[36:39], s41 offen offset:0 sc1 lds\n"
"v_mfma_f32_16x16x32_bf16 v[104:107], a[216:219], v[144:147], v[104:107]\n"
"v_mfma_f32_16x16x32_bf16 v[104:107], a[220:223], v[148:151], v[104:107]\n"
"v_mfma_f32_16x16x32_bf16 v[108:111], a[220:223], v[156:159], v[108:111]\n"
"v_mfma_f32_16x16x32_bf16 v[108:111], a[216:219], v[152:155], v[108:111]\n"
"s_waitcnt lgkmcnt(0)\n"
"buffer_load_dwordx4 v255, s[36:39], s41 offen offset:1024 sc1 lds\n"
"v_mfma_f32_16x16x32_bf16 v[48:51], a[224:227], v[128:131], v[48:51]\n"
"s_waitcnt vmcnt(15)\n"
"s_barrier\n"
"v_mfma_f32_16x16x32_bf16 v[48:51], a[228:231], v[132:135], v[48:51]\n"
"ds_read_b64_tr_b16 a[208:209], v232 offset:0\n"
"v_mfma_f32_16x16x32_bf16 v[52:55], a[228:231], v[140:143], v[52:55]\n"
"ds_read_b64_tr_b16 a[210:211], v232 offset:512\n"
"v_mfma_f32_16x16x32_bf16 v[52:55], a[224:227], v[136:139], v[52:55]\n"
"buffer_load_dwordx4 v255, s[36:39], s41 offen offset:2048 sc1 lds\n"
"v_mfma_f32_16x16x32_bf16 v[56:59], a[224:227], v[144:147], v[56:59]\n"
"ds_read_b64_tr_b16 a[212:213], v232 offset:4096\n"
"v_mfma_f32_16x16x32_bf16 v[56:59], a[228:231], v[148:151], v[56:59]\n"
"ds_read_b64_tr_b16 a[214:215], v232 offset:4608\n"
"v_mfma_f32_16x16x32_bf16 v[60:63], a[228:231], v[156:159], v[60:63]\n"
"v_mfma_f32_16x16x32_bf16 v[60:63], a[224:227], v[152:155], v[60:63]\n"
"ds_read_b64_tr_b16 a[216:217], v232 offset:8192\n"
"buffer_load_dwordx4 v255, s[36:39], s41 offen offset:3072 sc1 lds\n"
"v_mfma_f32_16x16x32_bf16 v[112:115], a[232:235], v[128:131], v[112:115]\n"
"s_add_u32 m0, s43, 86016\n"
"ds_read_b64_tr_b16 a[218:219], v232 offset:8704\n"
"v_mfma_f32_16x16x32_bf16 v[112:115], a[236:239], v[132:135], v[112:115]\n"
"v_mfma_f32_16x16x32_bf16 v[116:119], a[236:239], v[140:143], v[116:119]\n"
"ds_read_b64_tr_b16 a[220:221], v232 offset:12288\n"
"v_mfma_f32_16x16x32_bf16 v[116:119], a[232:235], v[136:139], v[116:119]\n"
"ds_read_b64_tr_b16 a[222:223], v232 offset:12800\n"
"buffer_load_dwordx4 v236, s[36:39], s41 offen sc1 lds\n"
"v_mfma_f32_16x16x32_bf16 v[120:123], a[232:235], v[144:147], v[120:123]\n"
"s_add_u32 s41, s41, 81920\n"
"s_add_u32 m0, s43, 102400\n"
"v_mfma_f32_16x16x32_bf16 v[120:123], a[236:239], v[148:151], v[120:123]\n"
"v_mfma_f32_16x16x32_bf16 v[124:127], a[236:239], v[156:159], v[124:127]\n"
"s_nop 0\n"
"v_and_b32 v221, 7, v229\n"
"v_lshlrev_b32 v221, 6, v221\n"
"v_mfma_f32_16x16x32_bf16 v[124:127], a[232:235], v[152:155], v[124:127]\n"
"s_waitcnt lgkmcnt(4)\n"
"s_and_b32 s95, s75, 0xf8\n"
"s_lshl_b32 s95, s95, 6\n"
"v_mfma_f32_16x16x32_bf16 v[0:3], a[208:211], v[160:163], v[0:3]\n"
"s_add_u32 s95, s95, 2686976\n"
"ds_read_b64_tr_b16 a[224:225], v232 offset:16384\n"
"buffer_load_dword v220, v221, s[36:39], s95 offen sc1\n"
"v_mfma_f32_16x16x32_bf16 v[0:3], a[212:215], v[164:167], v[0:3]\n"
"s_lshr_b32 s90, s68, 1\n"
"ds_read_b64_tr_b16 a[226:227], v232 offset:16896\n"
"buffer_load_dwordx4 v255, s[36:39], s41 offen offset:0 sc1 lds\n"
"v_mfma_f32_16x16x32_bf16 v[4:7], a[212:215], v[172:175], v[4:7]\n"
"ds_read_b128 a[32:35], v255 offset:0\n"
"v_mfma_f32_16x16x32_bf16 v[4:7], a[208:211], v[168:171], v[4:7]\n"
"s_lshl1_add_u32 s90, s69, s90\n"
"ds_read_b64_tr_b16 a[228:229], v232 offset:20480\n"
"v_mfma_f32_16x16x32_bf16 v[8:11], a[208:211], v[176:179], v[8:11]\n"
"ds_read_b64_tr_b16 a[230:231], v232 offset:20992\n"
"v_mfma_f32_16x16x32_bf16 v[8:11], a[212:215], v[180:183], v[8:11]\n"
"buffer_load_dwordx4 v255, s[36:39], s41 offen offset:1024 sc1 lds\n"
"v_mfma_f32_16x16x32_bf16 v[12:15], a[212:215], v[188:191], v[12:15]\n"
"s_mul_i32 s90, s90, 442368\n"
"ds_read_b64_tr_b16 a[232:233], v232 offset:24576\n"
"v_mfma_f32_16x16x32_bf16 v[12:15], a[208:211], v[184:187], v[12:15]\n"
"s_waitcnt lgkmcnt(6)\n"
"v_mfma_f32_16x16x32_bf16 v[64:67], a[216:219], v[160:163], v[64:67]\n"
"ds_read_b64_tr_b16 a[234:235], v232 offset:25088\n"
"v_mfma_f32_16x16x32_bf16 v[64:67], a[220:223], v[164:167], v[64:67]\n"
"ds_read_b64_tr_b16 a[236:237], v232 offset:28672\n"
"buffer_load_dwordx4 v255, s[36:39], s41 offen offset:2048 sc1 lds\n"
"v_mfma_f32_16x16x32_bf16 v[68:71], a[220:223], v[172:175], v[68:71]\n"
"s_lshl_b32 s91, s59, 14\n"
"ds_read_b128 a[36:39], v255 offset:10240\n"
"v_mfma_f32_16x16x32_bf16 v[68:71], a[216:219], v[168:171], v[68:71]\n"
"ds_read_b64_tr_b16 a[238:239], v232 offset:29184\n"
"v_mfma_f32_16x16x32_bf16 v[72:75], a[216:219], v[176:179], v[72:75]\n"
"v_mfma_f32_16x16x32_bf16 v[72:75], a[220:223], v[180:183], v[72:75]\n"
"s_add_u32 s90, s90, s91\n"
"buffer_load_dwordx4 v255, s[36:39], s41 offen offset:3072 sc1 lds\n"
"v_mfma_f32_16x16x32_bf16 v[76:79], a[220:223], v[188:191], v[76:79]\n"
"s_waitcnt vmcnt(15)\n"
"s_add_u32 m0, s43, 106496\n"
"ds_read_b128 a[40:43], v255 offset:1024\n"
"v_mfma_f32_16x16x32_bf16 v[76:79], a[216:219], v[184:187], v[76:79]\n"
"s_waitcnt lgkmcnt(6)\n"
"v_mfma_f32_16x16x32_bf16 v[0:3], a[224:227], a[0:3], v[0:3]\n"
"s_and_b32 s91, s68, 1\n"
"ds_read_b64_tr_b16 a[208:209], v233 offset:0\n"
"v_mfma_f32_16x16x32_bf16 v[0:3], a[228:231], a[4:7], v[0:3]\n"
"ds_read_b64_tr_b16 a[210:211], v233 offset:512\n"
"buffer_load_dwordx4 v236, s[36:39], s41 offen sc1 lds\n"
"v_mfma_f32_16x16x32_bf16 v[4:7], a[228:231], a[12:15], v[4:7]\n"
"s_add_u32 s41, s41, 81920\n"
"s_add_u32 m0, s43, 20480\n"
"v_mfma_f32_16x16x32_bf16 v[4:7], a[224:227], a[8:11], v[4:7]\n"
"ds_read_b64_tr_b16 a[212:213], v233 offset:4096\n"
"v_mfma_f32_16x16x32_bf16 v[8:11], a[224:227], a[16:19], v[8:11]\n"
"s_lshl_b32 s91, s91, 12\n"
"ds_read_b64_tr_b16 a[214:215], v233 offset:4608\n"
"v_mfma_f32_16x16x32_bf16 v[8:11], a[228:231], a[20:23], v[8:11]\n"
"ds_read_b128 a[44:47], v255 offset:11264\n"
"buffer_load_dwordx4 v255, s[36:39], s41 offen offset:0 sc1 lds\n"
"v_mfma_f32_16x16x32_bf16 v[12:15], a[228:231], a[28:31], v[12:15]\n"
"ds_read_b64_tr_b16 a[216:217], v233 offset:8192\n"
"v_mfma_f32_16x16x32_bf16 v[12:15], a[224:227], a[24:27], v[12:15]\n"
"s_waitcnt lgkmcnt(7)\n"
"s_add_u32 s90, s90, s91\n"
"v_mfma_f32_16x16x32_bf16 v[64:67], a[232:235], a[0:3], v[64:67]\n"
"ds_read_b64_tr_b16 a[218:219], v233 offset:8704\n"
"v_mfma_f32_16x16x32_bf16 v[64:67], a[236:239], a[4:7], v[64:67]\n"
"ds_read_b64_tr_b16 a[220:221], v233 offset:12288\n"
"buffer_load_dwordx4 v255, s[36:39], s41 offen offset:1024 sc1 lds\n"
"v_mfma_f32_16x16x32_bf16 v[68:71], a[236:239], a[12:15], v[68:71]\n"
"s_add_u32 s85, s90, 11534336\n"
"ds_read_b128 a[48:51], v255 offset:2048\n"
"v_mfma_f32_16x16x32_bf16 v[68:71], a[232:235], a[8:11], v[68:71]\n"
"ds_read_b64_tr_b16 a[222:223], v233 offset:12800\n"
"v_mfma_f32_16x16x32_bf16 v[72:75], a[232:235], a[16:19], v[72:75]\n"
"v_mfma_f32_16x16x32_bf16 v[72:75], a[236:239], a[20:23], v[72:75]\n"
"buffer_load_dwordx4 v255, s[36:39], s41 offen offset:2048 sc1 lds\n"
"v_mfma_f32_16x16x32_bf16 v[76:79], a[236:239], a[28:31], v[76:79]\n"
"v_mfma_f32_16x16x32_bf16 v[76:79], a[232:235], a[24:27], v[76:79]\n"
"s_waitcnt lgkmcnt(6)\n"
"v_mfma_f32_16x16x32_bf16 v[16:19], a[208:211], v[160:163], v[16:19]\n"
"ds_read_b64_tr_b16 a[224:225], v233 offset:16384\n"
"v_mfma_f32_16x16x32_bf16 v[16:19], a[212:215], v[164:167], v[16:19]\n"
"ds_read_b64_tr_b16 a[226:227], v233 offset:16896\n"
"buffer_load_dwordx4 v255, s[36:39], s41 offen offset:3072 sc1 lds\n"
"v_mfma_f32_16x16x32_bf16 v[20:23], a[212:215], v[172:175], v[20:23]\n"
"s_add_u32 m0, s43, 24576\n"
"ds_read_b128 a[52:55], v255 offset:12288\n"
"v_mfma_f32_16x16x32_bf16 v[20:23], a[208:211], v[168:171], v[20:23]\n"
"ds_read_b64_tr_b16 a[228:229], v233 offset:20480\n"
"v_mfma_f32_16x16x32_bf16 v[24:27], a[208:211], v[176:179], v[24:27]\n"
"ds_read_b64_tr_b16 a[230:231], v233 offset:20992\n"
"v_mfma_f32_16x16x32_bf16 v[24:27], a[212:215], v[180:183], v[24:27]\n"
"buffer_load_dwordx4 v236, s[36:39], s41 offen sc1 lds\n"
"v_mfma_f32_16x16x32_bf16 v[28:31], a[212:215], v[188:191], v[28:31]\n"
"s_add_u32 s41, s41, 81920\n"
"s_add_u32 m0, s43, 40960\n"
"ds_read_b64_tr_b16 a[232:233], v233 offset:24576\n"
"v_mfma_f32_16x16x32_bf16 v[28:31], a[208:211], v[184:187], v[28:31]\n"
"s_waitcnt lgkmcnt(6)\n"
"ds_read_b128 a[56:59], v255 offset:3072\n"
"v_mfma_f32_16x16x32_bf16 v[80:83], a[216:219], v[160:163], v[80:83]\n"
"ds_read_b64_tr_b16 a[234:235], v233 offset:25088\n"
"v_mfma_f32_16x16x32_bf16 v[80:83], a[220:223], v[164:167], v[80:83]\n"
"ds_read_b64_tr_b16 a[236:237], v233 offset:28672\n"
"buffer_load_dwordx4 v255, s[36:39], s41 offen offset:0 sc1 lds\n"
"v_mfma_f32_16x16x32_bf16 v[84:87], a[220:223], v[172:175], v[84:87]\n"
"v_exp_f32_e64 v208, -v0\n"
"v_mfma_f32_16x16x32_bf16 v[84:87], a[216:219], v[168:171], v[84:87]\n"
"ds_read_b64_tr_b16 a[238:239], v233 offset:29184\n"
"v_mul_f32 v0, v0, v64\n"
"v_mfma_f32_16x16x32_bf16 v[88:91], a[216:219], v[176:179], v[88:91]\n"
"ds_read_b128 a[60:63], v255 offset:13312\n"
"v_add_f32 v208, 1.0, v208\n"
"v_mfma_f32_16x16x32_bf16 v[88:91], a[220:223], v[180:183], v[88:91]\n"
"buffer_load_dwordx4 v255, s[36:39], s41 offen offset:1024 sc1 lds\n"
"v_mfma_f32_16x16x32_bf16 v[92:95], a[220:223], v[188:191], v[92:95]\n"
"v_rcp_f32 v208, v208\n"
"v_mfma_f32_16x16x32_bf16 v[92:95], a[216:219], v[184:187], v[92:95]\n"
"s_waitcnt lgkmcnt(6)\n"
"v_exp_f32_e64 v209, -v1\n"
"v_mfma_f32_16x16x32_bf16 v[16:19], a[224:227], a[0:3], v[16:19]\n"
"ds_read_b64_tr_b16 a[208:209], v234 offset:0\n"
"v_mul_f32 v0, v0, v208\n"
"v_mfma_f32_16x16x32_bf16 v[16:19], a[228:231], a[4:7], v[16:19]\n"
"ds_read_b64_tr_b16 a[210:211], v234 offset:512\n"
"buffer_load_dwordx4 v255, s[36:39], s41 offen offset:2048 sc1 lds\n"
"v_mfma_f32_16x16x32_bf16 v[20:23], a[228:231], a[12:15], v[20:23]\n"
"ds_read_b128 a[64:67], v255 offset:4096\n"
"v_mul_f32 v1, v1, v65\n"
"v_mfma_f32_16x16x32_bf16 v[20:23], a[224:227], a[8:11], v[20:23]\n"
"ds_read_b64_tr_b16 a[212:213], v234 offset:4096\n"
"v_add_f32 v209, 1.0, v209\n"
"v_mfma_f32_16x16x32_bf16 v[24:27], a[224:227], a[16:19], v[24:27]\n"
"ds_read_b64_tr_b16 a[214:215], v234 offset:4608\n"
"v_mfma_f32_16x16x32_bf16 v[24:27], a[228:231], a[20:23], v[24:27]\n"
"buffer_load_dwordx4 v255, s[36:39], s41 offen offset:3072 sc1 lds\n"
"v_mfma_f32_16x16x32_bf16 v[28:31], a[228:231], a[28:31], v[28:31]\n"
"s_waitcnt vmcnt(14)\n"
"ds_read_b64_tr_b16 a[216:217], v234 offset:8192\n"
"v_cmp_ne_u32 vcc, s73, v220\n"
"v_mfma_f32_16x16x32_bf16 v[28:31], a[224:227], a[24:27], v[28:31]\n"
"s_waitcnt lgkmcnt(7)\n"
"s_cbranch_vccnz .L_fo_retry\n"
".L_fo_ok:\n"
"s_add_u32 m0, s43, 45056\n"
"v_mfma_f32_16x16x32_bf16 v[80:83], a[232:235], a[0:3], v[80:83]\n"
"ds_read_b64_tr_b16 a[218:219], v234 offset:8704\n"
"v_mfma_f32_16x16x32_bf16 v[80:83], a[236:239], a[4:7], v[80:83]\n"
"ds_read_b64_tr_b16 a[220:221], v234 offset:12288\n"
"buffer_load_dwordx4 v236, s[36:39], s41 offen sc1 lds\n"
"v_mfma_f32_16x16x32_bf16 v[84:87], a[236:239], a[12:15], v[84:87]\n"
"s_add_u32 s41, s41, 81920\n"
"s_add_u32 s42, s40, 8192\n"
"ds_read_b128 a[68:71], v255 offset:14336\n"
"v_mfma_f32_16x16x32_bf16 v[84:87], a[232:235], a[8:11], v[84:87]\n"
"ds_read_b64_tr_b16 a[222:223], v234 offset:12800\n"
"v_mfma_f32_16x16x32_bf16 v[88:91], a[232:235], a[16:19], v[88:91]\n"
"v_rcp_f32 v209, v209\n"
"v_mfma_f32_16x16x32_bf16 v[88:91], a[236:239], a[20:23], v[88:91]\n"
"buffer_load_dwordx4 a[144:147], v240, s[36:39], s40 offen sc1\n"
"v_mul_f32 v1, v1, v209\n"
"v_mfma_f32_16x16x32_bf16 v[92:95], a[236:239], a[28:31], v[92:95]\n"
"ds_read_b128 a[72:75], v255 offset:5120\n"
"v_cvt_pk_bf16_f32 v64, v0, v1\n"
"v_mfma_f32_16x16x32_bf16 v[92:95], a[232:235], a[24:27], v[92:95]\n"
"s_waitcnt lgkmcnt(6)\n"
"v_exp_f32_e64 v210, -v2\n"
"v_mfma_f32_16x16x32_bf16 v[32:35], a[208:211], v[160:163], v[32:35]\n"
"ds_read_b64_tr_b16 a[224:225], v234 offset:16384\n"
"v_mul_f32 v2, v2, v66\n"
"v_mfma_f32_16x16x32_bf16 v[32:35], a[212:215], v[164:167], v[32:35]\n"
"ds_read_b64_tr_b16 a[226:227], v234 offset:16896\n"
"buffer_load_dwordx4 a[148:151], v241, s[36:39], s40 offen sc1\n"
"v_mfma_f32_16x16x32_bf16 v[36:39], a[212:215], v[172:175], v[36:39]\n"
"v_exp_f32_e64 v211, -v3\n"
"v_mfma_f32_16x16x32_bf16 v[36:39], a[208:211], v[168:171], v[36:39]\n"
"ds_read_b64_tr_b16 a[228:229], v234 offset:20480\n"
"v_add_f32 v210, 1.0, v210\n"
"v_mfma_f32_16x16x32_bf16 v[40:43], a[208:211], v[176:179], v[40:43]\n"
"ds_read_b64_tr_b16 a[230:231], v234 offset:20992\n"
"v_mul_f32 v3, v3, v67\n"
"v_mfma_f32_16x16x32_bf16 v[40:43], a[212:215], v[180:183], v[40:43]\n"
"ds_read_b128 a[76:79], v255 offset:15360\n"
"buffer_load_dwordx4 a[152:155], v240, s[36:39], s42 offen sc1\n"
"v_mfma_f32_16x16x32_bf16 v[44:47], a[212:215], v[188:191], v[44:47]\n"
"ds_read_b64_tr_b16 a[232:233], v234 offset:24576\n"
"v_add_f32 v211, 1.0, v211\n"
"v_mfma_f32_16x16x32_bf16 v[44:47], a[208:211], v[184:187], v[44:47]\n"
"s_waitcnt lgkmcnt(7)\n"
"v_rcp_f32 v210, v210\n"
"v_mfma_f32_16x16x32_bf16 v[96:99], a[216:219], v[160:163], v[96:99]\n"
"ds_read_b64_tr_b16 a[234:235], v234 offset:25088\n"
"v_mul_f32 v2, v2, v210\n"
"v_mfma_f32_16x16x32_bf16 v[96:99], a[220:223], v[164:167], v[96:99]\n"
"ds_read_b64_tr_b16 a[236:237], v234 offset:28672\n"
"buffer_load_dwordx4 a[156:159], v241, s[36:39], s42 offen sc1\n"
"v_mfma_f32_16x16x32_bf16 v[100:103], a[220:223], v[172:175], v[100:103]\n"
"s_add_u32 s40, s40, 16384\n"
"s_add_u32 s42, s40, 8192\n"
"ds_read_b128 a[80:83], v255 offset:6144\n"
"v_mfma_f32_16x16x32_bf16 v[100:103], a[216:219], v[168:171], v[100:103]\n"
"ds_read_b64_tr_b16 a[238:239], v234 offset:29184\n"
"v_mfma_f32_16x16x32_bf16 v[104:107], a[216:219], v[176:179], v[104:107]\n"
"v_rcp_f32 v211, v211\n"
"v_mfma_f32_16x16x32_bf16 v[104:107], a[220:223], v[180:183], v[104:107]\n"
"buffer_load_dwordx4 a[160:163], v240, s[36:39], s40 offen sc1\n"
"v_mul_f32 v3, v3, v211\n"
"v_mfma_f32_16x16x32_bf16 v[108:111], a[220:223], v[188:191], v[108:111]\n"
"v_exp_f32_e64 v212, -v4\n"
"v_mfma_f32_16x16x32_bf16 v[108:111], a[216:219], v[184:187], v[108:111]\n"
"s_waitcnt lgkmcnt(6)\n"
"v_exp_f32_e64 v213, -v5\n"
"v_mfma_f32_16x16x32_bf16 v[32:35], a[224:227], a[0:3], v[32:35]\n"
"ds_read_b64_tr_b16 a[208:209], v235 offset:0\n"
"v_cvt_pk_bf16_f32 v65, v2, v3\n"
"v_mfma_f32_16x16x32_bf16 v[32:35], a[228:231], a[4:7], v[32:35]\n"
"ds_read_b64_tr_b16 a[210:211], v235 offset:512\n"
"buffer_load_dwordx4 a[164:167], v241, s[36:39], s40 offen sc1\n"
"v_mfma_f32_16x16x32_bf16 v[36:39], a[228:231], a[12:15], v[36:39]\n"
"ds_read_b128 a[84:87], v255 offset:16384\n"
"v_mul_f32 v4, v4, v68\n"
"v_mfma_f32_16x16x32_bf16 v[36:39], a[224:227], a[8:11], v[36:39]\n"
"ds_read_b64_tr_b16 a[212:213], v235 offset:4096\n"
"v_add_f32 v212, 1.0, v212\n"
"v_mfma_f32_16x16x32_bf16 v[40:43], a[224:227], a[16:19], v[40:43]\n"
"ds_read_b64_tr_b16 a[214:215], v235 offset:4608\n"
"v_mul_f32 v5, v5, v69\n"
"v_mfma_f32_16x16x32_bf16 v[40:43], a[228:231], a[20:23], v[40:43]\n"
"buffer_load_dwordx4 a[168:171], v240, s[36:39], s42 offen sc1\n"
"v_add_f32 v213, 1.0, v213\n"
"v_mfma_f32_16x16x32_bf16 v[44:47], a[228:231], a[28:31], v[44:47]\n"
"ds_read_b64_tr_b16 a[216:217], v235 offset:8192\n"
"v_mfma_f32_16x16x32_bf16 v[44:47], a[224:227], a[24:27], v[44:47]\n"
"s_waitcnt lgkmcnt(6)\n"
"ds_read_b128 a[88:91], v255 offset:7168\n"
"v_mfma_f32_16x16x32_bf16 v[96:99], a[232:235], a[0:3], v[96:99]\n"
"ds_read_b64_tr_b16 a[218:219], v235 offset:8704\n"
"v_mfma_f32_16x16x32_bf16 v[96:99], a[236:239], a[4:7], v[96:99]\n"
"ds_read_b64_tr_b16 a[220:221], v235 offset:12288\n"
"buffer_load_dwordx4 a[172:175], v241, s[36:39], s42 offen sc1\n"
"v_mfma_f32_16x16x32_bf16 v[100:103], a[236:239], a[12:15], v[100:103]\n"
"s_add_u32 s40, s40, 16384\n"
"s_add_u32 s42, s40, 8192\n"
"v_mfma_f32_16x16x32_bf16 v[100:103], a[232:235], a[8:11], v[100:103]\n"
"ds_read_b64_tr_b16 a[222:223], v235 offset:12800\n"
"v_mfma_f32_16x16x32_bf16 v[104:107], a[232:235], a[16:19], v[104:107]\n"
"ds_read_b128 a[92:95], v255 offset:17408\n"
"v_mfma_f32_16x16x32_bf16 v[104:107], a[236:239], a[20:23], v[104:107]\n"
"buffer_load_dwordx4 a[176:179], v240, s[36:39], s40 offen sc1\n"
"v_mfma_f32_16x16x32_bf16 v[108:111], a[236:239], a[28:31], v[108:111]\n"
"v_rcp_f32 v212, v212\n"
"v_mfma_f32_16x16x32_bf16 v[108:111], a[232:235], a[24:27], v[108:111]\n"
"s_waitcnt lgkmcnt(6)\n"
"v_rcp_f32 v213, v213\n"
"v_mfma_f32_16x16x32_bf16 v[48:51], a[208:211], v[160:163], v[48:51]\n"
"ds_read_b64_tr_b16 a[224:225], v235 offset:16384\n"
"v_mul_f32 v4, v4, v212\n"
"v_mfma_f32_16x16x32_bf16 v[48:51], a[212:215], v[164:167], v[48:51]\n"
"ds_read_b64_tr_b16 a[226:227], v235 offset:16896\n"
"buffer_load_dwordx4 a[180:183], v241, s[36:39], s40 offen sc1\n"
"v_mfma_f32_16x16x32_bf16 v[52:55], a[212:215], v[172:175], v[52:55]\n"
"ds_read_b128 a[96:99], v255 offset:8192\n"
"v_mul_f32 v5, v5, v213\n"
"v_mfma_f32_16x16x32_bf16 v[52:55], a[208:211], v[168:171], v[52:55]\n"
"ds_read_b64_tr_b16 a[228:229], v235 offset:20480\n"
"v_cvt_pk_bf16_f32 v68, v4, v5\n"
"v_mfma_f32_16x16x32_bf16 v[56:59], a[208:211], v[176:179], v[56:59]\n"
"ds_read_b64_tr_b16 a[230:231], v235 offset:20992\n"
"v_mfma_f32_16x16x32_bf16 v[56:59], a[212:215], v[180:183], v[56:59]\n"
"buffer_load_dwordx4 a[184:187], v240, s[36:39], s42 offen sc1\n"
"v_mfma_f32_16x16x32_bf16 v[60:63], a[212:215], v[188:191], v[60:63]\n"
"ds_read_b64_tr_b16 a[232:233], v235 offset:24576\n"
"v_mfma_f32_16x16x32_bf16 v[60:63], a[208:211], v[184:187], v[60:63]\n"
"s_waitcnt lgkmcnt(7)\n"
"v_exp_f32_e64 v214, -v6\n"
"v_mfma_f32_16x16x32_bf16 v[112:115], a[216:219], v[160:163], v[112:115]\n"
"ds_read_b64_tr_b16 a[234:235], v235 offset:25088\n"
"v_mul_f32 v6, v6, v70\n"
"v_mfma_f32_16x16x32_bf16 v[112:115], a[220:223], v[164:167], v[112:115]\n"
"ds_read_b64_tr_b16 a[236:237], v235 offset:28672\n"
"buffer_load_dwordx4 a[188:191], v241, s[36:39], s42 offen sc1\n"
"v_mfma_f32_16x16x32_bf16 v[116:119], a[220:223], v[172:175], v[116:119]\n"
"s_add_u32 s40, s40, 16384\n"
"ds_read_b128 a[100:103], v255 offset:18432\n"
"v_add_f32 v214, 1.0, v214\n"
"v_mfma_f32_16x16x32_bf16 v[116:119], a[216:219], v[168:171], v[116:119]\n"
"ds_read_b64_tr_b16 a[238:239], v235 offset:29184\n"
"v_mfma_f32_16x16x32_bf16 v[120:123], a[216:219], v[176:179], v[120:123]\n"
"v_rcp_f32 v214, v214\n"
"v_mfma_f32_16x16x32_bf16 v[120:123], a[220:223], v[180:183], v[120:123]\n"
"v_exp_f32_e64 v215, -v7\n"
"v_mfma_f32_16x16x32_bf16 v[124:127], a[220:223], v[188:191], v[124:127]\n"
"ds_read_b128 a[104:107], v255 offset:9216\n"
"v_mul_f32 v6, v6, v214\n"
"v_mfma_f32_16x16x32_bf16 v[124:127], a[216:219], v[184:187], v[124:127]\n"
"s_waitcnt lgkmcnt(6)\n"
"v_exp_f32_e64 v216, -v8\n"
"v_mfma_f32_16x16x32_bf16 v[48:51], a[224:227], a[0:3], v[48:51]\n"
"v_exp_f32_e64 v217, -v9\n"
"v_mfma_f32_16x16x32_bf16 v[48:51], a[228:231], a[4:7], v[48:51]\n"
"v_exp_f32_e64 v219, -v10\n"
"v_mfma_f32_16x16x32_bf16 v[52:55], a[228:231], a[12:15], v[52:55]\n"
"v_exp_f32_e64 v222, -v11\n"
"v_mfma_f32_16x16x32_bf16 v[52:55], a[224:227], a[8:11], v[52:55]\n"
"v_exp_f32_e64 v223, -v12\n"
"v_mfma_f32_16x16x32_bf16 v[56:59], a[224:227], a[16:19], v[56:59]\n"
"ds_read_b128 a[108:111], v255 offset:19456\n"
"v_mul_f32 v7, v7, v71\n"
"v_mfma_f32_16x16x32_bf16 v[56:59], a[228:231], a[20:23], v[56:59]\n"
"v_exp_f32_e64 v224, -v13\n"
"v_mfma_f32_16x16x32_bf16 v[60:63], a[228:231], a[28:31], v[60:63]\n"
"v_exp_f32_e64 v208, -v14\n"
"v_mfma_f32_16x16x32_bf16 v[60:63], a[224:227], a[24:27], v[60:63]\n"
"s_waitcnt lgkmcnt(2)\n"
"v_add_f32 v215, 1.0, v215\n"
"v_mul_f32 v8, v8, v72\n"
"v_mfma_f32_16x16x32_bf16 v[112:115], a[232:235], a[0:3], v[112:115]\n"
"v_rcp_f32 v215, v215\n"
"v_mfma_f32_16x16x32_bf16 v[112:115], a[236:239], a[4:7], v[112:115]\n"
"v_mul_f32 v7, v7, v215\n"
"v_cvt_pk_bf16_f32 v69, v6, v7\n"
"v_mfma_f32_16x16x32_bf16 v[116:119], a[236:239], a[12:15], v[116:119]\n"
"v_exp_f32_e64 v209, -v15\n"
"v_mfma_f32_16x16x32_bf16 v[116:119], a[232:235], a[8:11], v[116:119]\n"
"v_add_f32 v216, 1.0, v216\n"
"v_mul_f32 v9, v9, v73\n"
"v_mfma_f32_16x16x32_bf16 v[120:123], a[232:235], a[16:19], v[120:123]\n"
"v_rcp_f32 v216, v216\n"
"v_mfma_f32_16x16x32_bf16 v[120:123], a[236:239], a[20:23], v[120:123]\n"
"v_mul_f32 v8, v8, v216\n"
"v_add_f32 v217, 1.0, v217\n"
"v_mfma_f32_16x16x32_bf16 v[124:127], a[236:239], a[28:31], v[124:127]\n"
"v_rcp_f32 v217, v217\n"
"v_mfma_f32_16x16x32_bf16 v[124:127], a[232:235], a[24:27], v[124:127]\n"
"v_exp_f32_e64 v210, -v16\n"
".L_e3:\n"
"s_waitcnt vmcnt(8) lgkmcnt(0)\n"
"v_mfma_f32_16x16x32_bf16 v[128:131], a[32:35], a[144:147], 0\n"
"s_nop 0\n"
"s_barrier\n"
"s_add_u32 s42, s40, 8192\n"
"v_mfma_f32_16x16x32_bf16 v[128:131], a[36:39], a[152:155], v[128:131]\n"
"ds_read_b128 a[112:115], v242 offset:0\n"
"buffer_load_dwordx4 a[192:195], v240, s[36:39], s40 offen sc1\n"
"v_mfma_f32_16x16x32_bf16 v[132:135], a[36:39], a[156:159], 0\n"
"ds_read_b128 a[116:119], v242 offset:10240\n"
"v_mul_f32 v9, v9, v217\n"
"v_mfma_f32_16x16x32_bf16 v[132:135], a[32:35], a[148:151], v[132:135]\n"
"v_exp_f32_e64 v211, -v17\n"
"v_mfma_f32_16x16x32_bf16 v[136:139], a[40:43], a[144:147], 0\n"
"ds_read_b128 a[120:123], v242 offset:1024\n"
"v_mul_f32 v10, v10, v74\n"
"v_mfma_f32_16x16x32_bf16 v[136:139], a[44:47], a[152:155], v[136:139]\n"
"ds_read_b128 a[124:127], v242 offset:11264\n"
"buffer_load_dwordx4 a[196:199], v241, s[36:39], s40 offen sc1\n"
"v_mfma_f32_16x16x32_bf16 v[140:143], a[44:47], a[156:159], 0\n"
"v_exp_f32_e64 v212, -v18\n"
"v_mfma_f32_16x16x32_bf16 v[140:143], a[40:43], a[148:151], v[140:143]\n"
"ds_read_b128 a[128:131], v242 offset:2048\n"
"v_add_f32 v219, 1.0, v219\n"
"v_mfma_f32_16x16x32_bf16 v[144:147], a[48:51], a[144:147], 0\n"
"v_rcp_f32 v219, v219\n"
"v_mfma_f32_16x16x32_bf16 v[144:147], a[52:55], a[152:155], v[144:147]\n"
"ds_read_b128 a[132:135], v242 offset:12288\n"
"buffer_load_dwordx4 a[200:203], v240, s[36:39], s42 offen sc1\n"
"v_mfma_f32_16x16x32_bf16 v[148:151], a[52:55], a[156:159], 0\n"
"ds_read_b128 a[136:139], v242 offset:3072\n"
"v_mul_f32 v10, v10, v219\n"
"v_mfma_f32_16x16x32_bf16 v[148:151], a[48:51], a[148:151], v[148:151]\n"
"v_exp_f32_e64 v213, -v19\n"
"v_mfma_f32_16x16x32_bf16 v[152:155], a[56:59], a[144:147], 0\n"
"ds_read_b128 a[140:143], v242 offset:13312\n"
"v_mul_f32 v11, v11, v75\n"
"v_mfma_f32_16x16x32_bf16 v[152:155], a[60:63], a[152:155], v[152:155]\n"
"buffer_load_dwordx4 a[204:207], v241, s[36:39], s42 offen sc1\n"
"v_add_f32 v222, 1.0, v222\n"
"v_mfma_f32_16x16x32_bf16 v[156:159], a[60:63], a[156:159], 0\n"
"s_add_u32 s40, s40, 16384\n"
"ds_read_b128 a[0:3], v242 offset:4096\n"
"v_cvt_pk_bf16_f32 v72, v8, v9\n"
"v_mfma_f32_16x16x32_bf16 v[156:159], a[56:59], a[148:151], v[156:159]\n"
"s_add_u32 s42, s40, 8192\n"
"ds_read_b128 a[4:7], v242 offset:14336\n"
"v_mul_f32 v12, v12, v76\n"
"v_mfma_f32_16x16x32_bf16 v[160:163], a[64:67], a[144:147], 0\n"
"v_rcp_f32 v222, v222\n"
"v_mfma_f32_16x16x32_bf16 v[160:163], a[68:71], a[152:155], v[160:163]\n"
"ds_read_b128 a[8:11], v242 offset:5120\n"
"buffer_load_dwordx4 a[240:243], v240, s[36:39], s40 offen sc1\n"
"v_mfma_f32_16x16x32_bf16 v[164:167], a[68:71], a[156:159], 0\n"
"v_mul_f32 v11, v11, v222\n"
"v_cvt_pk_bf16_f32 v73, v10, v11\n"
"v_mfma_f32_16x16x32_bf16 v[164:167], a[64:67], a[148:151], v[164:167]\n"
"ds_read_b128 a[12:15], v242 offset:15360\n"
"v_add_f32 v223, 1.0, v223\n"
"v_mfma_f32_16x16x32_bf16 v[168:171], a[72:75], a[144:147], 0\n"
"ds_read_b128 a[16:19], v242 offset:6144\n"
"v_mul_f32 v13, v13, v77\n"
"v_mfma_f32_16x16x32_bf16 v[168:171], a[76:79], a[152:155], v[168:171]\n"
"buffer_load_dwordx4 a[244:247], v241, s[36:39], s40 offen sc1\n"
"v_add_f32 v224, 1.0, v224\n"
"v_mfma_f32_16x16x32_bf16 v[172:175], a[76:79], a[156:159], 0\n"
"ds_read_b128 a[20:23], v242 offset:16384\n"
"v_mul_f32 v14, v14, v78\n"
"v_mfma_f32_16x16x32_bf16 v[172:175], a[72:75], a[148:151], v[172:175]\n"
"v_rcp_f32 v223, v223\n"
"v_mfma_f32_16x16x32_bf16 v[176:179], a[80:83], a[144:147], 0\n"
"ds_read_b128 a[24:27], v242 offset:7168\n"
"v_mul_f32 v12, v12, v223\n"
"v_mfma_f32_16x16x32_bf16 v[176:179], a[84:87], a[152:155], v[176:179]\n"
"ds_read_b128 a[28:31], v242 offset:17408\n"
"buffer_load_dwordx4 a[248:251], v240, s[36:39], s42 offen sc1\n"
"v_mfma_f32_16x16x32_bf16 v[180:183], a[84:87], a[156:159], 0\n"
"v_rcp_f32 v224, v224\n"
"v_mfma_f32_16x16x32_bf16 v[180:183], a[80:83], a[148:151], v[180:183]\n"
"ds_read_b128 a[208:211], v242 offset:8192\n"
"v_mul_f32 v13, v13, v224\n"
"v_mfma_f32_16x16x32_bf16 v[184:187], a[88:91], a[144:147], 0\n"
"v_exp_f32_e64 v214, -v20\n"
"v_mfma_f32_16x16x32_bf16 v[184:187], a[92:95], a[152:155], v[184:187]\n"
"ds_read_b128 a[212:215], v242 offset:18432\n"
"buffer_load_dwordx4 a[252:255], v241, s[36:39], s42 offen sc1\n"
"v_mfma_f32_16x16x32_bf16 v[188:191], a[92:95], a[156:159], 0\n"
"s_add_u32 s40, s40, 16384\n"
"ds_read_b128 a[216:219], v242 offset:9216\n"
"v_add_f32 v208, 1.0, v208\n"
"v_mfma_f32_16x16x32_bf16 v[188:191], a[88:91], a[148:151], v[188:191]\n"
"s_add_u32 m0, s43, 122880\n"
"v_rcp_f32 v208, v208\n"
"v_mfma_f32_16x16x32_bf16 v[192:195], a[96:99], a[144:147], 0\n"
"ds_read_b128 a[220:223], v242 offset:19456\n"
"v_mul_f32 v14, v14, v208\n"
"v_mfma_f32_16x16x32_bf16 v[192:195], a[100:103], a[152:155], v[192:195]\n"
"buffer_load_dwordx4 v255, s[36:39], s41 offen offset:0 sc1 lds\n"
"v_mul_f32 v15, v15, v79\n"
"v_mfma_f32_16x16x32_bf16 v[196:199], a[100:103], a[156:159], 0\n"
"v_exp_f32_e64 v215, -v21\n"
"v_mfma_f32_16x16x32_bf16 v[196:199], a[96:99], a[148:151], v[196:199]\n"
"v_exp_f32_e64 v216, -v22\n"
"v_mfma_f32_16x16x32_bf16 v[200:203], a[104:107], a[144:147], 0\n"
"v_add_f32 v209, 1.0, v209\n"
"v_cvt_pk_bf16_f32 v76, v12, v13\n"
"v_mfma_f32_16x16x32_bf16 v[200:203], a[108:111], a[152:155], v[200:203]\n"
"buffer_load_dwordx4 v255, s[36:39], s41 offen offset:1024 sc1 lds\n"
"v_mul_f32 v16, v16, v80\n"
"v_mfma_f32_16x16x32_bf16 v[204:207], a[108:111], a[156:159], 0\n"
"v_rcp_f32 v209, v209\n"
"v_mfma_f32_16x16x32_bf16 v[204:207], a[104:107], a[148:151], v[204:207]\n"
"s_waitcnt vmcnt(14) lgkmcnt(0)\n"
"v_mul_f32 v15, v15, v209\n"
"v_cvt_pk_bf16_f32 v77, v14, v15\n"
"v_mfma_f32_16x16x32_bf16 v[128:131], a[112:115], a[160:163], v[128:131]\n"
"s_nop 0\n"
"s_barrier\n"
"v_add_f32 v210, 1.0, v210\n"
"v_mfma_f32_16x16x32_bf16 v[128:131], a[116:119], a[168:171], v[128:131]\n"
"ds_read_b128 a[32:35], v242 offset:20480\n"
"buffer_load_dwordx4 v255, s[36:39], s41 offen offset:2048 sc1 lds\n"
"v_mfma_f32_16x16x32_bf16 v[132:135], a[116:119], a[172:175], v[132:135]\n"
"ds_read_b128 a[36:39], v242 offset:30720\n"
"v_mul_f32 v17, v17, v81\n"
"v_mfma_f32_16x16x32_bf16 v[132:135], a[112:115], a[164:167], v[132:135]\n"
"v_rcp_f32 v210, v210\n"
"v_mfma_f32_16x16x32_bf16 v[136:139], a[120:123], a[160:163], v[136:139]\n"
"ds_read_b128 a[40:43], v242 offset:21504\n"
"v_mul_f32 v16, v16, v210\n"
"v_mfma_f32_16x16x32_bf16 v[136:139], a[124:127], a[168:171], v[136:139]\n"
"ds_read_b128 a[44:47], v242 offset:31744\n"
"buffer_load_dwordx4 v255, s[36:39], s41 offen offset:3072 sc1 lds\n"
"v_mfma_f32_16x16x32_bf16 v[140:143], a[124:127], a[172:175], v[140:143]\n"
"s_add_u32 m0, s43, 126976\n"
"v_exp_f32_e64 v217, -v23\n"
"v_mfma_f32_16x16x32_bf16 v[140:143], a[120:123], a[164:167], v[140:143]\n"
"ds_read_b128 a[48:51], v242 offset:22528\n"
"v_add_f32 v211, 1.0, v211\n"
"v_mfma_f32_16x16x32_bf16 v[144:147], a[128:131], a[160:163], v[144:147]\n"
"v_rcp_f32 v211, v211\n"
"v_mfma_f32_16x16x32_bf16 v[144:147], a[132:135], a[168:171], v[144:147]\n"
"ds_read_b128 a[52:55], v242 offset:32768\n"
"buffer_load_dwordx4 v236, s[36:39], s41 offen sc1 lds\n"
"v_mfma_f32_16x16x32_bf16 v[148:151], a[132:135], a[172:175], v[148:151]\n"
"s_add_u32 s41, s41, 81920\n"
"ds_read_b128 a[56:59], v242 offset:23552\n"
"v_mul_f32 v17, v17, v211\n"
"v_mfma_f32_16x16x32_bf16 v[148:151], a[128:131], a[164:167], v[148:151]\n"
"s_add_u32 s42, s40, 8192\n"
"v_exp_f32_e64 v219, -v24\n"
"v_mfma_f32_16x16x32_bf16 v[152:155], a[136:139], a[160:163], v[152:155]\n"
"ds_read_b128 a[60:63], v242 offset:33792\n"
"v_mul_f32 v18, v18, v82\n"
"v_mfma_f32_16x16x32_bf16 v[152:155], a[140:143], a[168:171], v[152:155]\n"
"buffer_load_dwordx4 a[144:147], v240, s[36:39], s40 offen sc1\n"
"v_add_f32 v212, 1.0, v212\n"
"v_mfma_f32_16x16x32_bf16 v[156:159], a[140:143], a[172:175], v[156:159]\n"
"ds_read_b128 a[64:67], v242 offset:24576\n"
"v_mul_f32 v19, v19, v83\n"
"v_mfma_f32_16x16x32_bf16 v[156:159], a[136:139], a[164:167], v[156:159]\n"
"ds_read_b128 a[68:71], v242 offset:34816\n"
"v_add_f32 v213, 1.0, v213\n"
"v_mfma_f32_16x16x32_bf16 v[160:163], a[0:3], a[160:163], v[160:163]\n"
"v_rcp_f32 v212, v212\n"
"v_mfma_f32_16x16x32_bf16 v[160:163], a[4:7], a[168:171], v[160:163]\n"
"ds_read_b128 a[72:75], v242 offset:25600\n"
"buffer_load_dwordx4 a[148:151], v241, s[36:39], s40 offen sc1\n"
"v_mfma_f32_16x16x32_bf16 v[164:167], a[4:7], a[172:175], v[164:167]\n"
"v_rcp_f32 v213, v213\n"
"v_mfma_f32_16x16x32_bf16 v[164:167], a[0:3], a[164:167], v[164:167]\n"
"ds_read_b128 a[76:79], v242 offset:35840\n"
"v_mul_f32 v18, v18, v212\n"
"v_mfma_f32_16x16x32_bf16 v[168:171], a[8:11], a[160:163], v[168:171]\n"
"ds_read_b128 a[80:83], v242 offset:26624\n"
"v_mul_f32 v19, v19, v213\n"
"v_mfma_f32_16x16x32_bf16 v[168:171], a[12:15], a[168:171], v[168:171]\n"
"buffer_load_dwordx4 a[152:155], v240, s[36:39], s42 offen sc1\n"
"v_cvt_pk_bf16_f32 v66, v16, v17\n"
"v_mfma_f32_16x16x32_bf16 v[172:175], a[12:15], a[172:175], v[172:175]\n"
"ds_read_b128 a[84:87], v242 offset:36864\n"
"v_cvt_pk_bf16_f32 v67, v18, v19\n"
"v_mfma_f32_16x16x32_bf16 v[172:175], a[8:11], a[164:167], v[172:175]\n"
"v_exp_f32_e64 v222, -v25\n"
"v_mfma_f32_16x16x32_bf16 v[176:179], a[16:19], a[160:163], v[176:179]\n"
"ds_read_b128 a[88:91], v242 offset:27648\n"
"buffer_store_dwordx4 v[64:67], v255, s[36:39], s85 offen offset:0 sc1\n"
"v_mfma_f32_16x16x32_bf16 v[176:179], a[20:23], a[168:171], v[176:179]\n"
"ds_read_b128 a[92:95], v242 offset:37888\n"
".L_pad12:\n"
"s_cbranch_execz .L_pad16\n"
"s_nop 0\n"
"buffer_load_dwordx4 a[156:159], v241, s[36:39], s42 offen sc1\n"
"v_mfma_f32_16x16x32_bf16 v[180:183], a[20:23], a[172:175], v[180:183]\n"
"s_add_u32 s40, s40, 16384\n"
"v_exp_f32_e64 v223, -v26\n"
"v_mfma_f32_16x16x32_bf16 v[180:183], a[16:19], a[164:167], v[180:183]\n"
"s_add_u32 m0, s43, 0\n"
"ds_read_b128 a[96:99], v242 offset:28672\n"
"v_mul_f32 v20, v20, v84\n"
"v_mfma_f32_16x16x32_bf16 v[184:187], a[24:27], a[160:163], v[184:187]\n"
"v_exp_f32_e64 v224, -v27\n"
"v_mfma_f32_16x16x32_bf16 v[184:187], a[28:31], a[168:171], v[184:187]\n"
"ds_read_b128 a[100:103], v242 offset:38912\n"
"buffer_load_dwordx4 v255, s[36:39], s41 offen offset:0 sc1 lds\n"
"v_mfma_f32_16x16x32_bf16 v[188:191], a[28:31], a[172:175], v[188:191]\n"
"ds_read_b128 a[104:107], v242 offset:29696\n"
"v_add_f32 v214, 1.0, v214\n"
"v_mfma_f32_16x16x32_bf16 v[188:191], a[24:27], a[164:167], v[188:191]\n"
"v_rcp_f32 v214, v214\n"
"v_mfma_f32_16x16x32_bf16 v[192:195], a[208:211], a[160:163], v[192:195]\n"
"ds_read_b128 a[108:111], v242 offset:39936\n"
"v_mul_f32 v20, v20, v214\n"
"v_mfma_f32_16x16x32_bf16 v[192:195], a[212:215], a[168:171], v[192:195]\n"
"buffer_load_dwordx4 v255, s[36:39], s41 offen offset:1024 sc1 lds\n"
"v_mul_f32 v21, v21, v85\n"
"v_mfma_f32_16x16x32_bf16 v[196:199], a[212:215], a[172:175], v[196:199]\n"
"v_exp_f32_e64 v208, -v28\n"
"v_mfma_f32_16x16x32_bf16 v[196:199], a[208:211], a[164:167], v[196:199]\n"
"v_add_f32 v215, 1.0, v215\n"
"v_mul_f32 v22, v22, v86\n"
"v_mfma_f32_16x16x32_bf16 v[200:203], a[216:219], a[160:163], v[200:203]\n"
"v_rcp_f32 v215, v215\n"
"v_mfma_f32_16x16x32_bf16 v[200:203], a[220:223], a[168:171], v[200:203]\n"
"buffer_load_dwordx4 v255, s[36:39], s41 offen offset:2048 sc1 lds\n"
"v_mul_f32 v21, v21, v215\n"
"v_mfma_f32_16x16x32_bf16 v[204:207], a[220:223], a[172:175], v[204:207]\n"
"v_exp_f32_e64 v209, -v29\n"
"v_mfma_f32_16x16x32_bf16 v[204:207], a[216:219], a[164:167], v[204:207]\n"
"s_waitcnt vmcnt(21) lgkmcnt(0)\n"
"v_add_f32 v216, 1.0, v216\n"
"v_mul_f32 v23, v23, v87\n"
"v_mfma_f32_16x16x32_bf16 v[128:131], a[32:35], a[176:179], v[128:131]\n"
"s_nop 0\n"
"s_barrier\n"
"v_add_f32 v217, 1.0, v217\n"
"v_mfma_f32_16x16x32_bf16 v[128:131], a[36:39], a[184:187], v[128:131]\n"
"ds_read_b128 a[112:115], v242 offset:40960\n"
"buffer_load_dwordx4 v255, s[36:39], s41 offen offset:3072 sc1 lds\n"
"v_mfma_f32_16x16x32_bf16 v[132:135], a[36:39], a[188:191], v[132:135]\n"
"s_add_u32 m0, s43, 4096\n"
"ds_read_b128 a[116:119], v242 offset:51200\n"
"v_cvt_pk_bf16_f32 v70, v20, v21\n"
"v_mfma_f32_16x16x32_bf16 v[132:135], a[32:35], a[180:183], v[132:135]\n"
"v_rcp_f32 v216, v216\n"
"v_mfma_f32_16x16x32_bf16 v[136:139], a[40:43], a[176:179], v[136:139]\n"
"ds_read_b128 a[120:123], v242 offset:41984\n"
"v_mul_f32 v22, v22, v216\n"
"v_mfma_f32_16x16x32_bf16 v[136:139], a[44:47], a[184:187], v[136:139]\n"
"ds_read_b128 a[124:127], v242 offset:52224\n"
"buffer_load_dwordx4 v236, s[36:39], s41 offen sc1 lds\n"
"v_mfma_f32_16x16x32_bf16 v[140:143], a[44:47], a[188:191], v[140:143]\n"
"s_add_u32 s41, s41, 81920\n"
"v_rcp_f32 v217, v217\n"
"v_mfma_f32_16x16x32_bf16 v[140:143], a[40:43], a[180:183], v[140:143]\n"
"s_add_u32 s42, s40, 8192\n"
"ds_read_b128 a[128:131], v242 offset:43008\n"
"v_mul_f32 v23, v23, v217\n"
"v_mfma_f32_16x16x32_bf16 v[144:147], a[48:51], a[176:179], v[144:147]\n"
"v_exp_f32_e64 v210, -v30\n"
"v_mfma_f32_16x16x32_bf16 v[144:147], a[52:55], a[184:187], v[144:147]\n"
"ds_read_b128 a[132:135], v242 offset:53248\n"
"buffer_load_dwordx4 a[160:163], v240, s[36:39], s40 offen sc1\n"
"v_mfma_f32_16x16x32_bf16 v[148:151], a[52:55], a[188:191], v[148:151]\n"
"ds_read_b128 a[136:139], v242 offset:44032\n"
"v_cvt_pk_bf16_f32 v71, v22, v23\n"
"v_mfma_f32_16x16x32_bf16 v[148:151], a[48:51], a[180:183], v[148:151]\n"
"v_exp_f32_e64 v211, -v31\n"
"v_mfma_f32_16x16x32_bf16 v[152:155], a[56:59], a[176:179], v[152:155]\n"
"ds_read_b128 a[140:143], v242 offset:54272\n"
"buffer_store_dwordx4 v[68:71], v255, s[36:39], s85 offen offset:1024 sc1\n"
"v_mfma_f32_16x16x32_bf16 v[152:155], a[60:63], a[184:187], v[152:155]\n"
"buffer_load_dwordx4 a[164:167], v241, s[36:39], s40 offen sc1\n"
"v_mul_f32 v24, v24, v88\n"
"v_mfma_f32_16x16x32_bf16 v[156:159], a[60:63], a[188:191], v[156:159]\n"
"ds_read_b128 a[0:3], v242 offset:45056\n"
"v_add_f32 v219, 1.0, v219\n"
"v_mfma_f32_16x16x32_bf16 v[156:159], a[56:59], a[180:183], v[156:159]\n"
"ds_read_b128 a[4:7], v242 offset:55296\n"
"v_mul_f32 v25, v25, v89\n"
"v_mfma_f32_16x16x32_bf16 v[160:163], a[64:67], a[176:179], v[160:163]\n"
"v_rcp_f32 v219, v219\n"
"v_mfma_f32_16x16x32_bf16 v[160:163], a[68:71], a[184:187], v[160:163]\n"
"ds_read_b128 a[8:11], v242 offset:46080\n"
"buffer_load_dwordx4 a[168:171], v240, s[36:39], s42 offen sc1\n"
"v_mfma_f32_16x16x32_bf16 v[164:167], a[68:71], a[188:191], v[164:167]\n"
"v_mul_f32 v24, v24, v219\n"
"v_add_f32 v222, 1.0, v222\n"
"v_mfma_f32_16x16x32_bf16 v[164:167], a[64:67], a[180:183], v[164:167]\n"
"ds_read_b128 a[12:15], v242 offset:56320\n"
"v_mul_f32 v26, v26, v90\n"
"v_mfma_f32_16x16x32_bf16 v[168:171], a[72:75], a[176:179], v[168:171]\n"
"ds_read_b128 a[16:19], v242 offset:47104\n"
"v_add_f32 v223, 1.0, v223\n"
"v_mfma_f32_16x16x32_bf16 v[168:171], a[76:79], a[184:187], v[168:171]\n"
"buffer_load_dwordx4 a[172:175], v241, s[36:39], s42 offen sc1\n"
"v_mul_f32 v27, v27, v91\n"
"v_mfma_f32_16x16x32_bf16 v[172:175], a[76:79], a[188:191], v[172:175]\n"
"s_add_u32 s40, s40, 16384\n"
"ds_read_b128 a[20:23], v242 offset:57344\n"
"v_add_f32 v224, 1.0, v224\n"
"v_mfma_f32_16x16x32_bf16 v[172:175], a[72:75], a[180:183], v[172:175]\n"
"s_add_u32 m0, s43, 61440\n"
"v_rcp_f32 v222, v222\n"
"v_mfma_f32_16x16x32_bf16 v[176:179], a[80:83], a[176:179], v[176:179]\n"
"ds_read_b128 a[24:27], v242 offset:48128\n"
"v_mul_f32 v25, v25, v222\n"
"v_mfma_f32_16x16x32_bf16 v[176:179], a[84:87], a[184:187], v[176:179]\n"
"ds_read_b128 a[28:31], v242 offset:58368\n"
"buffer_load_dwordx4 v255, s[36:39], s41 offen offset:0 sc1 lds\n"
"v_mfma_f32_16x16x32_bf16 v[180:183], a[84:87], a[188:191], v[180:183]\n"
"v_rcp_f32 v223, v223\n"
"v_mfma_f32_16x16x32_bf16 v[180:183], a[80:83], a[180:183], v[180:183]\n"
"ds_read_b128 a[208:211], v242 offset:49152\n"
"v_mul_f32 v26, v26, v223\n"
"v_mfma_f32_16x16x32_bf16 v[184:187], a[88:91], a[176:179], v[184:187]\n"
"v_rcp_f32 v224, v224\n"
"v_mfma_f32_16x16x32_bf16 v[184:187], a[92:95], a[184:187], v[184:187]\n"
"ds_read_b128 a[212:215], v242 offset:59392\n"
"buffer_load_dwordx4 v255, s[36:39], s41 offen offset:1024 sc1 lds\n"
"v_mfma_f32_16x16x32_bf16 v[188:191], a[92:95], a[188:191], v[188:191]\n"
"ds_read_b128 a[216:219], v242 offset:50176\n"
"v_mul_f32 v27, v27, v224\n"
"v_mfma_f32_16x16x32_bf16 v[188:191], a[88:91], a[180:183], v[188:191]\n"
"v_exp_f32_e64 v212, -v32\n"
"v_mfma_f32_16x16x32_bf16 v[192:195], a[96:99], a[176:179], v[192:195]\n"
"ds_read_b128 a[220:223], v242 offset:60416\n"
"v_cvt_pk_bf16_f32 v74, v24, v25\n"
"v_mfma_f32_16x16x32_bf16 v[192:195], a[100:103], a[184:187], v[192:195]\n"
"buffer_load_dwordx4 v255, s[36:39], s41 offen offset:2048 sc1 lds\n"
"v_cvt_pk_bf16_f32 v75, v26, v27\n"
"v_mfma_f32_16x16x32_bf16 v[196:199], a[100:103], a[188:191], v[196:199]\n"
"v_exp_f32_e64 v213, -v33\n"
"v_mfma_f32_16x16x32_bf16 v[196:199], a[96:99], a[180:183], v[196:199]\n"
"v_exp_f32_e64 v214, -v34\n"
"v_mfma_f32_16x16x32_bf16 v[200:203], a[104:107], a[176:179], v[200:203]\n"
"buffer_store_dwordx4 v[72:75], v255, s[36:39], s85 offen offset:2048 sc1\n"
"v_mul_f32 v28, v28, v92\n"
"v_mfma_f32_16x16x32_bf16 v[200:203], a[108:111], a[184:187], v[200:203]\n"
"buffer_load_dwordx4 v255, s[36:39], s41 offen offset:3072 sc1 lds\n"
"v_add_f32 v208, 1.0, v208\n"
"v_mfma_f32_16x16x32_bf16 v[204:207], a[108:111], a[188:191], v[204:207]\n"
"s_add_u32 m0, s43, 65536\n"
"v_rcp_f32 v208, v208\n"
"v_mfma_f32_16x16x32_bf16 v[204:207], a[104:107], a[180:183], v[204:207]\n"
"s_waitcnt vmcnt(29) lgkmcnt(0)\n"
"v_exp_f32_e64 v215, -v35\n"
"v_mfma_f32_16x16x32_bf16 v[128:131], a[112:115], a[192:195], v[128:131]\n"
"s_nop 0\n"
"s_barrier\n"
"v_mul_f32 v28, v28, v208\n"
"v_mfma_f32_16x16x32_bf16 v[128:131], a[116:119], a[200:203], v[128:131]\n"
"ds_read_b128 a[32:35], v255 offset:20480\n"
"buffer_load_dwordx4 v236, s[36:39], s41 offen sc1 lds\n"
"v_mfma_f32_16x16x32_bf16 v[132:135], a[116:119], a[204:207], v[132:135]\n"
"s_add_u32 s41, s41, 81920\n"
"ds_read_b128 a[36:39], v255 offset:30720\n"
"v_mul_f32 v29, v29, v93\n"
"v_mfma_f32_16x16x32_bf16 v[132:135], a[112:115], a[196:199], v[132:135]\n"
"s_add_u32 s42, s40, 8192\n"
"v_exp_f32_e64 v216, -v36\n"
"v_mfma_f32_16x16x32_bf16 v[136:139], a[120:123], a[192:195], v[136:139]\n"
"ds_read_b128 a[40:43], v255 offset:21504\n"
"v_add_f32 v209, 1.0, v209\n"
"v_mfma_f32_16x16x32_bf16 v[136:139], a[124:127], a[200:203], v[136:139]\n"
"ds_read_b128 a[44:47], v255 offset:31744\n"
"buffer_load_dwordx4 a[176:179], v240, s[36:39], s40 offen sc1\n"
"v_mfma_f32_16x16x32_bf16 v[140:143], a[124:127], a[204:207], v[140:143]\n"
"v_rcp_f32 v209, v209\n"
"v_mfma_f32_16x16x32_bf16 v[140:143], a[120:123], a[196:199], v[140:143]\n"
"ds_read_b128 a[48:51], v255 offset:22528\n"
"v_mul_f32 v29, v29, v209\n"
"v_mfma_f32_16x16x32_bf16 v[144:147], a[128:131], a[192:195], v[144:147]\n"
"v_mul_f32 v30, v30, v94\n"
"v_add_f32 v210, 1.0, v210\n"
"v_mfma_f32_16x16x32_bf16 v[144:147], a[132:135], a[200:203], v[144:147]\n"
"ds_read_b128 a[52:55], v255 offset:32768\n"
"buffer_load_dwordx4 a[180:183], v241, s[36:39], s40 offen sc1\n"
"v_mfma_f32_16x16x32_bf16 v[148:151], a[132:135], a[204:207], v[148:151]\n"
"ds_read_b128 a[56:59], v255 offset:23552\n"
"v_mul_f32 v31, v31, v95\n"
"v_mfma_f32_16x16x32_bf16 v[148:151], a[128:131], a[196:199], v[148:151]\n"
"v_rcp_f32 v210, v210\n"
"v_mfma_f32_16x16x32_bf16 v[152:155], a[136:139], a[192:195], v[152:155]\n"
"ds_read_b128 a[60:63], v255 offset:33792\n"
"v_mul_f32 v30, v30, v210\n"
"v_mfma_f32_16x16x32_bf16 v[152:155], a[140:143], a[200:203], v[152:155]\n"
"buffer_load_dwordx4 a[184:187], v240, s[36:39], s42 offen sc1\n"
"v_add_f32 v211, 1.0, v211\n"
"v_mfma_f32_16x16x32_bf16 v[156:159], a[140:143], a[204:207], v[156:159]\n"
"ds_read_b128 a[64:67], v255 offset:24576\n"
"v_cvt_pk_bf16_f32 v78, v28, v29\n"
"v_mfma_f32_16x16x32_bf16 v[156:159], a[136:139], a[196:199], v[156:159]\n"
"ds_read_b128 a[68:71], v255 offset:34816\n"
"v_mul_f32 v32, v32, v96\n"
"v_mfma_f32_16x16x32_bf16 v[160:163], a[0:3], a[192:195], v[160:163]\n"
"v_rcp_f32 v211, v211\n"
"v_mfma_f32_16x16x32_bf16 v[160:163], a[4:7], a[200:203], v[160:163]\n"
"ds_read_b128 a[72:75], v255 offset:25600\n"
"buffer_load_dwordx4 a[188:191], v241, s[36:39], s42 offen sc1\n"
"v_mfma_f32_16x16x32_bf16 v[164:167], a[4:7], a[204:207], v[164:167]\n"
"s_add_u32 s40, s40, 16384\n"
"v_exp_f32_e64 v217, -v37\n"
"v_mfma_f32_16x16x32_bf16 v[164:167], a[0:3], a[196:199], v[164:167]\n"
"s_add_u32 m0, s43, 81920\n"
"ds_read_b128 a[76:79], v255 offset:35840\n"
"v_mul_f32 v31, v31, v211\n"
"v_mfma_f32_16x16x32_bf16 v[168:171], a[8:11], a[192:195], v[168:171]\n"
"ds_read_b128 a[80:83], v255 offset:26624\n"
"v_cvt_pk_bf16_f32 v79, v30, v31\n"
"v_mfma_f32_16x16x32_bf16 v[168:171], a[12:15], a[200:203], v[168:171]\n"
"buffer_load_dwordx4 v255, s[36:39], s41 offen offset:0 sc1 lds\n"
"buffer_store_dwordx4 v[76:79], v255, s[36:39], s85 offen offset:3072 sc1\n"
"v_mfma_f32_16x16x32_bf16 v[172:175], a[12:15], a[204:207], v[172:175]\n"
"s_add_u32 s85, s85, 8192\n"
"ds_read_b128 a[84:87], v255 offset:36864\n"
"v_add_f32 v212, 1.0, v212\n"
"v_mfma_f32_16x16x32_bf16 v[172:175], a[8:11], a[196:199], v[172:175]\n"
"v_rcp_f32 v212, v212\n"
"v_mfma_f32_16x16x32_bf16 v[176:179], a[16:19], a[192:195], v[176:179]\n"
"ds_read_b128 a[88:91], v255 offset:27648\n"
"v_mul_f32 v32, v32, v212\n"
"v_mfma_f32_16x16x32_bf16 v[176:179], a[20:23], a[200:203], v[176:179]\n"
"ds_read_b128 a[92:95], v255 offset:37888\n"
"buffer_load_dwordx4 v255, s[36:39], s41 offen offset:1024 sc1 lds\n"
"v_mfma_f32_16x16x32_bf16 v[180:183], a[20:23], a[204:207], v[180:183]\n"
"v_exp_f32_e64 v219, -v38\n"
"v_mfma_f32_16x16x32_bf16 v[180:183], a[16:19], a[196:199], v[180:183]\n"
"ds_read_b128 a[96:99], v255 offset:28672\n"
"v_mul_f32 v33, v33, v97\n"
"v_mfma_f32_16x16x32_bf16 v[184:187], a[24:27], a[192:195], v[184:187]\n"
"v_exp_f32_e64 v222, -v39\n"
"v_mfma_f32_16x16x32_bf16 v[184:187], a[28:31], a[200:203], v[184:187]\n"
"ds_read_b128 a[100:103], v255 offset:38912\n"
"buffer_load_dwordx4 v255, s[36:39], s41 offen offset:2048 sc1 lds\n"
"v_mfma_f32_16x16x32_bf16 v[188:191], a[28:31], a[204:207], v[188:191]\n"
"ds_read_b128 a[104:107], v255 offset:29696\n"
"v_add_f32 v213, 1.0, v213\n"
"v_mfma_f32_16x16x32_bf16 v[188:191], a[24:27], a[196:199], v[188:191]\n"
"v_rcp_f32 v213, v213\n"
"v_mfma_f32_16x16x32_bf16 v[192:195], a[208:211], a[192:195], v[192:195]\n"
"ds_read_b128 a[108:111], v255 offset:39936\n"
"v_mul_f32 v33, v33, v213\n"
"v_mfma_f32_16x16x32_bf16 v[192:195], a[212:215], a[200:203], v[192:195]\n"
"buffer_load_dwordx4 v255, s[36:39], s41 offen offset:3072 sc1 lds\n"
"v_mul_f32 v34, v34, v98\n"
"v_mfma_f32_16x16x32_bf16 v[196:199], a[212:215], a[204:207], v[196:199]\n"
"s_add_u32 m0, s43, 86016\n"
"v_exp_f32_e64 v223, -v40\n"
"v_mfma_f32_16x16x32_bf16 v[196:199], a[208:211], a[196:199], v[196:199]\n"
"v_exp_f32_e64 v224, -v41\n"
"v_mfma_f32_16x16x32_bf16 v[200:203], a[216:219], a[192:195], v[200:203]\n"
"v_add_f32 v214, 1.0, v214\n"
"v_mul_f32 v35, v35, v99\n"
"v_mfma_f32_16x16x32_bf16 v[200:203], a[220:223], a[200:203], v[200:203]\n"
"buffer_load_dwordx4 v236, s[36:39], s41 offen sc1 lds\n"
"v_add_f32 v215, 1.0, v215\n"
"v_mfma_f32_16x16x32_bf16 v[204:207], a[220:223], a[204:207], v[204:207]\n"
"s_add_u32 s41, s41, 81920\n"
"v_rcp_f32 v214, v214\n"
"v_mfma_f32_16x16x32_bf16 v[204:207], a[216:219], a[196:199], v[204:207]\n"
"s_waitcnt vmcnt(36) lgkmcnt(0)\n"
"v_rcp_f32 v215, v215\n"
"v_mfma_f32_16x16x32_bf16 v[128:131], a[32:35], a[240:243], v[128:131]\n"
"s_nop 0\n"
"s_barrier\n"
"s_add_u32 s42, s40, 8192\n"
"v_mfma_f32_16x16x32_bf16 v[128:131], a[36:39], a[248:251], v[128:131]\n"
"ds_read_b128 a[112:115], v255 offset:40960\n"
"buffer_load_dwordx4 a[192:195], v240, s[36:39], s40 offen sc1\n"
"v_mfma_f32_16x16x32_bf16 v[132:135], a[36:39], a[252:255], v[132:135]\n"
"ds_read_b128 a[116:119], v255 offset:51200\n"
"v_mul_f32 v34, v34, v214\n"
"v_mfma_f32_16x16x32_bf16 v[132:135], a[32:35], a[244:247], v[132:135]\n"
"v_exp_f32_e64 v208, -v42\n"
"v_mfma_f32_16x16x32_bf16 v[136:139], a[40:43], a[240:243], v[136:139]\n"
"ds_read_b128 a[120:123], v255 offset:41984\n"
"v_mul_f32 v35, v35, v215\n"
"v_mfma_f32_16x16x32_bf16 v[136:139], a[44:47], a[248:251], v[136:139]\n"
"ds_read_b128 a[124:127], v255 offset:52224\n"
"buffer_load_dwordx4 a[196:199], v241, s[36:39], s40 offen sc1\n"
"v_mfma_f32_16x16x32_bf16 v[140:143], a[44:47], a[252:255], v[140:143]\n"
"v_exp_f32_e64 v209, -v43\n"
"v_mfma_f32_16x16x32_bf16 v[140:143], a[40:43], a[244:247], v[140:143]\n"
"ds_read_b128 a[128:131], v255 offset:43008\n"
"v_cvt_pk_bf16_f32 v96, v32, v33\n"
"v_mfma_f32_16x16x32_bf16 v[144:147], a[48:51], a[240:243], v[144:147]\n"
"v_cvt_pk_bf16_f32 v97, v34, v35\n"
"v_mul_f32 v36, v36, v100\n"
"v_mfma_f32_16x16x32_bf16 v[144:147], a[52:55], a[248:251], v[144:147]\n"
"ds_read_b128 a[132:135], v255 offset:53248\n"
"buffer_load_dwordx4 a[200:203], v240, s[36:39], s42 offen sc1\n"
"v_mfma_f32_16x16x32_bf16 v[148:151], a[52:55], a[252:255], v[148:151]\n"
"ds_read_b128 a[136:139], v255 offset:44032\n"
"v_add_f32 v216, 1.0, v216\n"
"v_mfma_f32_16x16x32_bf16 v[148:151], a[48:51], a[244:247], v[148:151]\n"
"v_rcp_f32 v216, v216\n"
"v_mfma_f32_16x16x32_bf16 v[152:155], a[56:59], a[240:243], v[152:155]\n"
"ds_read_b128 a[140:143], v255 offset:54272\n"
"v_mul_f32 v36, v36, v216\n"
"v_mfma_f32_16x16x32_bf16 v[152:155], a[60:63], a[248:251], v[152:155]\n"
"buffer_load_dwordx4 a[204:207], v241, s[36:39], s42 offen sc1\n"
"v_mul_f32 v37, v37, v101\n"
"v_mfma_f32_16x16x32_bf16 v[156:159], a[60:63], a[252:255], v[156:159]\n"
"s_add_u32 s40, s40, 16384\n"
"ds_read_b128 a[0:3], v255 offset:45056\n"
"v_add_f32 v217, 1.0, v217\n"
"v_mfma_f32_16x16x32_bf16 v[156:159], a[56:59], a[244:247], v[156:159]\n"
"s_add_u32 m0, s43, 102400\n"
"ds_read_b128 a[4:7], v255 offset:55296\n"
"v_mul_f32 v38, v38, v102\n"
"v_mfma_f32_16x16x32_bf16 v[160:163], a[64:67], a[240:243], v[160:163]\n"
"v_rcp_f32 v217, v217\n"
"v_mfma_f32_16x16x32_bf16 v[160:163], a[68:71], a[248:251], v[160:163]\n"
"ds_read_b128 a[8:11], v255 offset:46080\n"
"buffer_load_dwordx4 v255, s[36:39], s41 offen offset:0 sc1 lds\n"
"v_mfma_f32_16x16x32_bf16 v[164:167], a[68:71], a[252:255], v[164:167]\n"
"v_exp_f32_e64 v210, -v44\n"
"v_mfma_f32_16x16x32_bf16 v[164:167], a[64:67], a[244:247], v[164:167]\n"
"ds_read_b128 a[12:15], v255 offset:56320\n"
"v_mul_f32 v37, v37, v217\n"
"v_mfma_f32_16x16x32_bf16 v[168:171], a[72:75], a[240:243], v[168:171]\n"
"ds_read_b128 a[16:19], v255 offset:47104\n"
"v_add_f32 v219, 1.0, v219\n"
"v_mfma_f32_16x16x32_bf16 v[168:171], a[76:79], a[248:251], v[168:171]\n"
"buffer_load_dwordx4 v255, s[36:39], s41 offen offset:1024 sc1 lds\n"
"v_mul_f32 v39, v39, v103\n"
"v_mfma_f32_16x16x32_bf16 v[172:175], a[76:79], a[252:255], v[172:175]\n"
"ds_read_b128 a[20:23], v255 offset:57344\n"
"v_add_f32 v222, 1.0, v222\n"
"v_mfma_f32_16x16x32_bf16 v[172:175], a[72:75], a[244:247], v[172:175]\n"
"v_rcp_f32 v219, v219\n"
"v_mfma_f32_16x16x32_bf16 v[176:179], a[80:83], a[240:243], v[176:179]\n"
"ds_read_b128 a[24:27], v255 offset:48128\n"
"v_mul_f32 v38, v38, v219\n"
"v_mfma_f32_16x16x32_bf16 v[176:179], a[84:87], a[248:251], v[176:179]\n"
"ds_read_b128 a[28:31], v255 offset:58368\n"
"buffer_load_dwordx4 v255, s[36:39], s41 offen offset:2048 sc1 lds\n"
"v_mfma_f32_16x16x32_bf16 v[180:183], a[84:87], a[252:255], v[180:183]\n"
"v_rcp_f32 v222, v222\n"
"v_mfma_f32_16x16x32_bf16 v[180:183], a[80:83], a[244:247], v[180:183]\n"
"ds_read_b128 a[208:211], v255 offset:49152\n"
"v_mul_f32 v39, v39, v222\n"
"v_mfma_f32_16x16x32_bf16 v[184:187], a[88:91], a[240:243], v[184:187]\n"
"v_exp_f32_e64 v211, -v45\n"
"v_mfma_f32_16x16x32_bf16 v[184:187], a[92:95], a[248:251], v[184:187]\n"
"ds_read_b128 a[212:215], v255 offset:59392\n"
"buffer_load_dwordx4 v255, s[36:39], s41 offen offset:3072 sc1 lds\n"
"v_mfma_f32_16x16x32_bf16 v[188:191], a[92:95], a[252:255], v[188:191]\n"
"s_add_u32 m0, s43, 106496\n"
"ds_read_b128 a[216:219], v255 offset:50176\n"
"v_cvt_pk_bf16_f32 v100, v36, v37\n"
"v_mfma_f32_16x16x32_bf16 v[188:191], a[88:91], a[244:247], v[188:191]\n"
"v_exp_f32_e64 v212, -v46\n"
"v_mfma_f32_16x16x32_bf16 v[192:195], a[96:99], a[240:243], v[192:195]\n"
"ds_read_b128 a[220:223], v255 offset:60416\n"
"v_cvt_pk_bf16_f32 v101, v38, v39\n"
"v_mfma_f32_16x16x32_bf16 v[192:195], a[100:103], a[248:251], v[192:195]\n"
"buffer_load_dwordx4 v236, s[36:39], s41 offen sc1 lds\n"
"v_mul_f32 v40, v40, v104\n"
"v_mfma_f32_16x16x32_bf16 v[196:199], a[100:103], a[252:255], v[196:199]\n"
"s_add_u32 s41, s41, 81920\n"
"v_exp_f32_e64 v213, -v47\n"
"v_mfma_f32_16x16x32_bf16 v[196:199], a[96:99], a[244:247], v[196:199]\n"
"v_add_f32 v223, 1.0, v223\n"
"v_mul_f32 v41, v41, v105\n"
"v_mfma_f32_16x16x32_bf16 v[200:203], a[104:107], a[240:243], v[200:203]\n"
"v_rcp_f32 v223, v223\n"
"v_mfma_f32_16x16x32_bf16 v[200:203], a[108:111], a[248:251], v[200:203]\n"
"v_mul_f32 v40, v40, v223\n"
"v_add_f32 v224, 1.0, v224\n"
"v_mfma_f32_16x16x32_bf16 v[204:207], a[108:111], a[252:255], v[204:207]\n"
"v_rcp_f32 v224, v224\n"
"v_mfma_f32_16x16x32_bf16 v[204:207], a[104:107], a[244:247], v[204:207]\n"
"s_waitcnt vmcnt(35) lgkmcnt(0)\n"
"v_exp_f32_e64 v214, -v48\n"
"v_mfma_f32_16x16x32_bf16 v[128:131], a[112:115], a[144:147], v[128:131]\n"
"s_nop 0\n"
"s_barrier\n"
"s_add_u32 s42, s40, 8192\n"
"v_mfma_f32_16x16x32_bf16 v[128:131], a[116:119], a[152:155], v[128:131]\n"
"ds_read_b128 a[32:35], v243 offset:0\n"
"buffer_load_dwordx4 a[240:243], v240, s[36:39], s40 offen sc1\n"
"v_mfma_f32_16x16x32_bf16 v[132:135], a[116:119], a[156:159], v[132:135]\n"
"ds_read_b128 a[36:39], v243 offset:10240\n"
"v_mul_f32 v41, v41, v224\n"
"v_mfma_f32_16x16x32_bf16 v[132:135], a[112:115], a[148:151], v[132:135]\n"
"v_exp_f32_e64 v215, -v49\n"
"v_mfma_f32_16x16x32_bf16 v[136:139], a[120:123], a[144:147], v[136:139]\n"
"ds_read_b128 a[40:43], v243 offset:1024\n"
"v_mul_f32 v42, v42, v106\n"
"v_mfma_f32_16x16x32_bf16 v[136:139], a[124:127], a[152:155], v[136:139]\n"
"ds_read_b128 a[44:47], v243 offset:11264\n"
"buffer_load_dwordx4 a[244:247], v241, s[36:39], s40 offen sc1\n"
"v_mfma_f32_16x16x32_bf16 v[140:143], a[124:127], a[156:159], v[140:143]\n"
"v_add_f32 v208, 1.0, v208\n"
"v_mul_f32 v43, v43, v107\n"
"v_mfma_f32_16x16x32_bf16 v[140:143], a[120:123], a[148:151], v[140:143]\n"
"ds_read_b128 a[48:51], v243 offset:2048\n"
"v_add_f32 v209, 1.0, v209\n"
"v_mfma_f32_16x16x32_bf16 v[144:147], a[128:131], a[144:147], v[144:147]\n"
"v_rcp_f32 v208, v208\n"
"v_mfma_f32_16x16x32_bf16 v[144:147], a[132:135], a[152:155], v[144:147]\n"
"ds_read_b128 a[52:55], v243 offset:12288\n"
"buffer_load_dwordx4 a[248:251], v240, s[36:39], s42 offen sc1\n"
"v_mfma_f32_16x16x32_bf16 v[148:151], a[132:135], a[156:159], v[148:151]\n"
"ds_read_b128 a[56:59], v243 offset:3072\n"
"v_mul_f32 v42, v42, v208\n"
"v_mfma_f32_16x16x32_bf16 v[148:151], a[128:131], a[148:151], v[148:151]\n"
"v_rcp_f32 v209, v209\n"
"v_mfma_f32_16x16x32_bf16 v[152:155], a[136:139], a[144:147], v[152:155]\n"
"ds_read_b128 a[60:63], v243 offset:13312\n"
"v_mul_f32 v43, v43, v209\n"
"v_mfma_f32_16x16x32_bf16 v[152:155], a[140:143], a[152:155], v[152:155]\n"
"buffer_load_dwordx4 a[252:255], v241, s[36:39], s42 offen sc1\n"
"v_cvt_pk_bf16_f32 v104, v40, v41\n"
"v_mfma_f32_16x16x32_bf16 v[156:159], a[140:143], a[156:159], v[156:159]\n"
"s_add_u32 s40, s40, 16384\n"
"ds_read_b128 a[64:67], v243 offset:4096\n"
"v_cvt_pk_bf16_f32 v105, v42, v43\n"
"v_mfma_f32_16x16x32_bf16 v[156:159], a[136:139], a[148:151], v[156:159]\n"
"s_add_u32 m0, s43, 20480\n"
"ds_read_b128 a[68:71], v243 offset:14336\n"
"v_mul_f32 v44, v44, v108\n"
"v_mfma_f32_16x16x32_bf16 v[160:163], a[0:3], a[144:147], v[160:163]\n"
"v_exp_f32_e64 v216, -v50\n"
"v_mfma_f32_16x16x32_bf16 v[160:163], a[4:7], a[152:155], v[160:163]\n"
"ds_read_b128 a[72:75], v243 offset:5120\n"
"buffer_load_dwordx4 v255, s[36:39], s41 offen offset:0 sc1 lds\n"
"v_mfma_f32_16x16x32_bf16 v[164:167], a[4:7], a[156:159], v[164:167]\n"
"v_exp_f32_e64 v217, -v51\n"
"v_mfma_f32_16x16x32_bf16 v[164:167], a[0:3], a[148:151], v[164:167]\n"
"ds_read_b128 a[76:79], v243 offset:15360\n"
"v_add_f32 v210, 1.0, v210\n"
"v_mfma_f32_16x16x32_bf16 v[168:171], a[8:11], a[144:147], v[168:171]\n"
"ds_read_b128 a[80:83], v243 offset:6144\n"
"v_mul_f32 v45, v45, v109\n"
"v_mfma_f32_16x16x32_bf16 v[168:171], a[12:15], a[152:155], v[168:171]\n"
"buffer_load_dwordx4 v255, s[36:39], s41 offen offset:1024 sc1 lds\n"
"v_add_f32 v211, 1.0, v211\n"
"v_mfma_f32_16x16x32_bf16 v[172:175], a[12:15], a[156:159], v[172:175]\n"
"ds_read_b128 a[84:87], v243 offset:16384\n"
"v_mul_f32 v46, v46, v110\n"
"v_mfma_f32_16x16x32_bf16 v[172:175], a[8:11], a[148:151], v[172:175]\n"
"v_rcp_f32 v210, v210\n"
"v_mfma_f32_16x16x32_bf16 v[176:179], a[16:19], a[144:147], v[176:179]\n"
"ds_read_b128 a[88:91], v243 offset:7168\n"
"v_mul_f32 v44, v44, v210\n"
"v_mfma_f32_16x16x32_bf16 v[176:179], a[20:23], a[152:155], v[176:179]\n"
"ds_read_b128 a[92:95], v243 offset:17408\n"
"buffer_load_dwordx4 v255, s[36:39], s41 offen offset:2048 sc1 lds\n"
"v_mfma_f32_16x16x32_bf16 v[180:183], a[20:23], a[156:159], v[180:183]\n"
"v_rcp_f32 v211, v211\n"
"v_mfma_f32_16x16x32_bf16 v[180:183], a[16:19], a[148:151], v[180:183]\n"
"ds_read_b128 a[96:99], v243 offset:8192\n"
"v_mul_f32 v45, v45, v211\n"
"v_mfma_f32_16x16x32_bf16 v[184:187], a[24:27], a[144:147], v[184:187]\n"
"v_exp_f32_e64 v219, -v52\n"
"v_mfma_f32_16x16x32_bf16 v[184:187], a[28:31], a[152:155], v[184:187]\n"
"ds_read_b128 a[100:103], v243 offset:18432\n"
"buffer_load_dwordx4 v255, s[36:39], s41 offen offset:3072 sc1 lds\n"
"v_mfma_f32_16x16x32_bf16 v[188:191], a[28:31], a[156:159], v[188:191]\n"
"s_add_u32 m0, s43, 24576\n"
"ds_read_b128 a[104:107], v243 offset:9216\n"
"v_add_f32 v212, 1.0, v212\n"
"v_mfma_f32_16x16x32_bf16 v[188:191], a[24:27], a[148:151], v[188:191]\n"
"v_rcp_f32 v212, v212\n"
"v_mfma_f32_16x16x32_bf16 v[192:195], a[208:211], a[144:147], v[192:195]\n"
"ds_read_b128 a[108:111], v243 offset:19456\n"
"v_mul_f32 v46, v46, v212\n"
"v_mfma_f32_16x16x32_bf16 v[192:195], a[212:215], a[152:155], v[192:195]\n"
"buffer_load_dwordx4 v236, s[36:39], s41 offen sc1 lds\n"
"v_mul_f32 v47, v47, v111\n"
"v_mfma_f32_16x16x32_bf16 v[196:199], a[212:215], a[156:159], v[196:199]\n"
"s_add_u32 s41, s41, 81920\n"
"v_exp_f32_e64 v222, -v53\n"
"v_mfma_f32_16x16x32_bf16 v[196:199], a[208:211], a[148:151], v[196:199]\n"
"v_exp_f32_e64 v223, -v54\n"
"v_mfma_f32_16x16x32_bf16 v[200:203], a[216:219], a[144:147], v[200:203]\n"
"v_add_f32 v213, 1.0, v213\n"
"v_cvt_pk_bf16_f32 v108, v44, v45\n"
"v_mfma_f32_16x16x32_bf16 v[200:203], a[220:223], a[152:155], v[200:203]\n"
"v_rcp_f32 v213, v213\n"
"v_mfma_f32_16x16x32_bf16 v[204:207], a[220:223], a[156:159], v[204:207]\n"
"v_mul_f32 v47, v47, v213\n"
"v_cvt_pk_bf16_f32 v109, v46, v47\n"
"v_mfma_f32_16x16x32_bf16 v[204:207], a[216:219], a[148:151], v[204:207]\n"
"s_waitcnt vmcnt(34) lgkmcnt(0)\n"
"v_exp_f32_e64 v224, -v55\n"
"v_mfma_f32_16x16x32_bf16 v[128:131], a[32:35], a[160:163], v[128:131]\n"
"s_nop 0\n"
"s_barrier\n"
"s_add_u32 s42, s40, 8192\n"
"v_mfma_f32_16x16x32_bf16 v[128:131], a[36:39], a[168:171], v[128:131]\n"
"ds_read_b128 a[112:115], v255 offset:0\n"
"buffer_load_dwordx4 a[144:147], v240, s[36:39], s40 offen sc1\n"
"v_mfma_f32_16x16x32_bf16 v[132:135], a[36:39], a[172:175], v[132:135]\n"
"ds_read_b128 a[116:119], v255 offset:10240\n"
"v_mul_f32 v48, v48, v112\n"
"v_mfma_f32_16x16x32_bf16 v[132:135], a[32:35], a[164:167], v[132:135]\n"
"v_add_f32 v214, 1.0, v214\n"
"v_mul_f32 v49, v49, v113\n"
"v_mfma_f32_16x16x32_bf16 v[136:139], a[40:43], a[160:163], v[136:139]\n"
"ds_read_b128 a[120:123], v255 offset:1024\n"
"v_add_f32 v215, 1.0, v215\n"
"v_mfma_f32_16x16x32_bf16 v[136:139], a[44:47], a[168:171], v[136:139]\n"
"ds_read_b128 a[124:127], v255 offset:11264\n"
"buffer_load_dwordx4 a[148:151], v241, s[36:39], s40 offen sc1\n"
"v_mfma_f32_16x16x32_bf16 v[140:143], a[44:47], a[172:175], v[140:143]\n"
"v_rcp_f32 v214, v214\n"
"v_mfma_f32_16x16x32_bf16 v[140:143], a[40:43], a[164:167], v[140:143]\n"
"ds_read_b128 a[128:131], v255 offset:2048\n"
"v_mul_f32 v48, v48, v214\n"
"v_mfma_f32_16x16x32_bf16 v[144:147], a[48:51], a[160:163], v[144:147]\n"
"v_rcp_f32 v215, v215\n"
"v_mfma_f32_16x16x32_bf16 v[144:147], a[52:55], a[168:171], v[144:147]\n"
"ds_read_b128 a[132:135], v255 offset:12288\n"
"buffer_load_dwordx4 a[152:155], v240, s[36:39], s42 offen sc1\n"
"v_mfma_f32_16x16x32_bf16 v[148:151], a[52:55], a[172:175], v[148:151]\n"
"ds_read_b128 a[136:139], v255 offset:3072\n"
"v_mul_f32 v49, v49, v215\n"
"v_mfma_f32_16x16x32_bf16 v[148:151], a[48:51], a[164:167], v[148:151]\n"
"v_exp_f32_e64 v208, -v56\n"
"v_mfma_f32_16x16x32_bf16 v[152:155], a[56:59], a[160:163], v[152:155]\n"
"ds_read_b128 a[140:143], v255 offset:13312\n"
"v_mul_f32 v50, v50, v114\n"
"v_mfma_f32_16x16x32_bf16 v[152:155], a[60:63], a[168:171], v[152:155]\n"
"buffer_load_dwordx4 a[156:159], v241, s[36:39], s42 offen sc1\n"
"v_add_f32 v216, 1.0, v216\n"
"v_mfma_f32_16x16x32_bf16 v[156:159], a[60:63], a[172:175], v[156:159]\n"
"s_add_u32 s40, s40, 16384\n"
"ds_read_b128 a[0:3], v255 offset:4096\n"
"v_mul_f32 v51, v51, v115\n"
"v_mfma_f32_16x16x32_bf16 v[156:159], a[56:59], a[164:167], v[156:159]\n"
"s_add_u32 m0, s43, 40960\n"
"ds_read_b128 a[4:7], v255 offset:14336\n"
"v_add_f32 v217, 1.0, v217\n"
"v_mfma_f32_16x16x32_bf16 v[160:163], a[64:67], a[160:163], v[160:163]\n"
"v_rcp_f32 v216, v216\n"
"v_mfma_f32_16x16x32_bf16 v[160:163], a[68:71], a[168:171], v[160:163]\n"
"ds_read_b128 a[8:11], v255 offset:5120\n"
"buffer_load_dwordx4 v255, s[36:39], s41 offen offset:0 sc1 lds\n"
"v_mfma_f32_16x16x32_bf16 v[164:167], a[68:71], a[172:175], v[164:167]\n"
"v_rcp_f32 v217, v217\n"
"v_mfma_f32_16x16x32_bf16 v[164:167], a[64:67], a[164:167], v[164:167]\n"
"ds_read_b128 a[12:15], v255 offset:15360\n"
"v_mul_f32 v50, v50, v216\n"
"v_mfma_f32_16x16x32_bf16 v[168:171], a[72:75], a[160:163], v[168:171]\n"
"ds_read_b128 a[16:19], v255 offset:6144\n"
"v_mul_f32 v51, v51, v217\n"
"v_mfma_f32_16x16x32_bf16 v[168:171], a[76:79], a[168:171], v[168:171]\n"
"buffer_load_dwordx4 v255, s[36:39], s41 offen offset:1024 sc1 lds\n"
"v_cvt_pk_bf16_f32 v98, v48, v49\n"
"v_mfma_f32_16x16x32_bf16 v[172:175], a[76:79], a[172:175], v[172:175]\n"
"ds_read_b128 a[20:23], v255 offset:16384\n"
"v_cvt_pk_bf16_f32 v99, v50, v51\n"
"v_mfma_f32_16x16x32_bf16 v[172:175], a[72:75], a[164:167], v[172:175]\n"
"v_exp_f32_e64 v209, -v57\n"
"v_mfma_f32_16x16x32_bf16 v[176:179], a[80:83], a[160:163], v[176:179]\n"
"ds_read_b128 a[24:27], v255 offset:7168\n"
"buffer_store_dwordx4 v[96:99], v255, s[36:39], s85 offen offset:0 sc1\n"
"v_mfma_f32_16x16x32_bf16 v[176:179], a[84:87], a[168:171], v[176:179]\n"
"ds_read_b128 a[28:31], v255 offset:17408\n"
"buffer_load_dwordx4 v255, s[36:39], s41 offen offset:2048 sc1 lds\n"
"v_mfma_f32_16x16x32_bf16 v[180:183], a[84:87], a[172:175], v[180:183]\n"
"v_exp_f32_e64 v210, -v58\n"
"v_mfma_f32_16x16x32_bf16 v[180:183], a[80:83], a[164:167], v[180:183]\n"
"ds_read_b128 a[208:211], v255 offset:8192\n"
"v_mul_f32 v52, v52, v116\n"
"v_mfma_f32_16x16x32_bf16 v[184:187], a[88:91], a[160:163], v[184:187]\n"
"v_exp_f32_e64 v211, -v59\n"
"v_mfma_f32_16x16x32_bf16 v[184:187], a[92:95], a[168:171], v[184:187]\n"
"ds_read_b128 a[212:215], v255 offset:18432\n"
"buffer_load_dwordx4 v255, s[36:39], s41 offen offset:3072 sc1 lds\n"
"v_mfma_f32_16x16x32_bf16 v[188:191], a[92:95], a[172:175], v[188:191]\n"
"s_add_u32 m0, s43, 45056\n"
"ds_read_b128 a[216:219], v255 offset:9216\n"
"v_add_f32 v219, 1.0, v219\n"
"v_mfma_f32_16x16x32_bf16 v[188:191], a[88:91], a[164:167], v[188:191]\n"
".L_pad13:\n"
"s_cbranch_execz .L_padret1\n"
"v_rcp_f32 v219, v219\n"
"v_mfma_f32_16x16x32_bf16 v[192:195], a[96:99], a[160:163], v[192:195]\n"
"ds_read_b128 a[220:223], v255 offset:19456\n"
"v_mul_f32 v52, v52, v219\n"
"v_mfma_f32_16x16x32_bf16 v[192:195], a[100:103], a[168:171], v[192:195]\n"
"buffer_load_dwordx4 v236, s[36:39], s41 offen sc1 lds\n"
"v_mul_f32 v53, v53, v117\n"
"v_mfma_f32_16x16x32_bf16 v[196:199], a[100:103], a[172:175], v[196:199]\n"
"s_add_u32 s41, s41, 81920\n"
"v_exp_f32_e64 v212, -v60\n"
"v_mfma_f32_16x16x32_bf16 v[196:199], a[96:99], a[164:167], v[196:199]\n"
"v_add_f32 v222, 1.0, v222\n"
"v_mul_f32 v54, v54, v118\n"
"v_mfma_f32_16x16x32_bf16 v[200:203], a[104:107], a[160:163], v[200:203]\n"
"v_rcp_f32 v222, v222\n"
"v_mfma_f32_16x16x32_bf16 v[200:203], a[108:111], a[168:171], v[200:203]\n"
"v_mul_f32 v53, v53, v222\n"
"v_add_f32 v223, 1.0, v223\n"
"v_mfma_f32_16x16x32_bf16 v[204:207], a[108:111], a[172:175], v[204:207]\n"
"v_rcp_f32 v223, v223\n"
"v_mfma_f32_16x16x32_bf16 v[204:207], a[104:107], a[164:167], v[204:207]\n"
"s_waitcnt vmcnt(34) lgkmcnt(0)\n"
"v_exp_f32_e64 v213, -v61\n"
"v_mfma_f32_16x16x32_bf16 v[128:131], a[112:115], a[176:179], v[128:131]\n"
"s_nop 0\n"
"s_barrier\n"
"s_add_u32 s42, s40, 8192\n"
"v_mfma_f32_16x16x32_bf16 v[128:131], a[116:119], a[184:187], v[128:131]\n"
"ds_read_b128 a[32:35], v242 offset:0\n"
"buffer_load_dwordx4 a[160:163], v240, s[36:39], s40 offen sc1\n"
"v_mfma_f32_16x16x32_bf16 v[132:135], a[116:119], a[188:191], v[132:135]\n"
"s_lshl1_add_u32 s97, s69, s45\n"
"s_mul_i32 s97, s97, 442368\n"
"ds_read_b128 a[36:39], v242 offset:10240\n"
"v_mfma_f32_16x16x32_bf16 v[132:135], a[112:115], a[180:183], v[132:135]\n"
"s_add_u32 s97, s97, 11534336\n"
"v_mul_f32 v54, v54, v223\n"
"v_mul_f32 v55, v55, v119\n"
"v_mfma_f32_16x16x32_bf16 v[136:139], a[120:123], a[176:179], v[136:139]\n"
"s_add_u32 s24, s36, s97\n"
"s_addc_u32 s25, s37, 0\n"
"ds_read_b128 a[40:43], v242 offset:1024\n"
"v_mfma_f32_16x16x32_bf16 v[136:139], a[124:127], a[184:187], v[136:139]\n"
"s_mov_b32 s26, 442368\n"
"s_mov_b32 s30, 2211840\n"
"ds_read_b128 a[44:47], v242 offset:11264\n"
"v_mfma_f32_16x16x32_bf16 v[140:143], a[124:127], a[188:191], v[140:143]\n"
"s_add_u32 s28, s36, 8388608\n"
"s_addc_u32 s29, s37, 0\n"
"buffer_load_dwordx4 a[164:167], v241, s[36:39], s40 offen sc1\n"
"v_mfma_f32_16x16x32_bf16 v[140:143], a[120:123], a[180:183], v[140:143]\n"
"ds_read_b128 a[48:51], v242 offset:2048\n"
"v_add_f32 v224, 1.0, v224\n"
"v_mfma_f32_16x16x32_bf16 v[144:147], a[128:131], a[176:179], v[144:147]\n"
"v_rcp_f32 v224, v224\n"
"v_mfma_f32_16x16x32_bf16 v[144:147], a[132:135], a[184:187], v[144:147]\n"
"ds_read_b128 a[52:55], v242 offset:12288\n"
"v_mul_f32 v55, v55, v224\n"
"v_mfma_f32_16x16x32_bf16 v[148:151], a[132:135], a[188:191], v[148:151]\n"
"ds_read_b128 a[56:59], v242 offset:3072\n"
"buffer_load_dwordx4 a[168:171], v240, s[36:39], s42 offen sc1\n"
"v_mfma_f32_16x16x32_bf16 v[148:151], a[128:131], a[180:183], v[148:151]\n"
"v_exp_f32_e64 v214, -v62\n"
"v_mfma_f32_16x16x32_bf16 v[152:155], a[136:139], a[176:179], v[152:155]\n"
"ds_read_b128 a[60:63], v242 offset:13312\n"
"v_cvt_pk_bf16_f32 v102, v52, v53\n"
"v_mfma_f32_16x16x32_bf16 v[152:155], a[140:143], a[184:187], v[152:155]\n"
"v_cvt_pk_bf16_f32 v103, v54, v55\n"
"v_mul_f32 v56, v56, v120\n"
"v_mfma_f32_16x16x32_bf16 v[156:159], a[140:143], a[188:191], v[156:159]\n"
"ds_read_b128 a[64:67], v242 offset:4096\n"
"buffer_load_dwordx4 a[172:175], v241, s[36:39], s42 offen sc1\n"
"v_mfma_f32_16x16x32_bf16 v[156:159], a[136:139], a[180:183], v[156:159]\n"
"s_add_u32 s40, s40, 16384\n"
"ds_read_b128 a[68:71], v242 offset:14336\n"
"buffer_store_dwordx4 v[100:103], v255, s[36:39], s85 offen offset:1024 sc1\n"
"v_mfma_f32_16x16x32_bf16 v[160:163], a[0:3], a[176:179], v[160:163]\n"
"s_add_u32 m0, s43, 122880\n"
"v_exp_f32_e64 v215, -v63\n"
"v_mfma_f32_16x16x32_bf16 v[160:163], a[4:7], a[184:187], v[160:163]\n"
"ds_read_b128 a[72:75], v242 offset:5120\n"
"v_add_f32 v208, 1.0, v208\n"
"v_mfma_f32_16x16x32_bf16 v[164:167], a[4:7], a[188:191], v[164:167]\n"
"buffer_load_dwordx4 v255, s[36:39], s41 offen offset:0 sc1 lds\n"
"v_mul_f32 v57, v57, v121\n"
"v_mfma_f32_16x16x32_bf16 v[164:167], a[0:3], a[180:183], v[164:167]\n"
"ds_read_b128 a[76:79], v242 offset:15360\n"
"v_add_f32 v209, 1.0, v209\n"
"v_mfma_f32_16x16x32_bf16 v[168:171], a[8:11], a[176:179], v[168:171]\n"
"ds_read_b128 a[80:83], v242 offset:6144\n"
"v_mul_f32 v58, v58, v122\n"
"v_mfma_f32_16x16x32_bf16 v[168:171], a[12:15], a[184:187], v[168:171]\n"
"v_rcp_f32 v208, v208\n"
"v_mfma_f32_16x16x32_bf16 v[172:175], a[12:15], a[188:191], v[172:175]\n"
"ds_read_b128 a[84:87], v242 offset:16384\n"
"buffer_load_dwordx4 v255, s[36:39], s41 offen offset:1024 sc1 lds\n"
"v_mfma_f32_16x16x32_bf16 v[172:175], a[8:11], a[180:183], v[172:175]\n"
"v_rcp_f32 v209, v209\n"
"v_mfma_f32_16x16x32_bf16 v[176:179], a[16:19], a[176:179], v[176:179]\n"
"ds_read_b128 a[88:91], v242 offset:7168\n"
"v_mul_f32 v56, v56, v208\n"
"v_mfma_f32_16x16x32_bf16 v[176:179], a[20:23], a[184:187], v[176:179]\n"
"ds_read_b128 a[92:95], v242 offset:17408\n"
"v_mul_f32 v57, v57, v209\n"
"v_mfma_f32_16x16x32_bf16 v[180:183], a[20:23], a[188:191], v[180:183]\n"
"buffer_load_dwordx4 v255, s[36:39], s41 offen offset:2048 sc1 lds\n"
"v_add_f32 v210, 1.0, v210\n"
"v_mfma_f32_16x16x32_bf16 v[180:183], a[16:19], a[180:183], v[180:183]\n"
"ds_read_b128 a[96:99], v242 offset:8192\n"
"v_mul_f32 v59, v59, v123\n"
"v_mfma_f32_16x16x32_bf16 v[184:187], a[24:27], a[176:179], v[184:187]\n"
"v_rcp_f32 v210, v210\n"
"v_mfma_f32_16x16x32_bf16 v[184:187], a[28:31], a[184:187], v[184:187]\n"
"ds_read_b128 a[100:103], v242 offset:18432\n"
"v_mul_f32 v58, v58, v210\n"
"v_mfma_f32_16x16x32_bf16 v[188:191], a[28:31], a[188:191], v[188:191]\n"
"ds_read_b128 a[104:107], v242 offset:9216\n"
"buffer_load_dwordx4 v255, s[36:39], s41 offen offset:3072 sc1 lds\n"
"v_mfma_f32_16x16x32_bf16 v[188:191], a[24:27], a[180:183], v[188:191]\n"
"s_add_u32 m0, s43, 126976\n"
"v_add_f32 v211, 1.0, v211\n"
"v_cvt_pk_bf16_f32 v106, v56, v57\n"
"v_mfma_f32_16x16x32_bf16 v[192:195], a[208:211], a[176:179], v[192:195]\n"
"ds_read_b128 a[108:111], v242 offset:19456\n"
"v_mul_f32 v60, v60, v124\n"
"v_mfma_f32_16x16x32_bf16 v[192:195], a[212:215], a[184:187], v[192:195]\n"
"v_rcp_f32 v211, v211\n"
"v_mfma_f32_16x16x32_bf16 v[196:199], a[212:215], a[188:191], v[196:199]\n"
"buffer_load_dwordx4 v236, s[36:39], s41 offen sc1 lds\n"
"v_mul_f32 v59, v59, v211\n"
"v_mfma_f32_16x16x32_bf16 v[196:199], a[208:211], a[180:183], v[196:199]\n"
"s_add_u32 s41, s41, 81920\n"
"v_cvt_pk_bf16_f32 v107, v58, v59\n"
"v_add_f32 v212, 1.0, v212\n"
"v_mfma_f32_16x16x32_bf16 v[200:203], a[216:219], a[176:179], v[200:203]\n"
"v_rcp_f32 v212, v212\n"
"v_mfma_f32_16x16x32_bf16 v[200:203], a[220:223], a[184:187], v[200:203]\n"
"buffer_store_dwordx4 v[104:107], v255, s[36:39], s85 offen offset:2048 sc1\n"
"v_mul_f32 v60, v60, v212\n"
"v_mfma_f32_16x16x32_bf16 v[204:207], a[220:223], a[188:191], v[204:207]\n"
"v_mul_f32 v61, v61, v125\n"
"v_add_f32 v213, 1.0, v213\n"
"v_mfma_f32_16x16x32_bf16 v[204:207], a[216:219], a[180:183], v[204:207]\n"
"s_waitcnt vmcnt(35) lgkmcnt(0)\n"
"v_rcp_f32 v213, v213\n"
"v_mfma_f32_16x16x32_bf16 v[128:131], a[32:35], a[192:195], v[128:131]\n"
"s_nop 0\n"
"s_barrier\n"
"s_add_u32 s42, s40, 8192\n"
"v_mfma_f32_16x16x32_bf16 v[128:131], a[36:39], a[200:203], v[128:131]\n"
"ds_read_b128 a[112:115], v242 offset:20480\n"
"buffer_load_dwordx4 a[176:179], v240, s[36:39], s40 offen sc1\n"
"v_mfma_f32_16x16x32_bf16 v[132:135], a[36:39], a[204:207], v[132:135]\n"
"ds_read_b128 a[116:119], v242 offset:30720\n"
"v_mul_f32 v61, v61, v213\n"
"v_mfma_f32_16x16x32_bf16 v[132:135], a[32:35], a[196:199], v[132:135]\n"
"v_mul_f32 v62, v62, v126\n"
"v_add_f32 v214, 1.0, v214\n"
"v_mfma_f32_16x16x32_bf16 v[136:139], a[40:43], a[192:195], v[136:139]\n"
"ds_read_b128 a[120:123], v242 offset:21504\n"
"v_mul_f32 v63, v63, v127\n"
"v_mfma_f32_16x16x32_bf16 v[136:139], a[44:47], a[200:203], v[136:139]\n"
"ds_read_b128 a[124:127], v242 offset:31744\n"
"buffer_load_dwordx4 a[180:183], v241, s[36:39], s40 offen sc1\n"
"v_mfma_f32_16x16x32_bf16 v[140:143], a[44:47], a[204:207], v[140:143]\n"
"v_rcp_f32 v214, v214\n"
"v_mfma_f32_16x16x32_bf16 v[140:143], a[40:43], a[196:199], v[140:143]\n"
"ds_read_b128 a[128:131], v242 offset:22528\n"
"v_mul_f32 v62, v62, v214\n"
"v_mfma_f32_16x16x32_bf16 v[144:147], a[48:51], a[192:195], v[144:147]\n"
"v_add_f32 v215, 1.0, v215\n"
"v_cvt_pk_bf16_f32 v110, v60, v61\n"
"v_mfma_f32_16x16x32_bf16 v[144:147], a[52:55], a[200:203], v[144:147]\n"
"ds_read_b128 a[132:135], v242 offset:32768\n"
"buffer_load_dwordx4 a[184:187], v240, s[36:39], s42 offen sc1\n"
"v_mfma_f32_16x16x32_bf16 v[148:151], a[52:55], a[204:207], v[148:151]\n"
"ds_read_b128 a[136:139], v242 offset:23552\n"
"v_mfma_f32_16x16x32_bf16 v[148:151], a[48:51], a[196:199], v[148:151]\n"
"v_rcp_f32 v215, v215\n"
"v_mfma_f32_16x16x32_bf16 v[152:155], a[56:59], a[192:195], v[152:155]\n"
"ds_read_b128 a[140:143], v242 offset:33792\n"
"v_mul_f32 v63, v63, v215\n"
"v_mfma_f32_16x16x32_bf16 v[152:155], a[60:63], a[200:203], v[152:155]\n"
"buffer_load_dwordx4 a[188:191], v241, s[36:39], s42 offen sc1\n"
"v_cvt_pk_bf16_f32 v111, v62, v63\n"
"v_mfma_f32_16x16x32_bf16 v[156:159], a[60:63], a[204:207], v[156:159]\n"
"s_add_u32 s40, s40, 16384\n"
"ds_read_b128 a[0:3], v242 offset:24576\n"
"buffer_store_dwordx4 v[108:111], v255, s[36:39], s85 offen offset:3072 sc1\n"
"v_mfma_f32_16x16x32_bf16 v[156:159], a[56:59], a[196:199], v[156:159]\n"
"s_add_u32 m0, s43, 0\n"
"s_add_u32 s85, s85, 8192\n"
"ds_read_b128 a[4:7], v242 offset:34816\n"
"v_mfma_f32_16x16x32_bf16 v[160:163], a[64:67], a[192:195], v[160:163]\n"
"v_mfma_f32_16x16x32_bf16 v[160:163], a[68:71], a[200:203], v[160:163]\n"
"ds_read_b128 a[8:11], v242 offset:25600\n"
"buffer_load_dwordx4 v255, s[36:39], s41 offen offset:0 sc1 lds\n"
"v_mfma_f32_16x16x32_bf16 v[164:167], a[68:71], a[204:207], v[164:167]\n"
"v_mfma_f32_16x16x32_bf16 v[164:167], a[64:67], a[196:199], v[164:167]\n"
"ds_read_b128 a[12:15], v242 offset:35840\n"
"v_mfma_f32_16x16x32_bf16 v[168:171], a[72:75], a[192:195], v[168:171]\n"
"ds_read_b128 a[16:19], v242 offset:26624\n"
"v_mfma_f32_16x16x32_bf16 v[168:171], a[76:79], a[200:203], v[168:171]\n"
"buffer_load_dwordx4 v255, s[36:39], s41 offen offset:1024 sc1 lds\n"
"v_mfma_f32_16x16x32_bf16 v[172:175], a[76:79], a[204:207], v[172:175]\n"
"ds_read_b128 a[20:23], v242 offset:36864\n"
"v_mfma_f32_16x16x32_bf16 v[172:175], a[72:75], a[196:199], v[172:175]\n"
"v_mfma_f32_16x16x32_bf16 v[176:179], a[80:83], a[192:195], v[176:179]\n"
"ds_read_b128 a[24:27], v242 offset:27648\n"
"v_mfma_f32_16x16x32_bf16 v[176:179], a[84:87], a[200:203], v[176:179]\n"
"ds_read_b128 a[28:31], v242 offset:37888\n"
"buffer_load_dwordx4 v255, s[36:39], s41 offen offset:2048 sc1 lds\n"
"v_mfma_f32_16x16x32_bf16 v[180:183], a[84:87], a[204:207], v[180:183]\n"
"v_mfma_f32_16x16x32_bf16 v[180:183], a[80:83], a[196:199], v[180:183]\n"
"ds_read_b128 a[208:211], v242 offset:28672\n"
"v_mfma_f32_16x16x32_bf16 v[184:187], a[88:91], a[192:195], v[184:187]\n"
"v_mfma_f32_16x16x32_bf16 v[184:187], a[92:95], a[200:203], v[184:187]\n"
"ds_read_b128 a[212:215], v242 offset:38912\n"
"buffer_load_dwordx4 v255, s[36:39], s41 offen offset:3072 sc1 lds\n"
"v_mfma_f32_16x16x32_bf16 v[188:191], a[92:95], a[204:207], v[188:191]\n"
"s_add_u32 m0, s43, 4096\n"
"ds_read_b128 a[216:219], v242 offset:29696\n"
"v_mfma_f32_16x16x32_bf16 v[188:191], a[88:91], a[196:199], v[188:191]\n"
"v_mfma_f32_16x16x32_bf16 v[192:195], a[96:99], a[192:195], v[192:195]\n"
"ds_read_b128 a[220:223], v242 offset:39936\n"
"v_mfma_f32_16x16x32_bf16 v[192:195], a[100:103], a[200:203], v[192:195]\n"
"buffer_load_dwordx4 v236, s[36:39], s41 offen sc1 lds\n"
"v_mfma_f32_16x16x32_bf16 v[196:199], a[100:103], a[204:207], v[196:199]\n"
"s_add_u32 s41, s41, 81920\n"
"v_mfma_f32_16x16x32_bf16 v[196:199], a[96:99], a[196:199], v[196:199]\n"
"v_mfma_f32_16x16x32_bf16 v[200:203], a[104:107], a[192:195], v[200:203]\n"
"v_mfma_f32_16x16x32_bf16 v[200:203], a[108:111], a[200:203], v[200:203]\n"
"v_mfma_f32_16x16x32_bf16 v[204:207], a[108:111], a[204:207], v[204:207]\n"
"v_mfma_f32_16x16x32_bf16 v[204:207], a[104:107], a[196:199], v[204:207]\n"
"s_waitcnt vmcnt(36) lgkmcnt(0)\n"
"v_mfma_f32_16x16x32_bf16 v[128:131], a[112:115], a[240:243], v[128:131]\n"
"s_nop 0\n"
"s_barrier\n"
"s_add_u32 s42, s40, 8192\n"
"v_mfma_f32_16x16x32_bf16 v[128:131], a[116:119], a[248:251], v[128:131]\n"
"ds_read_b128 a[32:35], v242 offset:40960\n"
"buffer_load_dwordx4 a[192:195], v240, s[36:39], s40 offen sc1\n"
"v_mfma_f32_16x16x32_bf16 v[132:135], a[116:119], a[252:255], v[132:135]\n"
"ds_read_b128 a[36:39], v242 offset:51200\n"
"v_mfma_f32_16x16x32_bf16 v[132:135], a[112:115], a[244:247], v[132:135]\n"
"v_mfma_f32_16x16x32_bf16 v[136:139], a[120:123], a[240:243], v[136:139]\n"
"ds_read_b128 a[40:43], v242 offset:41984\n"
"v_mfma_f32_16x16x32_bf16 v[136:139], a[124:127], a[248:251], v[136:139]\n"
"ds_read_b128 a[44:47], v242 offset:52224\n"
"buffer_load_dwordx4 a[196:199], v241, s[36:39], s40 offen sc1\n"
"v_mfma_f32_16x16x32_bf16 v[140:143], a[124:127], a[252:255], v[140:143]\n"
"v_mfma_f32_16x16x32_bf16 v[140:143], a[120:123], a[244:247], v[140:143]\n"
"ds_read_b128 a[48:51], v242 offset:43008\n"
"v_mfma_f32_16x16x32_bf16 v[144:147], a[128:131], a[240:243], v[144:147]\n"
"v_mfma_f32_16x16x32_bf16 v[144:147], a[132:135], a[248:251], v[144:147]\n"
"ds_read_b128 a[52:55], v242 offset:53248\n"
"buffer_load_dwordx4 a[200:203], v240, s[36:39], s42 offen sc1\n"
"v_mfma_f32_16x16x32_bf16 v[148:151], a[132:135], a[252:255], v[148:151]\n"
"ds_read_b128 a[56:59], v242 offset:44032\n"
"v_mfma_f32_16x16x32_bf16 v[148:151], a[128:131], a[244:247], v[148:151]\n"
"v_mfma_f32_16x16x32_bf16 v[152:155], a[136:139], a[240:243], v[152:155]\n"
"ds_read_b128 a[60:63], v242 offset:54272\n"
"v_mfma_f32_16x16x32_bf16 v[152:155], a[140:143], a[248:251], v[152:155]\n"
"buffer_load_dwordx4 a[204:207], v241, s[36:39], s42 offen sc1\n"
"v_mfma_f32_16x16x32_bf16 v[156:159], a[140:143], a[252:255], v[156:159]\n"
"s_add_u32 s40, s40, 16384\n"
"ds_read_b128 a[64:67], v242 offset:45056\n"
"v_mfma_f32_16x16x32_bf16 v[156:159], a[136:139], a[244:247], v[156:159]\n"
"s_add_u32 m0, s43, 61440\n"
"ds_read_b128 a[68:71], v242 offset:55296\n"
"v_mfma_f32_16x16x32_bf16 v[160:163], a[0:3], a[240:243], v[160:163]\n"
"v_mfma_f32_16x16x32_bf16 v[160:163], a[4:7], a[248:251], v[160:163]\n"
"ds_read_b128 a[72:75], v242 offset:46080\n"
"buffer_load_dwordx4 v255, s[36:39], s41 offen offset:0 sc1 lds\n"
"v_mfma_f32_16x16x32_bf16 v[164:167], a[4:7], a[252:255], v[164:167]\n"
"v_mfma_f32_16x16x32_bf16 v[164:167], a[0:3], a[244:247], v[164:167]\n"
"ds_read_b128 a[76:79], v242 offset:56320\n"
"v_mfma_f32_16x16x32_bf16 v[168:171], a[8:11], a[240:243], v[168:171]\n"
"ds_read_b128 a[80:83], v242 offset:47104\n"
"v_mfma_f32_16x16x32_bf16 v[168:171], a[12:15], a[248:251], v[168:171]\n"
"buffer_load_dwordx4 v255, s[36:39], s41 offen offset:1024 sc1 lds\n"
"v_mfma_f32_16x16x32_bf16 v[172:175], a[12:15], a[252:255], v[172:175]\n"
"ds_read_b128 a[84:87], v242 offset:57344\n"
"v_mfma_f32_16x16x32_bf16 v[172:175], a[8:11], a[244:247], v[172:175]\n"
"v_mfma_f32_16x16x32_bf16 v[176:179], a[16:19], a[240:243], v[176:179]\n"
"ds_read_b128 a[88:91], v242 offset:48128\n"
"v_mfma_f32_16x16x32_bf16 v[176:179], a[20:23], a[248:251], v[176:179]\n"
"ds_read_b128 a[92:95], v242 offset:58368\n"
"buffer_load_dwordx4 v255, s[36:39], s41 offen offset:2048 sc1 lds\n"
"v_mfma_f32_16x16x32_bf16 v[180:183], a[20:23], a[252:255], v[180:183]\n"
"v_mfma_f32_16x16x32_bf16 v[180:183], a[16:19], a[244:247], v[180:183]\n"
"ds_read_b128 a[96:99], v242 offset:49152\n"
"v_mfma_f32_16x16x32_bf16 v[184:187], a[24:27], a[240:243], v[184:187]\n"
"v_mfma_f32_16x16x32_bf16 v[184:187], a[28:31], a[248:251], v[184:187]\n"
"ds_read_b128 a[100:103], v242 offset:59392\n"
"buffer_load_dwordx4 v255, s[36:39], s41 offen offset:3072 sc1 lds\n"
"v_mfma_f32_16x16x32_bf16 v[188:191], a[28:31], a[252:255], v[188:191]\n"
"s_add_u32 m0, s43, 65536\n"
"ds_read_b128 a[104:107], v242 offset:50176\n"
"v_mfma_f32_16x16x32_bf16 v[188:191], a[24:27], a[244:247], v[188:191]\n"
"v_mfma_f32_16x16x32_bf16 v[192:195], a[208:211], a[240:243], v[192:195]\n"
"ds_read_b128 a[108:111], v242 offset:60416\n"
"v_mfma_f32_16x16x32_bf16 v[192:195], a[212:215], a[248:251], v[192:195]\n"
"buffer_load_dwordx4 v236, s[36:39], s41 offen sc1 lds\n"
"v_mfma_f32_16x16x32_bf16 v[196:199], a[212:215], a[252:255], v[196:199]\n"
"s_add_u32 s41, s41, 81920\n"
"v_mfma_f32_16x16x32_bf16 v[196:199], a[208:211], a[244:247], v[196:199]\n"
"v_mfma_f32_16x16x32_bf16 v[200:203], a[216:219], a[240:243], v[200:203]\n"
"v_mfma_f32_16x16x32_bf16 v[200:203], a[220:223], a[248:251], v[200:203]\n"
"v_mfma_f32_16x16x32_bf16 v[204:207], a[220:223], a[252:255], v[204:207]\n"
"v_mfma_f32_16x16x32_bf16 v[204:207], a[216:219], a[244:247], v[204:207]\n"
"s_waitcnt vmcnt(36) lgkmcnt(0)\n"
"v_mfma_f32_16x16x32_bf16 v[128:131], a[32:35], a[144:147], v[128:131]\n"
"s_waitcnt vmcnt(14)\n"
"s_barrier\n"
"s_add_u32 s42, s40, 8192\n"
"v_mfma_f32_16x16x32_bf16 v[128:131], a[36:39], a[152:155], v[128:131]\n"
"s_lshl_b32 s90, s75, 6\n"
"s_add_u32 s90, s90, 2703360\n"
"ds_read_b128 a[112:115], v255 offset:20480\n"
"v_mfma_f32_16x16x32_bf16 v[132:135], a[36:39], a[156:159], v[132:135]\n"
"ds_read_b128 a[116:119], v255 offset:30720\n"
"v_mov_b32 v218, s73\n"
"v_mfma_f32_16x16x32_bf16 v[132:135], a[32:35], a[148:151], v[132:135]\n"
"buffer_store_dword v218, v225, s[36:39], s90 offen sc1\n"
"buffer_load_dwordx4 a[240:243], v240, s[36:39], s40 offen sc1\n"
"v_mfma_f32_16x16x32_bf16 v[136:139], a[40:43], a[144:147], v[136:139]\n"
"ds_read_b128 a[120:123], v255 offset:21504\n"
"v_mfma_f32_16x16x32_bf16 v[136:139], a[44:47], a[152:155], v[136:139]\n"
"ds_read_b128 a[124:127], v255 offset:31744\n"
"v_mfma_f32_16x16x32_bf16 v[140:143], a[44:47], a[156:159], v[140:143]\n"
"v_mfma_f32_16x16x32_bf16 v[140:143], a[40:43], a[148:151], v[140:143]\n"
"ds_read_b128 a[128:131], v255 offset:22528\n"
"buffer_load_dwordx4 a[244:247], v241, s[36:39], s40 offen sc1\n"
"v_mfma_f32_16x16x32_bf16 v[144:147], a[48:51], a[144:147], v[144:147]\n"
"v_mfma_f32_16x16x32_bf16 v[144:147], a[52:55], a[152:155], v[144:147]\n"
"ds_read_b128 a[132:135], v255 offset:32768\n"
"v_mfma_f32_16x16x32_bf16 v[148:151], a[52:55], a[156:159], v[148:151]\n"
"ds_read_b128 a[136:139], v255 offset:23552\n"
"v_mfma_f32_16x16x32_bf16 v[148:151], a[48:51], a[148:151], v[148:151]\n"
"buffer_load_dwordx4 a[248:251], v240, s[36:39], s42 offen sc1\n"
"v_mfma_f32_16x16x32_bf16 v[152:155], a[56:59], a[144:147], v[152:155]\n"
"ds_read_b128 a[140:143], v255 offset:33792\n"
"v_mfma_f32_16x16x32_bf16 v[152:155], a[60:63], a[152:155], v[152:155]\n"
"v_mfma_f32_16x16x32_bf16 v[156:159], a[60:63], a[156:159], v[156:159]\n"
"ds_read_b128 a[0:3], v255 offset:24576\n"
"v_mfma_f32_16x16x32_bf16 v[156:159], a[56:59], a[148:151], v[156:159]\n"
"ds_read_b128 a[4:7], v255 offset:34816\n"
"buffer_load_dwordx4 a[252:255], v241, s[36:39], s42 offen sc1\n"
"v_mfma_f32_16x16x32_bf16 v[160:163], a[64:67], a[144:147], v[160:163]\n"
"s_add_u32 s40, s40, 16384\n"
"v_mfma_f32_16x16x32_bf16 v[160:163], a[68:71], a[152:155], v[160:163]\n"
"s_add_u32 m0, s43, 81920\n"
"ds_read_b128 a[8:11], v255 offset:25600\n"
"v_mfma_f32_16x16x32_bf16 v[164:167], a[68:71], a[156:159], v[164:167]\n"
"v_mfma_f32_16x16x32_bf16 v[164:167], a[64:67], a[148:151], v[164:167]\n"
"ds_read_b128 a[12:15], v255 offset:35840\n"
"buffer_load_dwordx4 v255, s[28:31], s83 offen offset:0 sc1 lds\n"
"v_mfma_f32_16x16x32_bf16 v[168:171], a[72:75], a[144:147], v[168:171]\n"
"ds_read_b128 a[16:19], v255 offset:26624\n"
"v_mfma_f32_16x16x32_bf16 v[168:171], a[76:79], a[152:155], v[168:171]\n"
"v_mfma_f32_16x16x32_bf16 v[172:175], a[76:79], a[156:159], v[172:175]\n"
"ds_read_b128 a[20:23], v255 offset:36864\n"
"v_mfma_f32_16x16x32_bf16 v[172:175], a[72:75], a[148:151], v[172:175]\n"
"buffer_load_dwordx4 v255, s[28:31], s83 offen offset:1024 sc1 lds\n"
"v_mfma_f32_16x16x32_bf16 v[176:179], a[80:83], a[144:147], v[176:179]\n"
"ds_read_b128 a[24:27], v255 offset:27648\n"
"v_mfma_f32_16x16x32_bf16 v[176:179], a[84:87], a[152:155], v[176:179]\n"
"ds_read_b128 a[28:31], v255 offset:37888\n"
"v_mfma_f32_16x16x32_bf16 v[180:183], a[84:87], a[156:159], v[180:183]\n"
"v_mfma_f32_16x16x32_bf16 v[180:183], a[80:83], a[148:151], v[180:183]\n"
"ds_read_b128 a[208:211], v255 offset:28672\n"
"buffer_load_dwordx4 v255, s[28:31], s83 offen offset:2048 sc1 lds\n"
"v_mfma_f32_16x16x32_bf16 v[184:187], a[88:91], a[144:147], v[184:187]\n"
"v_mfma_f32_16x16x32_bf16 v[184:187], a[92:95], a[152:155], v[184:187]\n"
"ds_read_b128 a[212:215], v255 offset:38912\n"
"v_mfma_f32_16x16x32_bf16 v[188:191], a[92:95], a[156:159], v[188:191]\n"
"ds_read_b128 a[216:219], v255 offset:29696\n"
"v_and_b32 v221, 31, v229\n"
"v_mfma_f32_16x16x32_bf16 v[188:191], a[88:91], a[148:151], v[188:191]\n"
"buffer_load_dwordx4 v255, s[28:31], s83 offen offset:3072 sc1 lds\n"
"v_lshlrev_b32 v221, 6, v221\n"
"v_mfma_f32_16x16x32_bf16 v[192:195], a[96:99], a[144:147], v[192:195]\n"
"s_lshl_b32 s95, s69, 11\n"
"s_add_u32 s95, s95, 2703360\n"
"ds_read_b128 a[220:223], v255 offset:39936\n"
"v_mfma_f32_16x16x32_bf16 v[192:195], a[100:103], a[152:155], v[192:195]\n"
"s_add_u32 m0, s43, 86016\n"
"v_mfma_f32_16x16x32_bf16 v[196:199], a[100:103], a[156:159], v[196:199]\n"
"v_mfma_f32_16x16x32_bf16 v[196:199], a[96:99], a[148:151], v[196:199]\n"
"buffer_load_dwordx4 v236, s[28:31], s83 offen sc1 lds\n"
"v_mfma_f32_16x16x32_bf16 v[200:203], a[104:107], a[144:147], v[200:203]\n"
"s_add_u32 s83, s83, 163840\n"
"v_mfma_f32_16x16x32_bf16 v[200:203], a[108:111], a[152:155], v[200:203]\n"
"v_mfma_f32_16x16x32_bf16 v[204:207], a[108:111], a[156:159], v[204:207]\n"
"v_mfma_f32_16x16x32_bf16 v[204:207], a[104:107], a[148:151], v[204:207]\n"
"s_waitcnt lgkmcnt(0)\n"
"v_mfma_f32_16x16x32_bf16 v[128:131], a[112:115], a[160:163], v[128:131]\n"
"s_nop 0\n"
"s_barrier\n"
"s_add_u32 s42, s40, 8192\n"
"v_mfma_f32_16x16x32_bf16 v[128:131], a[116:119], a[168:171], v[128:131]\n"
"s_mul_i32 s92, s68, 2560\n"
"ds_read_b128 a[32:35], v255 offset:40960\n"
"buffer_load_dwordx4 a[144:147], v240, s[36:39], s40 offen sc1\n"
"v_mfma_f32_16x16x32_bf16 v[132:135], a[116:119], a[172:175], v[132:135]\n"
"s_add_u32 s92, s92, 18874368\n"
"ds_read_b128 a[36:39], v255 offset:51200\n"
"v_mfma_f32_16x16x32_bf16 v[132:135], a[112:115], a[164:167], v[132:135]\n"
"v_mfma_f32_16x16x32_bf16 v[136:139], a[120:123], a[160:163], v[136:139]\n"
"s_xor_b32 s90, s71, 1\n"
"ds_read_b128 a[40:43], v255 offset:41984\n"
"v_mfma_f32_16x16x32_bf16 v[136:139], a[124:127], a[168:171], v[136:139]\n"
"ds_read_b128 a[44:47], v255 offset:52224\n"
"buffer_load_dwordx4 a[148:151], v241, s[36:39], s40 offen sc1\n"
"v_mfma_f32_16x16x32_bf16 v[140:143], a[124:127], a[172:175], v[140:143]\n"
"s_lshl2_add_u32 s90, s47, s90\n"
"v_mfma_f32_16x16x32_bf16 v[140:143], a[120:123], a[164:167], v[140:143]\n"
"ds_read_b128 a[48:51], v255 offset:43008\n"
"v_mfma_f32_16x16x32_bf16 v[144:147], a[128:131], a[160:163], v[144:147]\n"
"s_lshl_b32 s90, s90, 15\n"
"v_mfma_f32_16x16x32_bf16 v[144:147], a[132:135], a[168:171], v[144:147]\n"
"ds_read_b128 a[52:55], v255 offset:53248\n"
"buffer_load_dwordx4 a[152:155], v240, s[36:39], s42 offen sc1\n"
"v_mfma_f32_16x16x32_bf16 v[148:151], a[132:135], a[172:175], v[148:151]\n"
"s_add_u32 s90, s90, s92\n"
"ds_read_b128 a[56:59], v255 offset:44032\n"
"v_mfma_f32_16x16x32_bf16 v[148:151], a[128:131], a[164:167], v[148:151]\n"
"v_mfma_f32_16x16x32_bf16 v[152:155], a[136:139], a[160:163], v[152:155]\n"
"s_add_u32 s85, s90, 0\n"
"ds_read_b128 a[60:63], v255 offset:54272\n"
"v_mfma_f32_16x16x32_bf16 v[152:155], a[140:143], a[168:171], v[152:155]\n"
"buffer_load_dwordx4 a[156:159], v241, s[36:39], s42 offen sc1\n"
"v_mfma_f32_16x16x32_bf16 v[156:159], a[140:143], a[172:175], v[156:159]\n"
"s_add_u32 s40, s40, 16384\n"
"s_xor_b32 s90, s71, 2\n"
"ds_read_b128 a[64:67], v255 offset:45056\n"
"v_mfma_f32_16x16x32_bf16 v[156:159], a[136:139], a[164:167], v[156:159]\n"
"s_add_u32 m0, s43, 102400\n"
"ds_read_b128 a[68:71], v255 offset:55296\n"
"v_mfma_f32_16x16x32_bf16 v[160:163], a[0:3], a[160:163], v[160:163]\n"
"s_lshl2_add_u32 s90, s47, s90\n"
"v_mfma_f32_16x16x32_bf16 v[160:163], a[4:7], a[168:171], v[160:163]\n"
"ds_read_b128 a[72:75], v255 offset:46080\n"
"buffer_load_dwordx4 v255, s[28:31], s83 offen offset:0 sc1 lds\n"
"v_mfma_f32_16x16x32_bf16 v[164:167], a[4:7], a[172:175], v[164:167]\n"
"s_lshl_b32 s90, s90, 15\n"
"v_mfma_f32_16x16x32_bf16 v[164:167], a[0:3], a[164:167], v[164:167]\n"
"ds_read_b128 a[76:79], v255 offset:56320\n"
"v_mfma_f32_16x16x32_bf16 v[168:171], a[8:11], a[160:163], v[168:171]\n"
"s_add_u32 s90, s90, s92\n"
"ds_read_b128 a[80:83], v255 offset:47104\n"
"v_mfma_f32_16x16x32_bf16 v[168:171], a[12:15], a[168:171], v[168:171]\n"
"buffer_load_dwordx4 v255, s[28:31], s83 offen offset:1024 sc1 lds\n"
"v_mfma_f32_16x16x32_bf16 v[172:175], a[12:15], a[172:175], v[172:175]\n"
"s_add_u32 s77, s90, 10240\n"
"ds_read_b128 a[84:87], v255 offset:57344\n"
"v_mfma_f32_16x16x32_bf16 v[172:175], a[8:11], a[164:167], v[172:175]\n"
"v_mfma_f32_16x16x32_bf16 v[176:179], a[16:19], a[160:163], v[176:179]\n"
"s_xor_b32 s90, s71, 3\n"
"ds_read_b128 a[88:91], v255 offset:48128\n"
"v_mfma_f32_16x16x32_bf16 v[176:179], a[20:23], a[168:171], v[176:179]\n"
"ds_read_b128 a[92:95], v255 offset:58368\n"
"buffer_load_dwordx4 v255, s[28:31], s83 offen offset:2048 sc1 lds\n"
"v_mfma_f32_16x16x32_bf16 v[180:183], a[20:23], a[172:175], v[180:183]\n"
"s_lshl2_add_u32 s90, s47, s90\n"
"v_mfma_f32_16x16x32_bf16 v[180:183], a[16:19], a[164:167], v[180:183]\n"
"ds_read_b128 a[96:99], v255 offset:49152\n"
"v_mfma_f32_16x16x32_bf16 v[184:187], a[24:27], a[160:163], v[184:187]\n"
"s_lshl_b32 s90, s90, 15\n"
"v_mfma_f32_16x16x32_bf16 v[184:187], a[28:31], a[168:171], v[184:187]\n"
"ds_read_b128 a[100:103], v255 offset:59392\n"
"buffer_load_dwordx4 v255, s[28:31], s83 offen offset:3072 sc1 lds\n"
"v_mfma_f32_16x16x32_bf16 v[188:191], a[28:31], a[172:175], v[188:191]\n"
"s_add_u32 m0, s43, 106496\n"
"s_add_u32 s90, s90, s92\n"
"ds_read_b128 a[104:107], v255 offset:50176\n"
"v_mfma_f32_16x16x32_bf16 v[188:191], a[24:27], a[164:167], v[188:191]\n"
"v_mfma_f32_16x16x32_bf16 v[192:195], a[208:211], a[160:163], v[192:195]\n"
"s_add_u32 s78, s90, 20480\n"
"ds_read_b128 a[108:111], v255 offset:60416\n"
"v_mfma_f32_16x16x32_bf16 v[192:195], a[212:215], a[168:171], v[192:195]\n"
"buffer_load_dwordx4 v236, s[28:31], s83 offen sc1 lds\n"
"v_mfma_f32_16x16x32_bf16 v[196:199], a[212:215], a[172:175], v[196:199]\n"
"s_add_u32 s83, s83, 163840\n"
"s_lshl2_add_u32 s91, s47, s71\n"
"v_mfma_f32_16x16x32_bf16 v[196:199], a[208:211], a[164:167], v[196:199]\n"
"s_add_u32 m0, s43, 20480\n"
"v_mfma_f32_16x16x32_bf16 v[200:203], a[216:219], a[160:163], v[200:203]\n"
"s_lshl_b32 s91, s91, 15\n"
"v_mfma_f32_16x16x32_bf16 v[200:203], a[220:223], a[168:171], v[200:203]\n"
"buffer_load_dwordx4 v255, s[28:31], s83 offen offset:0 sc1 lds\n"
"v_mfma_f32_16x16x32_bf16 v[204:207], a[220:223], a[172:175], v[204:207]\n"
"s_add_u32 s86, s91, s92\n"
"v_mfma_f32_16x16x32_bf16 v[204:207], a[216:219], a[164:167], v[204:207]\n"
"s_waitcnt lgkmcnt(0)\n"
"v_mfma_f32_16x16x32_bf16 v[128:131], a[32:35], a[176:179], v[128:131]\n"
"s_nop 0\n"
"s_barrier\n"
"s_mul_i32 s93, s68, 3072\n"
"v_mfma_f32_16x16x32_bf16 v[128:131], a[36:39], a[184:187], v[128:131]\n"
"s_add_u32 s76, s93, 0x24000\n"
"ds_read_b128 a[112:115], v243 offset:0\n"
"buffer_load_dwordx4 v255, s[28:31], s83 offen offset:1024 sc1 lds\n"
"v_mfma_f32_16x16x32_bf16 v[132:135], a[36:39], a[188:191], v[132:135]\n"
"ds_read_b128 a[116:119], v243 offset:10240\n"
"v_add_u32 v223, 0, v229\n"
"v_mfma_f32_16x16x32_bf16 v[132:135], a[32:35], a[180:183], v[132:135]\n"
"v_mfma_f32_16x16x32_bf16 v[136:139], a[40:43], a[176:179], v[136:139]\n"
"ds_read_b128 a[120:123], v243 offset:1024\n"
"v_mul_u32_u24 v224, 6554, v223\n"
"v_mfma_f32_16x16x32_bf16 v[136:139], a[44:47], a[184:187], v[136:139]\n"
"ds_read_b128 a[124:127], v243 offset:11264\n"
"buffer_load_dwordx4 v255, s[28:31], s83 offen offset:2048 sc1 lds\n"
"v_mfma_f32_16x16x32_bf16 v[140:143], a[44:47], a[188:191], v[140:143]\n"
"v_mfma_f32_16x16x32_bf16 v[140:143], a[40:43], a[180:183], v[140:143]\n"
"ds_read_b128 a[128:131], v243 offset:2048\n"
"v_mfma_f32_16x16x32_bf16 v[144:147], a[48:51], a[176:179], v[144:147]\n"
"v_lshrrev_b32 v224, 16, v224\n"
"v_mfma_f32_16x16x32_bf16 v[144:147], a[52:55], a[184:187], v[144:147]\n"
"ds_read_b128 a[132:135], v243 offset:12288\n"
"buffer_load_dwordx4 v255, s[28:31], s83 offen offset:3072 sc1 lds\n"
"v_mfma_f32_16x16x32_bf16 v[148:151], a[52:55], a[188:191], v[148:151]\n"
"s_add_u32 m0, s43, 24576\n"
"ds_read_b128 a[136:139], v243 offset:3072\n"
"v_mfma_f32_16x16x32_bf16 v[148:151], a[48:51], a[180:183], v[148:151]\n"
"v_mul_u32_u24 v248, 1120, v224\n"
"v_mfma_f32_16x16x32_bf16 v[152:155], a[56:59], a[176:179], v[152:155]\n"
"ds_read_b128 a[140:143], v243 offset:13312\n"
"v_mfma_f32_16x16x32_bf16 v[152:155], a[60:63], a[184:187], v[152:155]\n"
"buffer_load_dwordx4 v236, s[28:31], s83 offen sc1 lds\n"
"v_mfma_f32_16x16x32_bf16 v[156:159], a[60:63], a[188:191], v[156:159]\n"
"s_add_u32 s83, s83, 163840\n"
"ds_read_b128 a[0:3], v243 offset:4096\n"
"v_lshl_add_u32 v248, v223, 4, v248\n"
"v_mfma_f32_16x16x32_bf16 v[156:159], a[56:59], a[180:183], v[156:159]\n"
"s_add_u32 m0, s43, 40960\n"
"ds_read_b128 a[4:7], v243 offset:14336\n"
"v_mfma_f32_16x16x32_bf16 v[160:163], a[64:67], a[176:179], v[160:163]\n"
"v_mfma_f32_16x16x32_bf16 v[160:163], a[68:71], a[184:187], v[160:163]\n"
"ds_read_b128 a[8:11], v243 offset:5120\n"
"buffer_load_dwordx4 v255, s[28:31], s83 offen offset:0 sc1 lds\n"
"v_mfma_f32_16x16x32_bf16 v[164:167], a[68:71], a[188:191], v[164:167]\n"
"v_add_u32 v223, 64, v229\n"
"v_mfma_f32_16x16x32_bf16 v[164:167], a[64:67], a[180:183], v[164:167]\n"
"ds_read_b128 a[12:15], v243 offset:15360\n"
"v_mfma_f32_16x16x32_bf16 v[168:171], a[72:75], a[176:179], v[168:171]\n"
"ds_read_b128 a[16:19], v243 offset:6144\n"
"v_mfma_f32_16x16x32_bf16 v[168:171], a[76:79], a[184:187], v[168:171]\n"
"buffer_load_dwordx4 v255, s[28:31], s83 offen offset:1024 sc1 lds\n"
"v_mul_u32_u24 v224, 6554, v223\n"
"v_mfma_f32_16x16x32_bf16 v[172:175], a[76:79], a[188:191], v[172:175]\n"
"ds_read_b128 a[20:23], v243 offset:16384\n"
"v_mfma_f32_16x16x32_bf16 v[172:175], a[72:75], a[180:183], v[172:175]\n"
"v_mfma_f32_16x16x32_bf16 v[176:179], a[80:83], a[176:179], v[176:179]\n"
".L_pad14:\n"
"s_cbranch_execz .L_padret1\n"
"ds_read_b128 a[24:27], v243 offset:7168\n"
"v_lshrrev_b32 v224, 16, v224\n"
"v_mfma_f32_16x16x32_bf16 v[176:179], a[84:87], a[184:187], v[176:179]\n"
"ds_read_b128 a[28:31], v243 offset:17408\n"
"buffer_load_dwordx4 v255, s[28:31], s83 offen offset:2048 sc1 lds\n"
"v_mfma_f32_16x16x32_bf16 v[180:183], a[84:87], a[188:191], v[180:183]\n"
"v_mfma_f32_16x16x32_bf16 v[180:183], a[80:83], a[180:183], v[180:183]\n"
"ds_read_b128 a[208:211], v243 offset:8192\n"
"v_mul_u32_u24 v249, 1120, v224\n"
"v_mfma_f32_16x16x32_bf16 v[184:187], a[88:91], a[176:179], v[184:187]\n"
"v_mfma_f32_16x16x32_bf16 v[184:187], a[92:95], a[184:187], v[184:187]\n"
"ds_read_b128 a[212:215], v243 offset:18432\n"
"buffer_load_dwordx4 v255, s[28:31], s83 offen offset:3072 sc1 lds\n"
"v_mfma_f32_16x16x32_bf16 v[188:191], a[92:95], a[188:191], v[188:191]\n"
"s_add_u32 m0, s43, 45056\n"
"ds_read_b128 a[216:219], v243 offset:9216\n"
"v_mfma_f32_16x16x32_bf16 v[188:191], a[88:91], a[180:183], v[188:191]\n"
"v_lshl_add_u32 v249, v223, 4, v249\n"
"v_mfma_f32_16x16x32_bf16 v[192:195], a[96:99], a[176:179], v[192:195]\n"
"ds_read_b128 a[220:223], v243 offset:19456\n"
"v_mfma_f32_16x16x32_bf16 v[192:195], a[100:103], a[184:187], v[192:195]\n"
"buffer_load_dwordx4 v236, s[28:31], s83 offen sc1 lds\n"
"v_mfma_f32_16x16x32_bf16 v[196:199], a[100:103], a[188:191], v[196:199]\n"
"s_add_u32 s83, s83, 163840\n"
"v_add_u32 v223, 128, v229\n"
"v_mfma_f32_16x16x32_bf16 v[196:199], a[96:99], a[180:183], v[196:199]\n"
"v_mfma_f32_16x16x32_bf16 v[200:203], a[104:107], a[176:179], v[200:203]\n"
"v_mfma_f32_16x16x32_bf16 v[200:203], a[108:111], a[184:187], v[200:203]\n"
"v_mul_u32_u24 v224, 6554, v223\n"
"v_mfma_f32_16x16x32_bf16 v[204:207], a[108:111], a[188:191], v[204:207]\n"
"v_mfma_f32_16x16x32_bf16 v[204:207], a[104:107], a[180:183], v[204:207]\n"
"s_waitcnt vmcnt(34) lgkmcnt(0)\n"
"v_mfma_f32_16x16x32_bf16 v[128:131], a[112:115], a[192:195], v[128:131]\n"
"s_nop 0\n"
"s_barrier\n"
"v_lshrrev_b32 v224, 16, v224\n"
"v_mfma_f32_16x16x32_bf16 v[128:131], a[116:119], a[200:203], v[128:131]\n"
"s_add_u32 m0, s43, 122880\n"
"ds_read_b128 a[32:35], v255 offset:0\n"
"v_mfma_f32_16x16x32_bf16 v[132:135], a[116:119], a[204:207], v[132:135]\n"
"ds_read_b128 a[36:39], v255 offset:10240\n"
"buffer_load_dwordx4 v255, s[28:31], s83 offen offset:0 sc1 lds\n"
"v_mfma_f32_16x16x32_bf16 v[132:135], a[112:115], a[196:199], v[132:135]\n"
"v_mfma_f32_16x16x32_bf16 v[136:139], a[120:123], a[192:195], v[136:139]\n"
"ds_read_b128 a[40:43], v255 offset:1024\n"
"v_mul_u32_u24 v250, 1120, v224\n"
"v_mfma_f32_16x16x32_bf16 v[136:139], a[124:127], a[200:203], v[136:139]\n"
"ds_read_b128 a[44:47], v255 offset:11264\n"
"v_mfma_f32_16x16x32_bf16 v[140:143], a[124:127], a[204:207], v[140:143]\n"
"buffer_load_dwordx4 v255, s[28:31], s83 offen offset:1024 sc1 lds\n"
"v_mfma_f32_16x16x32_bf16 v[140:143], a[120:123], a[196:199], v[140:143]\n"
"ds_read_b128 a[48:51], v255 offset:2048\n"
"v_lshl_add_u32 v250, v223, 4, v250\n"
"v_mfma_f32_16x16x32_bf16 v[144:147], a[128:131], a[192:195], v[144:147]\n"
"v_mfma_f32_16x16x32_bf16 v[144:147], a[132:135], a[200:203], v[144:147]\n"
"ds_read_b128 a[52:55], v255 offset:12288\n"
"v_mfma_f32_16x16x32_bf16 v[148:151], a[132:135], a[204:207], v[148:151]\n"
"ds_read_b128 a[56:59], v255 offset:3072\n"
"buffer_load_dwordx4 v255, s[28:31], s83 offen offset:2048 sc1 lds\n"
"v_mfma_f32_16x16x32_bf16 v[148:151], a[128:131], a[196:199], v[148:151]\n"
"v_lshrrev_b32 v223, 5, v229\n"
"v_mfma_f32_16x16x32_bf16 v[152:155], a[136:139], a[192:195], v[152:155]\n"
"ds_read_b128 a[60:63], v255 offset:13312\n"
"v_mfma_f32_16x16x32_bf16 v[152:155], a[140:143], a[200:203], v[152:155]\n"
"v_lshl_or_b32 v250, v223, 30, v250\n"
"v_mfma_f32_16x16x32_bf16 v[156:159], a[140:143], a[204:207], v[156:159]\n"
"ds_read_b128 a[64:67], v255 offset:4096\n"
"buffer_load_dwordx4 v255, s[28:31], s83 offen offset:3072 sc1 lds\n"
"v_mfma_f32_16x16x32_bf16 v[156:159], a[136:139], a[196:199], v[156:159]\n"
"s_add_u32 m0, s43, 126976\n"
"ds_read_b128 a[68:71], v255 offset:14336\n"
"v_mfma_f32_16x16x32_bf16 v[160:163], a[0:3], a[192:195], v[160:163]\n"
"v_mfma_f32_16x16x32_bf16 v[160:163], a[4:7], a[200:203], v[160:163]\n"
"ds_read_b128 a[72:75], v255 offset:5120\n"
"v_and_b32 v223, 15, v229\n"
"v_mfma_f32_16x16x32_bf16 v[164:167], a[4:7], a[204:207], v[164:167]\n"
"buffer_load_dwordx4 v236, s[28:31], s83 offen sc1 lds\n"
"v_mfma_f32_16x16x32_bf16 v[164:167], a[0:3], a[196:199], v[164:167]\n"
"s_add_u32 s83, s83, 163840\n"
"ds_read_b128 a[76:79], v255 offset:15360\n"
"v_mfma_f32_16x16x32_bf16 v[168:171], a[8:11], a[192:195], v[168:171]\n"
"ds_read_b128 a[80:83], v255 offset:6144\n"
"buffer_load_dword v220, v221, s[36:39], s95 offen sc1\n"
"v_mfma_f32_16x16x32_bf16 v[168:171], a[12:15], a[200:203], v[168:171]\n"
"v_mul_u32_u24 v223, 160, v223\n"
"v_mfma_f32_16x16x32_bf16 v[172:175], a[12:15], a[204:207], v[172:175]\n"
"ds_read_b128 a[84:87], v255 offset:16384\n"
"v_mfma_f32_16x16x32_bf16 v[172:175], a[8:11], a[196:199], v[172:175]\n"
"v_lshrrev_b32 v224, 4, v229\n"
"v_mfma_f32_16x16x32_bf16 v[176:179], a[16:19], a[192:195], v[176:179]\n"
"ds_read_b128 a[88:91], v255 offset:7168\n"
"v_mfma_f32_16x16x32_bf16 v[176:179], a[20:23], a[200:203], v[176:179]\n"
"ds_read_b128 a[92:95], v255 offset:17408\n"
"v_mfma_f32_16x16x32_bf16 v[180:183], a[20:23], a[204:207], v[180:183]\n"
"v_lshl_add_u32 v223, v224, 3, v223\n"
"v_mfma_f32_16x16x32_bf16 v[180:183], a[16:19], a[196:199], v[180:183]\n"
"ds_read_b128 a[96:99], v255 offset:8192\n"
"v_mfma_f32_16x16x32_bf16 v[184:187], a[24:27], a[192:195], v[184:187]\n"
"v_mfma_f32_16x16x32_bf16 v[184:187], a[28:31], a[200:203], v[184:187]\n"
"ds_read_b128 a[100:103], v255 offset:18432\n"
"v_mfma_f32_16x16x32_bf16 v[188:191], a[28:31], a[204:207], v[188:191]\n"
"ds_read_b128 a[104:107], v255 offset:9216\n"
"v_add_u32 v244, s76, v223\n"
"v_mfma_f32_16x16x32_bf16 v[188:191], a[24:27], a[196:199], v[188:191]\n"
"v_mfma_f32_16x16x32_bf16 v[192:195], a[208:211], a[192:195], v[192:195]\n"
"ds_read_b128 a[108:111], v255 offset:19456\n"
"v_mfma_f32_16x16x32_bf16 v[192:195], a[212:215], a[200:203], v[192:195]\n"
"v_add_u32 v245, s76, v255\n"
"v_mfma_f32_16x16x32_bf16 v[196:199], a[212:215], a[204:207], v[196:199]\n"
"v_mfma_f32_16x16x32_bf16 v[196:199], a[208:211], a[196:199], v[196:199]\n"
"v_mfma_f32_16x16x32_bf16 v[200:203], a[216:219], a[192:195], v[200:203]\n"
"v_add_u32 v246, s76, v237\n"
"v_mfma_f32_16x16x32_bf16 v[200:203], a[220:223], a[200:203], v[200:203]\n"
"v_mfma_f32_16x16x32_bf16 v[204:207], a[220:223], a[204:207], v[204:207]\n"
"v_mfma_f32_16x16x32_bf16 v[204:207], a[216:219], a[196:199], v[204:207]\n"
"s_waitcnt vmcnt(30) lgkmcnt(0)\n"
"v_mfma_f32_16x16x32_bf16 v[128:131], a[32:35], a[240:243], v[128:131]\n"
"s_nop 0\n"
"s_barrier\n"
"v_mfma_f32_16x16x32_bf16 v[128:131], a[36:39], a[248:251], v[128:131]\n"
"s_add_u32 m0, s43, 0\n"
"ds_read_b128 a[112:115], v242 offset:0\n"
"v_mfma_f32_16x16x32_bf16 v[132:135], a[36:39], a[252:255], v[132:135]\n"
"ds_read_b128 a[116:119], v242 offset:10240\n"
"buffer_load_dwordx4 v255, s[28:31], s83 offen offset:0 sc1 lds\n"
"v_mfma_f32_16x16x32_bf16 v[132:135], a[32:35], a[244:247], v[132:135]\n"
"v_mfma_f32_16x16x32_bf16 v[136:139], a[40:43], a[240:243], v[136:139]\n"
"ds_read_b128 a[120:123], v242 offset:1024\n"
"v_mfma_f32_16x16x32_bf16 v[136:139], a[44:47], a[248:251], v[136:139]\n"
"ds_read_b128 a[124:127], v242 offset:11264\n"
"v_mfma_f32_16x16x32_bf16 v[140:143], a[44:47], a[252:255], v[140:143]\n"
"buffer_load_dwordx4 v255, s[28:31], s83 offen offset:1024 sc1 lds\n"
"v_mfma_f32_16x16x32_bf16 v[140:143], a[40:43], a[244:247], v[140:143]\n"
"ds_read_b128 a[128:131], v242 offset:2048\n"
"v_mfma_f32_16x16x32_bf16 v[144:147], a[48:51], a[240:243], v[144:147]\n"
"v_mfma_f32_16x16x32_bf16 v[144:147], a[52:55], a[248:251], v[144:147]\n"
"ds_read_b128 a[132:135], v242 offset:12288\n"
"v_mfma_f32_16x16x32_bf16 v[148:151], a[52:55], a[252:255], v[148:151]\n"
"ds_read_b128 a[136:139], v242 offset:3072\n"
"buffer_load_dwordx4 v255, s[28:31], s83 offen offset:2048 sc1 lds\n"
"v_mfma_f32_16x16x32_bf16 v[148:151], a[48:51], a[244:247], v[148:151]\n"
"v_mfma_f32_16x16x32_bf16 v[152:155], a[56:59], a[240:243], v[152:155]\n"
"ds_read_b128 a[140:143], v242 offset:13312\n"
"v_mfma_f32_16x16x32_bf16 v[152:155], a[60:63], a[248:251], v[152:155]\n"
"v_mfma_f32_16x16x32_bf16 v[156:159], a[60:63], a[252:255], v[156:159]\n"
"ds_read_b128 a[0:3], v242 offset:4096\n"
"buffer_load_dwordx4 v255, s[28:31], s83 offen offset:3072 sc1 lds\n"
"v_mfma_f32_16x16x32_bf16 v[156:159], a[56:59], a[244:247], v[156:159]\n"
"s_add_u32 m0, s43, 4096\n"
"ds_read_b128 a[4:7], v242 offset:14336\n"
"v_mfma_f32_16x16x32_bf16 v[160:163], a[64:67], a[240:243], v[160:163]\n"
"v_mfma_f32_16x16x32_bf16 v[160:163], a[68:71], a[248:251], v[160:163]\n"
"ds_read_b128 a[8:11], v242 offset:5120\n"
"v_mfma_f32_16x16x32_bf16 v[164:167], a[68:71], a[252:255], v[164:167]\n"
"buffer_load_dwordx4 v236, s[28:31], s83 offen sc1 lds\n"
"v_mfma_f32_16x16x32_bf16 v[164:167], a[64:67], a[244:247], v[164:167]\n"
"s_add_u32 s83, s83, 163840\n"
"ds_read_b128 a[12:15], v242 offset:15360\n"
"v_mfma_f32_16x16x32_bf16 v[168:171], a[72:75], a[240:243], v[168:171]\n"
"ds_read_b128 a[16:19], v242 offset:6144\n"
"v_mfma_f32_16x16x32_bf16 v[168:171], a[76:79], a[248:251], v[168:171]\n"
"s_waitcnt vmcnt(5)\n"
"v_cmp_ne_u32 vcc, s73, v220\n"
"v_mfma_f32_16x16x32_bf16 v[172:175], a[76:79], a[252:255], v[172:175]\n"
"s_cbranch_vccnz .L_fh_retry\n"
".L_fh_ok:\n"
"ds_read_b128 a[20:23], v242 offset:16384\n"
"v_mfma_f32_16x16x32_bf16 v[172:175], a[72:75], a[244:247], v[172:175]\n"
"buffer_load_dwordx4 a[160:163], v240, s[24:27], s82 offen sc1\n"
"v_mfma_f32_16x16x32_bf16 v[176:179], a[80:83], a[240:243], v[176:179]\n"
"s_add_u32 s82, s82, 8192\n"
"s_lshl2_add_u32 s91, s75, s68\n"
"ds_read_b128 a[24:27], v242 offset:7168\n"
"v_mfma_f32_16x16x32_bf16 v[176:179], a[84:87], a[248:251], v[176:179]\n"
"s_lshl_b32 s91, s91, 6\n"
"s_add_u32 s80, s91, 2752512\n"
"ds_read_b128 a[28:31], v242 offset:17408\n"
"v_mfma_f32_16x16x32_bf16 v[180:183], a[84:87], a[252:255], v[180:183]\n"
"s_and_b32 s91, s75, 0xe7\n"
"s_lshl2_add_u32 s91, s91, s68\n"
"s_lshl_b32 s91, s91, 6\n"
"v_mfma_f32_16x16x32_bf16 v[180:183], a[80:83], a[244:247], v[180:183]\n"
"s_add_u32 s95, s91, 2752512\n"
"ds_read_b128 a[208:211], v242 offset:8192\n"
"buffer_load_dwordx4 a[164:167], v240, s[24:27], s82 offen sc1\n"
"v_mfma_f32_16x16x32_bf16 v[184:187], a[88:91], a[240:243], v[184:187]\n"
"s_add_u32 s82, s82, 8192\n"
"v_min_u32 v252, 1, v229\n"
"v_lshlrev_b32 v252, 30, v252\n"
"v_mfma_f32_16x16x32_bf16 v[184:187], a[92:95], a[248:251], v[184:187]\n"
"ds_read_b128 a[212:215], v242 offset:18432\n"
"v_mov_b32 v218, s73\n"
"v_mfma_f32_16x16x32_bf16 v[188:191], a[92:95], a[252:255], v[188:191]\n"
"ds_read_b128 a[216:219], v242 offset:9216\n"
"v_and_b32 v221, 3, v229\n"
"v_mfma_f32_16x16x32_bf16 v[188:191], a[88:91], a[244:247], v[188:191]\n"
"buffer_load_dwordx4 a[168:171], v240, s[24:27], s82 offen sc1\n"
"v_max_u32 v221, 1, v221\n"
"v_mfma_f32_16x16x32_bf16 v[192:195], a[96:99], a[240:243], v[192:195]\n"
"s_add_u32 s82, s82, 8192\n"
"ds_read_b128 a[220:223], v242 offset:19456\n"
"v_xor_b32 v221, s71, v221\n"
"v_mfma_f32_16x16x32_bf16 v[192:195], a[100:103], a[248:251], v[192:195]\n"
"v_lshlrev_b32 v221, 11, v221\n"
"v_mfma_f32_16x16x32_bf16 v[196:199], a[100:103], a[252:255], v[196:199]\n"
"s_lshl1_add_u32 s90, s69, s45\n"
"s_lshr_b32 s91, s71, 1\n"
"s_lshl1_add_u32 s90, s90, s91\n"
"v_mfma_f32_16x16x32_bf16 v[196:199], a[96:99], a[244:247], v[196:199]\n"
"s_lshl2_add_u32 s90, s90, s68\n"
"s_mul_i32 s84, s90, 20480\n"
"buffer_load_dwordx4 a[172:175], v240, s[24:27], s82 offen sc1\n"
"v_mfma_f32_16x16x32_bf16 v[200:203], a[104:107], a[240:243], v[200:203]\n"
"s_add_u32 s82, s82, 8192\n"
"s_and_b32 s91, s71, 1\n"
"s_lshl1_add_u32 s91, s46, s91\n"
"v_mfma_f32_16x16x32_bf16 v[200:203], a[108:111], a[248:251], v[200:203]\n"
"s_mul_i32 s91, s91, 160\n"
"s_add_u32 s84, s84, s91\n"
"s_mov_b32 s60, s56\n"
"v_mfma_f32_16x16x32_bf16 v[204:207], a[108:111], a[252:255], v[204:207]\n"
"s_and_b32 s61, s57, 0xffff\n"
"s_mov_b32 s62, 2621440\n"
"s_mov_b32 s63, 0x20000\n"
"v_mfma_f32_16x16x32_bf16 v[204:207], a[104:107], a[244:247], v[204:207]\n"
"s_waitcnt lgkmcnt(0)\n"
"buffer_load_dwordx4 a[176:179], v240, s[24:27], s82 offen sc1\n"
"v_mfma_f32_16x16x32_bf16 v[168:171], a[8:11], a[144:147], v[168:171]\n"
"s_nop 0\n"
"s_barrier\n"
"s_add_u32 s82, s82, 8192\n"
"v_mfma_f32_16x16x32_bf16 v[168:171], a[12:15], a[152:155], v[168:171]\n"
"ds_read_b128 a[32:35], v242 offset:20480\n"
"v_mfma_f32_16x16x32_bf16 v[176:179], a[16:19], a[144:147], v[176:179]\n"
"ds_read_b128 a[36:39], v242 offset:25600\n"
"v_mfma_f32_16x16x32_bf16 v[176:179], a[20:23], a[152:155], v[176:179]\n"
"buffer_load_dwordx4 a[180:183], v240, s[24:27], s82 offen sc1\n"
"v_mfma_f32_16x16x32_bf16 v[184:187], a[24:27], a[144:147], v[184:187]\n"
"s_add_u32 s82, s82, 8192\n"
"ds_read_b128 a[40:43], v242 offset:30720\n"
"v_mfma_f32_16x16x32_bf16 v[184:187], a[28:31], a[152:155], v[184:187]\n"
"ds_read_b128 a[44:47], v242 offset:35840\n"
"v_mfma_f32_16x16x32_bf16 v[192:195], a[208:211], a[144:147], v[192:195]\n"
"v_mfma_f32_16x16x32_bf16 v[192:195], a[212:215], a[152:155], v[192:195]\n"
"ds_read_b128 a[48:51], v242 offset:21504\n"
"buffer_load_dwordx4 a[184:187], v240, s[24:27], s82 offen sc1\n"
"v_mfma_f32_16x16x32_bf16 v[200:203], a[216:219], a[144:147], v[200:203]\n"
"s_add_u32 s82, s82, 8192\n"
"v_mfma_f32_16x16x32_bf16 v[200:203], a[220:223], a[152:155], v[200:203]\n"
"ds_read_b128 a[52:55], v242 offset:26624\n"
"v_mfma_f32_16x16x32_bf16 v[132:135], a[116:119], a[156:159], v[132:135]\n"
"ds_read_b128 a[56:59], v242 offset:31744\n"
"v_mfma_f32_16x16x32_bf16 v[132:135], a[112:115], a[148:151], v[132:135]\n"
"buffer_load_dwordx4 a[188:191], v240, s[24:27], s82 offen sc1\n"
"v_mfma_f32_16x16x32_bf16 v[140:143], a[124:127], a[156:159], v[140:143]\n"
"s_add_u32 s82, s82, 8192\n"
"ds_read_b128 a[60:63], v242 offset:36864\n"
"v_mfma_f32_16x16x32_bf16 v[140:143], a[120:123], a[148:151], v[140:143]\n"
"v_mfma_f32_16x16x32_bf16 v[148:151], a[132:135], a[156:159], v[148:151]\n"
"ds_read_b128 a[64:67], v242 offset:22528\n"
"v_mfma_f32_16x16x32_bf16 v[148:151], a[128:131], a[148:151], v[148:151]\n"
"ds_read_b128 a[68:71], v242 offset:27648\n"
"buffer_load_dwordx4 a[192:195], v240, s[24:27], s82 offen sc1\n"
"v_mfma_f32_16x16x32_bf16 v[156:159], a[140:143], a[156:159], v[156:159]\n"
"s_add_u32 s82, s82, 8192\n"
"v_mfma_f32_16x16x32_bf16 v[156:159], a[136:139], a[148:151], v[156:159]\n"
"ds_read_b128 a[72:75], v242 offset:32768\n"
"v_mfma_f32_16x16x32_bf16 v[164:167], a[4:7], a[156:159], v[164:167]\n"
"v_cvt_pk_bf16_f32 v168, v168, v169\n"
"v_mfma_f32_16x16x32_bf16 v[164:167], a[0:3], a[148:151], v[164:167]\n"
"ds_read_b128 a[76:79], v242 offset:37888\n"
"buffer_load_dwordx4 a[196:199], v240, s[24:27], s82 offen sc1\n"
"v_mfma_f32_16x16x32_bf16 v[172:175], a[12:15], a[156:159], v[172:175]\n"
"s_add_u32 s82, s82, 8192\n"
"ds_read_b128 a[80:83], v242 offset:23552\n"
"v_cvt_pk_bf16_f32 v169, v170, v171\n"
"v_mfma_f32_16x16x32_bf16 v[172:175], a[8:11], a[148:151], v[172:175]\n"
"v_mfma_f32_16x16x32_bf16 v[180:183], a[20:23], a[156:159], v[180:183]\n"
"ds_read_b128 a[84:87], v242 offset:28672\n"
"v_cvt_pk_bf16_f32 v170, v176, v177\n"
"v_mfma_f32_16x16x32_bf16 v[180:183], a[16:19], a[148:151], v[180:183]\n"
"buffer_load_dwordx4 a[200:203], v240, s[24:27], s82 offen sc1\n"
"v_mfma_f32_16x16x32_bf16 v[188:191], a[28:31], a[156:159], v[188:191]\n"
"s_add_u32 s82, s82, 8192\n"
"ds_read_b128 a[88:91], v242 offset:33792\n"
"v_cvt_pk_bf16_f32 v171, v178, v179\n"
"v_mfma_f32_16x16x32_bf16 v[188:191], a[24:27], a[148:151], v[188:191]\n"
"ds_read_b128 a[92:95], v242 offset:38912\n"
"v_mfma_f32_16x16x32_bf16 v[196:199], a[212:215], a[156:159], v[196:199]\n"
"buffer_store_dwordx4 v[168:171], v255, s[36:39], s85 offen offset:0 sc1\n"
"v_mfma_f32_16x16x32_bf16 v[196:199], a[208:211], a[148:151], v[196:199]\n"
"ds_read_b128 a[96:99], v242 offset:24576\n"
"buffer_load_dwordx4 a[204:207], v240, s[24:27], s82 offen sc1\n"
"v_mfma_f32_16x16x32_bf16 v[204:207], a[220:223], a[156:159], v[204:207]\n"
"s_add_u32 s82, s82, 8192\n"
"v_cvt_pk_bf16_f32 v184, v184, v185\n"
"v_mfma_f32_16x16x32_bf16 v[204:207], a[216:219], a[148:151], v[204:207]\n"
"ds_read_b128 a[100:103], v242 offset:29696\n"
"buffer_load_dwordx4 a[240:243], v240, s[24:27], s82 offen sc1\n"
"v_mfma_f32_16x16x32_bf16 v[128:131], a[112:115], a[144:147], v[128:131]\n"
"ds_read_b128 a[104:107], v242 offset:34816\n"
"v_cvt_pk_bf16_f32 v185, v186, v187\n"
"v_mfma_f32_16x16x32_bf16 v[128:131], a[116:119], a[152:155], v[128:131]\n"
"s_add_u32 s82, s82, 8192\n"
"buffer_load_dwordx4 a[244:247], v240, s[24:27], s82 offen sc1\n"
"v_mfma_f32_16x16x32_bf16 v[136:139], a[120:123], a[144:147], v[136:139]\n"
"ds_read_b128 a[108:111], v242 offset:39936\n"
"v_cvt_pk_bf16_f32 v186, v192, v193\n"
"v_mfma_f32_16x16x32_bf16 v[136:139], a[124:127], a[152:155], v[136:139]\n"
"s_add_u32 s82, s82, 8192\n"
"v_mfma_f32_16x16x32_bf16 v[144:147], a[128:131], a[144:147], v[144:147]\n"
"buffer_load_dwordx4 a[248:251], v240, s[24:27], s82 offen sc1\n"
"v_cvt_pk_bf16_f32 v187, v194, v195\n"
"v_mfma_f32_16x16x32_bf16 v[144:147], a[132:135], a[152:155], v[144:147]\n"
"v_mfma_f32_16x16x32_bf16 v[152:155], a[136:139], a[144:147], v[152:155]\n"
"s_add_u32 s82, s82, 8192\n"
"buffer_store_dwordx4 v[184:187], v255, s[36:39], s85 offen offset:1024 sc1\n"
"v_mfma_f32_16x16x32_bf16 v[152:155], a[140:143], a[152:155], v[152:155]\n"
"buffer_load_dwordx4 a[252:255], v240, s[24:27], s82 offen sc1\n"
"v_mfma_f32_16x16x32_bf16 v[160:163], a[0:3], a[144:147], v[160:163]\n"
"v_cvt_pk_bf16_f32 v200, v200, v201\n"
"v_mfma_f32_16x16x32_bf16 v[160:163], a[4:7], a[152:155], v[160:163]\n"
"s_waitcnt vmcnt(14) lgkmcnt(0)\n"
"s_add_u32 s82, s82, 8192\n"
"v_mfma_f32_16x16x32_bf16 v[0:3], a[32:35], a[160:163], 0\n"
"s_nop 0\n"
"s_barrier\n"
"ds_read_b128 a[112:115], v242 offset:40960\n"
"ds_read_b128 a[116:119], v242 offset:46080\n"
"buffer_load_dwordx4 a[144:147], v240, s[24:27], s82 offen sc1\n"
"v_mfma_f32_16x16x32_bf16 v[0:3], a[36:39], a[164:167], v[0:3]\n"
"s_add_u32 s82, s82, 8192\n"
"ds_read_b128 a[120:123], v242 offset:51200\n"
"v_cvt_pk_bf16_f32 v201, v202, v203\n"
"v_mfma_f32_16x16x32_bf16 v[0:3], a[40:43], a[168:171], v[0:3]\n"
"ds_read_b128 a[124:127], v242 offset:56320\n"
"buffer_load_dwordx4 a[148:151], v240, s[24:27], s82 offen sc1\n"
"buffer_store_dwordx2 v[200:201], v237, s[36:39], s85 offen offset:2048 sc1\n"
"v_mfma_f32_16x16x32_bf16 v[0:3], a[44:47], a[172:175], v[0:3]\n"
"ds_read_b128 a[128:131], v242 offset:41984\n"
"v_mfma_f32_16x16x32_bf16 v[4:7], a[48:51], a[160:163], 0\n"
"s_add_u32 s82, s82, 8192\n"
"ds_read_b128 a[132:135], v242 offset:47104\n"
"ds_read_b128 a[136:139], v242 offset:52224\n"
"v_cvt_pk_bf16_f32 v132, v132, v133\n"
"v_mfma_f32_16x16x32_bf16 v[4:7], a[52:55], a[164:167], v[4:7]\n"
"ds_read_b128 a[140:143], v242 offset:57344\n"
"buffer_load_dwordx4 a[152:155], v240, s[24:27], s82 offen sc1\n"
"v_mfma_f32_16x16x32_bf16 v[4:7], a[56:59], a[168:171], v[4:7]\n"
"ds_read_b128 a[0:3], v242 offset:43008\n"
"v_cvt_pk_bf16_f32 v133, v134, v135\n"
"v_mfma_f32_16x16x32_bf16 v[4:7], a[60:63], a[172:175], v[4:7]\n"
"s_add_u32 s82, s82, 8192\n"
"ds_read_b128 a[4:7], v242 offset:48128\n"
"buffer_load_dwordx4 a[156:159], v240, s[24:27], s82 offen sc1\n"
"v_mfma_f32_16x16x32_bf16 v[8:11], a[64:67], a[160:163], 0\n"
"ds_read_b128 a[8:11], v242 offset:53248\n"
"ds_read_b128 a[12:15], v242 offset:58368\n"
"v_cvt_pk_bf16_f32 v134, v140, v141\n"
"v_mfma_f32_16x16x32_bf16 v[8:11], a[68:71], a[164:167], v[8:11]\n"
"s_add_u32 s82, s82, 8192\n"
"ds_read_b128 a[16:19], v242 offset:44032\n"
"v_mfma_f32_16x16x32_bf16 v[8:11], a[72:75], a[168:171], v[8:11]\n"
"ds_read_b128 a[20:23], v242 offset:49152\n"
"v_cvt_pk_bf16_f32 v135, v142, v143\n"
"v_mfma_f32_16x16x32_bf16 v[8:11], a[76:79], a[172:175], v[8:11]\n"
"ds_read_b128 a[24:27], v242 offset:54272\n"
"v_mfma_f32_16x16x32_bf16 v[12:15], a[80:83], a[160:163], 0\n"
"ds_read_b128 a[28:31], v242 offset:59392\n"
"ds_read_b128 a[208:211], v242 offset:45056\n"
"buffer_store_dwordx4 v[132:135], v255, s[36:39], s77 offen offset:0 sc1\n"
"v_mfma_f32_16x16x32_bf16 v[12:15], a[84:87], a[164:167], v[12:15]\n"
"ds_read_b128 a[212:215], v242 offset:50176\n"
"v_mfma_f32_16x16x32_bf16 v[12:15], a[88:91], a[168:171], v[12:15]\n"
"ds_read_b128 a[216:219], v242 offset:55296\n"
"v_cvt_pk_bf16_f32 v148, v148, v149\n"
"v_mfma_f32_16x16x32_bf16 v[12:15], a[92:95], a[172:175], v[12:15]\n"
"ds_read_b128 a[220:223], v242 offset:60416\n"
"v_mfma_f32_16x16x32_bf16 v[16:19], a[96:99], a[160:163], 0\n"
"v_cvt_pk_bf16_f32 v149, v150, v151\n"
"v_mfma_f32_16x16x32_bf16 v[16:19], a[100:103], a[164:167], v[16:19]\n"
"v_mfma_f32_16x16x32_bf16 v[16:19], a[104:107], a[168:171], v[16:19]\n"
"v_cvt_pk_bf16_f32 v150, v156, v157\n"
"v_mfma_f32_16x16x32_bf16 v[16:19], a[108:111], a[172:175], v[16:19]\n"
"s_waitcnt vmcnt(16) lgkmcnt(0)\n"
"v_mfma_f32_16x16x32_bf16 v[0:3], a[112:115], a[176:179], v[0:3]\n"
"s_nop 0\n"
"s_barrier\n"
"ds_read_b128 a[32:35], v255 offset:20480\n"
"ds_read_b128 a[36:39], v255 offset:25600\n"
"buffer_load_dwordx4 a[160:163], v240, s[24:27], s82 offen sc1\n"
"v_mfma_f32_16x16x32_bf16 v[0:3], a[116:119], a[180:183], v[0:3]\n"
"s_add_u32 s82, s82, 8192\n"
"ds_read_b128 a[40:43], v255 offset:30720\n"
"v_cvt_pk_bf16_f32 v151, v158, v159\n"
"v_mfma_f32_16x16x32_bf16 v[0:3], a[120:123], a[184:187], v[0:3]\n"
"ds_read_b128 a[44:47], v255 offset:35840\n"
"buffer_load_dwordx4 a[164:167], v240, s[24:27], s82 offen sc1\n"
"buffer_store_dwordx4 v[148:151], v255, s[36:39], s77 offen offset:1024 sc1\n"
"v_mfma_f32_16x16x32_bf16 v[0:3], a[124:127], a[188:191], v[0:3]\n"
"s_add_u32 s82, s82, 8192\n"
"ds_read_b128 a[48:51], v255 offset:21504\n"
"v_mfma_f32_16x16x32_bf16 v[4:7], a[128:131], a[176:179], v[4:7]\n"
"ds_read_b128 a[52:55], v255 offset:26624\n"
"ds_read_b128 a[56:59], v255 offset:31744\n"
"buffer_load_dwordx4 a[168:171], v240, s[24:27], s82 offen sc1\n"
"v_cvt_pk_bf16_f32 v164, v164, v165\n"
"v_mfma_f32_16x16x32_bf16 v[4:7], a[132:135], a[180:183], v[4:7]\n"
"s_add_u32 s82, s82, 8192\n"
"ds_read_b128 a[60:63], v255 offset:36864\n"
"v_mfma_f32_16x16x32_bf16 v[4:7], a[136:139], a[184:187], v[4:7]\n"
"ds_read_b128 a[64:67], v255 offset:22528\n"
"buffer_load_dwordx4 a[172:175], v240, s[24:27], s82 offen sc1\n"
"v_cvt_pk_bf16_f32 v165, v166, v167\n"
"v_mfma_f32_16x16x32_bf16 v[4:7], a[140:143], a[188:191], v[4:7]\n"
"s_add_u32 s82, s82, 8192\n"
"s_add_u32 m0, s43, 61440\n"
"ds_read_b128 a[68:71], v255 offset:27648\n"
"v_mfma_f32_16x16x32_bf16 v[8:11], a[0:3], a[176:179], v[8:11]\n"
"ds_read_b128 a[72:75], v255 offset:32768\n"
"ds_read_b128 a[76:79], v255 offset:37888\n"
"buffer_load_dwordx4 v255, s[28:31], s83 offen offset:0 sc1 lds\n"
"buffer_store_dwordx2 v[164:165], v237, s[36:39], s77 offen offset:2048 sc1\n"
"v_mfma_f32_16x16x32_bf16 v[8:11], a[4:7], a[180:183], v[8:11]\n"
"ds_read_b128 a[80:83], v255 offset:23552\n"
"v_mfma_f32_16x16x32_bf16 v[8:11], a[8:11], a[184:187], v[8:11]\n"
"ds_read_b128 a[84:87], v255 offset:28672\n"
"buffer_load_dwordx4 v255, s[28:31], s83 offen offset:1024 sc1 lds\n"
"v_mfma_f32_16x16x32_bf16 v[8:11], a[12:15], a[188:191], v[8:11]\n"
"ds_read_b128 a[88:91], v255 offset:33792\n"
"v_cvt_pk_bf16_f32 v172, v172, v173\n"
"v_mfma_f32_16x16x32_bf16 v[12:15], a[16:19], a[176:179], v[12:15]\n"
"ds_read_b128 a[92:95], v255 offset:38912\n"
"ds_read_b128 a[96:99], v255 offset:24576\n"
"buffer_load_dwordx4 v255, s[28:31], s83 offen offset:2048 sc1 lds\n"
"v_mfma_f32_16x16x32_bf16 v[12:15], a[20:23], a[180:183], v[12:15]\n"
"ds_read_b128 a[100:103], v255 offset:29696\n"
"v_cvt_pk_bf16_f32 v173, v174, v175\n"
"v_mfma_f32_16x16x32_bf16 v[12:15], a[24:27], a[184:187], v[12:15]\n"
"ds_read_b128 a[104:107], v255 offset:34816\n"
"buffer_load_dwordx4 v255, s[28:31], s83 offen offset:3072 sc1 lds\n"
"v_mfma_f32_16x16x32_bf16 v[12:15], a[28:31], a[188:191], v[12:15]\n"
"s_add_u32 m0, s43, 65536\n"
"ds_read_b128 a[108:111], v255 offset:39936\n"
"v_cvt_pk_bf16_f32 v174, v180, v181\n"
"v_mfma_f32_16x16x32_bf16 v[16:19], a[208:211], a[176:179], v[16:19]\n"
"buffer_load_dwordx4 v236, s[28:31], s83 offen sc1 lds\n"
"v_mfma_f32_16x16x32_bf16 v[16:19], a[212:215], a[180:183], v[16:19]\n"
"s_add_u32 s83, s83, 163840\n"
"v_cvt_pk_bf16_f32 v175, v182, v183\n"
"v_mfma_f32_16x16x32_bf16 v[16:19], a[216:219], a[184:187], v[16:19]\n"
"v_mfma_f32_16x16x32_bf16 v[16:19], a[220:223], a[188:191], v[16:19]\n"
"s_waitcnt vmcnt(22) lgkmcnt(0)\n"
"buffer_store_dwordx4 v[172:175], v255, s[36:39], s78 offen offset:0 sc1\n"
"v_mfma_f32_16x16x32_bf16 v[0:3], a[32:35], a[192:195], v[0:3]\n"
"s_nop 0\n"
"s_barrier\n"
"ds_read_b128 a[112:115], v255 offset:40960\n"
"ds_read_b128 a[116:119], v255 offset:46080\n"
"buffer_load_dwordx4 a[176:179], v240, s[24:27], s82 offen sc1\n"
"v_mfma_f32_16x16x32_bf16 v[0:3], a[36:39], a[196:199], v[0:3]\n"
"s_add_u32 s82, s82, 8192\n"
"ds_read_b128 a[120:123], v255 offset:51200\n"
"v_cvt_pk_bf16_f32 v188, v188, v189\n"
"v_mfma_f32_16x16x32_bf16 v[0:3], a[40:43], a[200:203], v[0:3]\n"
"ds_read_b128 a[124:127], v255 offset:56320\n"
"buffer_load_dwordx4 a[180:183], v240, s[24:27], s82 offen sc1\n"
"v_mfma_f32_16x16x32_bf16 v[0:3], a[44:47], a[204:207], v[0:3]\n"
"s_add_u32 s82, s82, 8192\n"
"ds_read_b128 a[128:131], v255 offset:41984\n"
"v_cvt_pk_bf16_f32 v189, v190, v191\n"
"v_mfma_f32_16x16x32_bf16 v[4:7], a[48:51], a[192:195], v[4:7]\n"
"ds_read_b128 a[132:135], v255 offset:47104\n"
"ds_read_b128 a[136:139], v255 offset:52224\n"
"buffer_load_dwordx4 a[184:187], v240, s[24:27], s82 offen sc1\n"
"v_mfma_f32_16x16x32_bf16 v[4:7], a[52:55], a[196:199], v[4:7]\n"
"s_add_u32 s82, s82, 8192\n"
"ds_read_b128 a[140:143], v255 offset:57344\n"
"v_cvt_pk_bf16_f32 v190, v196, v197\n"
"v_mfma_f32_16x16x32_bf16 v[4:7], a[56:59], a[200:203], v[4:7]\n"
"ds_read_b128 a[0:3], v255 offset:43008\n"
"buffer_load_dwordx4 a[188:191], v240, s[24:27], s82 offen sc1\n"
"v_mfma_f32_16x16x32_bf16 v[4:7], a[60:63], a[204:207], v[4:7]\n"
"s_add_u32 s82, s82, 8192\n"
"s_add_u32 m0, s43, 81920\n"
"ds_read_b128 a[4:7], v255 offset:48128\n"
"v_cvt_pk_bf16_f32 v191, v198, v199\n"
"v_mfma_f32_16x16x32_bf16 v[8:11], a[64:67], a[192:195], v[8:11]\n"
"ds_read_b128 a[8:11], v255 offset:53248\n"
"ds_read_b128 a[12:15], v255 offset:58368\n"
"buffer_load_dwordx4 v255, s[28:31], s83 offen offset:0 sc1 lds\n"
"v_mfma_f32_16x16x32_bf16 v[8:11], a[68:71], a[196:199], v[8:11]\n"
"ds_read_b128 a[16:19], v255 offset:44032\n"
"buffer_store_dwordx4 v[188:191], v255, s[36:39], s78 offen offset:1024 sc1\n"
"v_mfma_f32_16x16x32_bf16 v[8:11], a[72:75], a[200:203], v[8:11]\n"
"ds_read_b128 a[20:23], v255 offset:49152\n"
"buffer_load_dwordx4 v255, s[28:31], s83 offen offset:1024 sc1 lds\n"
"v_mfma_f32_16x16x32_bf16 v[8:11], a[76:79], a[204:207], v[8:11]\n"
"ds_read_b128 a[24:27], v255 offset:54272\n"
"v_cvt_pk_bf16_f32 v204, v204, v205\n"
"v_mfma_f32_16x16x32_bf16 v[12:15], a[80:83], a[192:195], v[12:15]\n"
"ds_read_b128 a[28:31], v255 offset:59392\n"
"ds_read_b128 a[208:211], v255 offset:45056\n"
"buffer_load_dwordx4 v255, s[28:31], s83 offen offset:2048 sc1 lds\n"
"v_mfma_f32_16x16x32_bf16 v[12:15], a[84:87], a[196:199], v[12:15]\n"
"ds_read_b128 a[212:215], v255 offset:50176\n"
"v_cvt_pk_bf16_f32 v205, v206, v207\n"
"v_mfma_f32_16x16x32_bf16 v[12:15], a[88:91], a[200:203], v[12:15]\n"
"ds_read_b128 a[216:219], v255 offset:55296\n"
"buffer_load_dwordx4 v255, s[28:31], s83 offen offset:3072 sc1 lds\n"
"v_mfma_f32_16x16x32_bf16 v[12:15], a[92:95], a[204:207], v[12:15]\n"
"s_add_u32 m0, s43, 86016\n"
"ds_read_b128 a[220:223], v255 offset:60416\n"
"buffer_store_dwordx2 v[204:205], v237, s[36:39], s78 offen offset:2048 sc1\n"
"v_mfma_f32_16x16x32_bf16 v[16:19], a[96:99], a[192:195], v[16:19]\n"
"buffer_load_dwordx4 v236, s[28:31], s83 offen sc1 lds\n"
"v_mfma_f32_16x16x32_bf16 v[16:19], a[100:103], a[196:199], v[16:19]\n"
"s_add_u32 s83, s83, 163840\n"
"v_mfma_f32_16x16x32_bf16 v[16:19], a[104:107], a[200:203], v[16:19]\n"
"v_mfma_f32_16x16x32_bf16 v[16:19], a[108:111], a[204:207], v[16:19]\n"
"s_waitcnt vmcnt(29) lgkmcnt(0)\n"
"v_mfma_f32_16x16x32_bf16 v[0:3], a[112:115], a[240:243], v[0:3]\n"
"s_nop 0\n"
"s_barrier\n"
"ds_read_b128 a[32:35], v243 offset:0\n"
"ds_read_b128 a[36:39], v243 offset:5120\n"
"buffer_load_dwordx4 a[192:195], v240, s[24:27], s82 offen sc1\n"
"v_mfma_f32_16x16x32_bf16 v[0:3], a[116:119], a[244:247], v[0:3]\n"
"s_add_u32 s82, s82, 8192\n"
"ds_read_b128 a[40:43], v243 offset:10240\n"
"v_mfma_f32_16x16x32_bf16 v[0:3], a[120:123], a[248:251], v[0:3]\n"
"ds_read_b128 a[44:47], v243 offset:15360\n"
"buffer_load_dwordx4 a[196:199], v240, s[24:27], s82 offen sc1\n"
"v_mfma_f32_16x16x32_bf16 v[0:3], a[124:127], a[252:255], v[0:3]\n"
"s_add_u32 s82, s82, 8192\n"
"ds_read_b128 a[48:51], v243 offset:1024\n"
"v_mfma_f32_16x16x32_bf16 v[4:7], a[128:131], a[240:243], v[4:7]\n"
"ds_read_b128 a[52:55], v243 offset:6144\n"
"ds_read_b128 a[56:59], v243 offset:11264\n"
"buffer_load_dwordx4 a[200:203], v240, s[24:27], s82 offen sc1\n"
"v_mfma_f32_16x16x32_bf16 v[4:7], a[132:135], a[244:247], v[4:7]\n"
"s_add_u32 s82, s82, 8192\n"
"ds_read_b128 a[60:63], v243 offset:16384\n"
"v_mfma_f32_16x16x32_bf16 v[4:7], a[136:139], a[248:251], v[4:7]\n"
".L_pad15:\n"
"s_cbranch_execz .L_padret1\n"
"ds_read_b128 a[64:67], v243 offset:2048\n"
"buffer_load_dwordx4 a[204:207], v240, s[24:27], s82 offen sc1\n"
"v_mfma_f32_16x16x32_bf16 v[4:7], a[140:143], a[252:255], v[4:7]\n"
"s_add_u32 s82, s82, 8192\n"
"s_add_u32 m0, s43, 102400\n"
"ds_read_b128 a[68:71], v243 offset:7168\n"
"v_mfma_f32_16x16x32_bf16 v[8:11], a[0:3], a[240:243], v[8:11]\n"
"ds_read_b128 a[72:75], v243 offset:12288\n"
"ds_read_b128 a[76:79], v243 offset:17408\n"
"buffer_load_dwordx4 v255, s[28:31], s83 offen offset:0 sc1 lds\n"
"v_mfma_f32_16x16x32_bf16 v[8:11], a[4:7], a[244:247], v[8:11]\n"
"ds_read_b128 a[80:83], v243 offset:3072\n"
"v_mfma_f32_16x16x32_bf16 v[8:11], a[8:11], a[248:251], v[8:11]\n"
"ds_read_b128 a[84:87], v243 offset:8192\n"
"buffer_load_dwordx4 v255, s[28:31], s83 offen offset:1024 sc1 lds\n"
"v_mfma_f32_16x16x32_bf16 v[8:11], a[12:15], a[252:255], v[8:11]\n"
"ds_read_b128 a[88:91], v243 offset:13312\n"
"v_mfma_f32_16x16x32_bf16 v[12:15], a[16:19], a[240:243], v[12:15]\n"
"ds_read_b128 a[92:95], v243 offset:18432\n"
"ds_read_b128 a[96:99], v243 offset:4096\n"
"buffer_load_dwordx4 v255, s[28:31], s83 offen offset:2048 sc1 lds\n"
"v_mfma_f32_16x16x32_bf16 v[12:15], a[20:23], a[244:247], v[12:15]\n"
"ds_read_b128 a[100:103], v243 offset:9216\n"
"v_mfma_f32_16x16x32_bf16 v[12:15], a[24:27], a[248:251], v[12:15]\n"
"ds_read_b128 a[104:107], v243 offset:14336\n"
"buffer_load_dwordx4 v255, s[28:31], s83 offen offset:3072 sc1 lds\n"
"v_mfma_f32_16x16x32_bf16 v[12:15], a[28:31], a[252:255], v[12:15]\n"
"ds_read_b128 a[108:111], v243 offset:19456\n"
"v_mfma_f32_16x16x32_bf16 v[16:19], a[208:211], a[240:243], v[16:19]\n"
"s_add_u32 m0, s43, 106496\n"
"v_mfma_f32_16x16x32_bf16 v[16:19], a[212:215], a[244:247], v[16:19]\n"
"buffer_load_dwordx4 v236, s[28:31], s83 offen sc1 lds\n"
"v_mfma_f32_16x16x32_bf16 v[16:19], a[216:219], a[248:251], v[16:19]\n"
"s_add_u32 s83, s83, 163840\n"
"v_mfma_f32_16x16x32_bf16 v[16:19], a[220:223], a[252:255], v[16:19]\n"
"s_waitcnt vmcnt(33) lgkmcnt(0)\n"
"v_mfma_f32_16x16x32_bf16 v[0:3], a[32:35], a[144:147], v[0:3]\n"
"s_nop 0\n"
"s_barrier\n"
"ds_read_b128 a[112:115], v255 offset:0\n"
"ds_read_b128 a[116:119], v255 offset:5120\n"
"buffer_load_dwordx4 a[240:243], v240, s[24:27], s82 offen sc1\n"
"v_mfma_f32_16x16x32_bf16 v[0:3], a[36:39], a[148:151], v[0:3]\n"
"s_add_u32 s82, s82, 8192\n"
"ds_read_b128 a[120:123], v255 offset:10240\n"
"v_mfma_f32_16x16x32_bf16 v[0:3], a[40:43], a[152:155], v[0:3]\n"
"ds_read_b128 a[124:127], v255 offset:15360\n"
"buffer_load_dwordx4 a[244:247], v240, s[24:27], s82 offen sc1\n"
"v_mfma_f32_16x16x32_bf16 v[0:3], a[44:47], a[156:159], v[0:3]\n"
"ds_read_b128 a[128:131], v255 offset:1024\n"
"v_mfma_f32_16x16x32_bf16 v[4:7], a[48:51], a[144:147], v[4:7]\n"
"s_add_u32 s82, s82, 8192\n"
"ds_read_b128 a[132:135], v255 offset:6144\n"
"ds_read_b128 a[136:139], v255 offset:11264\n"
"buffer_load_dwordx4 a[248:251], v240, s[24:27], s82 offen sc1\n"
"v_mfma_f32_16x16x32_bf16 v[4:7], a[52:55], a[148:151], v[4:7]\n"
"ds_read_b128 a[140:143], v255 offset:16384\n"
"v_mfma_f32_16x16x32_bf16 v[4:7], a[56:59], a[152:155], v[4:7]\n"
"ds_read_b128 a[0:3], v255 offset:2048\n"
"v_mfma_f32_16x16x32_bf16 v[4:7], a[60:63], a[156:159], v[4:7]\n"
"s_add_u32 s82, s82, 8192\n"
"ds_read_b128 a[4:7], v255 offset:7168\n"
"buffer_load_dwordx4 a[252:255], v240, s[24:27], s82 offen sc1\n"
"v_mfma_f32_16x16x32_bf16 v[8:11], a[64:67], a[144:147], v[8:11]\n"
"ds_read_b128 a[8:11], v255 offset:12288\n"
"ds_read_b128 a[12:15], v255 offset:17408\n"
"v_mfma_f32_16x16x32_bf16 v[8:11], a[68:71], a[148:151], v[8:11]\n"
"s_add_u32 s82, s82, 8192\n"
"ds_read_b128 a[16:19], v255 offset:3072\n"
"v_mfma_f32_16x16x32_bf16 v[8:11], a[72:75], a[152:155], v[8:11]\n"
"s_add_u32 m0, s43, 20480\n"
"ds_read_b128 a[20:23], v255 offset:8192\n"
"v_mfma_f32_16x16x32_bf16 v[8:11], a[76:79], a[156:159], v[8:11]\n"
"ds_read_b128 a[24:27], v255 offset:13312\n"
"buffer_load_dwordx4 v255, s[28:31], s83 offen offset:0 sc1 lds\n"
"v_mfma_f32_16x16x32_bf16 v[12:15], a[80:83], a[144:147], v[12:15]\n"
"ds_read_b128 a[28:31], v255 offset:18432\n"
"ds_read_b128 a[208:211], v255 offset:4096\n"
"v_mfma_f32_16x16x32_bf16 v[12:15], a[84:87], a[148:151], v[12:15]\n"
"ds_read_b128 a[212:215], v255 offset:9216\n"
"buffer_load_dwordx4 v255, s[28:31], s83 offen offset:1024 sc1 lds\n"
"v_mfma_f32_16x16x32_bf16 v[12:15], a[88:91], a[152:155], v[12:15]\n"
"ds_read_b128 a[216:219], v255 offset:14336\n"
"v_mfma_f32_16x16x32_bf16 v[12:15], a[92:95], a[156:159], v[12:15]\n"
"ds_read_b128 a[220:223], v255 offset:19456\n"
"buffer_load_dwordx4 v255, s[28:31], s83 offen offset:2048 sc1 lds\n"
"v_mfma_f32_16x16x32_bf16 v[16:19], a[96:99], a[144:147], v[16:19]\n"
"v_mfma_f32_16x16x32_bf16 v[16:19], a[100:103], a[148:151], v[16:19]\n"
"buffer_load_dwordx4 v255, s[28:31], s83 offen offset:3072 sc1 lds\n"
"v_mfma_f32_16x16x32_bf16 v[16:19], a[104:107], a[152:155], v[16:19]\n"
"v_mfma_f32_16x16x32_bf16 v[16:19], a[108:111], a[156:159], v[16:19]\n"
"s_waitcnt vmcnt(35) lgkmcnt(0)\n"
"s_add_u32 m0, s43, 24576\n"
"v_mfma_f32_16x16x32_bf16 v[0:3], a[112:115], a[160:163], v[0:3]\n"
"s_waitcnt vmcnt(29)\n"
"s_barrier\n"
"ds_read_b128 a[32:35], v242 offset:0\n"
"ds_read_b128 a[36:39], v242 offset:5120\n"
"buffer_load_dwordx4 v236, s[28:31], s83 offen sc1 lds\n"
"v_mfma_f32_16x16x32_bf16 v[0:3], a[116:119], a[164:167], v[0:3]\n"
"s_add_u32 s83, s83, 163840\n"
"ds_read_b128 a[40:43], v242 offset:10240\n"
"v_mfma_f32_16x16x32_bf16 v[0:3], a[120:123], a[168:171], v[0:3]\n"
"ds_read_b128 a[44:47], v242 offset:15360\n"
"buffer_load_dwordx4 a[144:147], v240, s[24:27], s82 offen sc1\n"
"v_mfma_f32_16x16x32_bf16 v[0:3], a[124:127], a[172:175], v[0:3]\n"
"ds_read_b128 a[48:51], v242 offset:1024\n"
"v_mfma_f32_16x16x32_bf16 v[4:7], a[128:131], a[160:163], v[4:7]\n"
"s_add_u32 s82, s82, 8192\n"
"ds_read_b128 a[52:55], v242 offset:6144\n"
"ds_read_b128 a[56:59], v242 offset:11264\n"
"v_mfma_f32_16x16x32_bf16 v[4:7], a[132:135], a[164:167], v[4:7]\n"
"ds_read_b128 a[60:63], v242 offset:16384\n"
"buffer_load_dwordx4 a[148:151], v240, s[24:27], s82 offen sc1\n"
"v_mfma_f32_16x16x32_bf16 v[4:7], a[136:139], a[168:171], v[4:7]\n"
"ds_read_b128 a[64:67], v242 offset:2048\n"
"v_mfma_f32_16x16x32_bf16 v[4:7], a[140:143], a[172:175], v[4:7]\n"
"s_add_u32 s82, s82, 8192\n"
"ds_read_b128 a[68:71], v242 offset:7168\n"
"buffer_load_dwordx4 a[152:155], v240, s[24:27], s82 offen sc1\n"
"v_mfma_f32_16x16x32_bf16 v[8:11], a[0:3], a[160:163], v[8:11]\n"
"ds_read_b128 a[72:75], v242 offset:12288\n"
"ds_read_b128 a[76:79], v242 offset:17408\n"
"v_mfma_f32_16x16x32_bf16 v[8:11], a[4:7], a[164:167], v[8:11]\n"
"ds_read_b128 a[80:83], v242 offset:3072\n"
"v_mfma_f32_16x16x32_bf16 v[8:11], a[8:11], a[168:171], v[8:11]\n"
"s_add_u32 s82, s82, 8192\n"
"ds_read_b128 a[84:87], v242 offset:8192\n"
"buffer_load_dwordx4 a[156:159], v240, s[24:27], s82 offen sc1\n"
"v_mfma_f32_16x16x32_bf16 v[8:11], a[12:15], a[172:175], v[8:11]\n"
"ds_read_b128 a[88:91], v242 offset:13312\n"
"v_mfma_f32_16x16x32_bf16 v[12:15], a[16:19], a[160:163], v[12:15]\n"
"s_add_u32 s82, s82, 8192\n"
"ds_read_b128 a[92:95], v242 offset:18432\n"
"ds_read_b128 a[96:99], v242 offset:4096\n"
"v_mfma_f32_16x16x32_bf16 v[12:15], a[20:23], a[164:167], v[12:15]\n"
"s_add_u32 m0, s43, 40960\n"
"ds_read_b128 a[100:103], v242 offset:9216\n"
"v_mfma_f32_16x16x32_bf16 v[12:15], a[24:27], a[168:171], v[12:15]\n"
"ds_read_b128 a[104:107], v242 offset:14336\n"
"buffer_load_dwordx4 v255, s[28:31], s83 offen offset:0 sc1 lds\n"
"v_mfma_f32_16x16x32_bf16 v[12:15], a[28:31], a[172:175], v[12:15]\n"
"ds_read_b128 a[108:111], v242 offset:19456\n"
"v_mfma_f32_16x16x32_bf16 v[16:19], a[208:211], a[160:163], v[16:19]\n"
"buffer_load_dwordx4 v255, s[28:31], s83 offen offset:1024 sc1 lds\n"
"v_mfma_f32_16x16x32_bf16 v[16:19], a[212:215], a[164:167], v[16:19]\n"
"v_mfma_f32_16x16x32_bf16 v[16:19], a[216:219], a[168:171], v[16:19]\n"
"buffer_load_dwordx4 v255, s[28:31], s83 offen offset:2048 sc1 lds\n"
"v_mfma_f32_16x16x32_bf16 v[16:19], a[220:223], a[172:175], v[16:19]\n"
"s_waitcnt vmcnt(32) lgkmcnt(0)\n"
"v_mfma_f32_16x16x32_bf16 v[0:3], a[32:35], a[176:179], v[0:3]\n"
"s_waitcnt vmcnt(25)\n"
"s_barrier\n"
"ds_read_b128 a[112:115], v242 offset:20480\n"
"ds_read_b128 a[116:119], v242 offset:25600\n"
"buffer_load_dwordx4 v255, s[28:31], s83 offen offset:3072 sc1 lds\n"
"v_mfma_f32_16x16x32_bf16 v[0:3], a[36:39], a[180:183], v[0:3]\n"
"s_nop 0\n"
"ds_read_b128 a[120:123], v242 offset:30720\n"
"buffer_store_dword v218, v252, s[36:39], s80 offen sc1\n"
"v_mfma_f32_16x16x32_bf16 v[0:3], a[40:43], a[184:187], v[0:3]\n"
"s_add_u32 m0, s43, 45056\n"
"ds_read_b128 a[124:127], v242 offset:35840\n"
"v_mfma_f32_16x16x32_bf16 v[0:3], a[44:47], a[188:191], v[0:3]\n"
"ds_read_b128 a[128:131], v242 offset:21504\n"
"buffer_load_dwordx4 v236, s[28:31], s83 offen sc1 lds\n"
"v_mfma_f32_16x16x32_bf16 v[4:7], a[48:51], a[176:179], v[4:7]\n"
"ds_read_b128 a[132:135], v242 offset:26624\n"
"ds_read_b128 a[136:139], v242 offset:31744\n"
"v_mfma_f32_16x16x32_bf16 v[4:7], a[52:55], a[180:183], v[4:7]\n"
"s_add_u32 s83, s83, 163840\n"
"ds_read_b128 a[140:143], v242 offset:36864\n"
"buffer_load_dwordx4 a[160:163], v240, s[24:27], s82 offen sc1\n"
"v_mfma_f32_16x16x32_bf16 v[4:7], a[56:59], a[184:187], v[4:7]\n"
"ds_read_b128 a[0:3], v242 offset:22528\n"
"v_mfma_f32_16x16x32_bf16 v[4:7], a[60:63], a[188:191], v[4:7]\n"
"s_add_u32 s82, s82, 8192\n"
"ds_read_b128 a[4:7], v242 offset:27648\n"
"v_mfma_f32_16x16x32_bf16 v[8:11], a[64:67], a[176:179], v[8:11]\n"
"ds_read_b128 a[8:11], v242 offset:32768\n"
"ds_read_b128 a[12:15], v242 offset:37888\n"
"buffer_load_dwordx4 a[164:167], v240, s[24:27], s82 offen sc1\n"
"v_mfma_f32_16x16x32_bf16 v[8:11], a[68:71], a[180:183], v[8:11]\n"
"ds_read_b128 a[16:19], v242 offset:23552\n"
"v_mfma_f32_16x16x32_bf16 v[8:11], a[72:75], a[184:187], v[8:11]\n"
"s_add_u32 s82, s82, 8192\n"
"ds_read_b128 a[20:23], v242 offset:28672\n"
"buffer_load_dwordx4 a[168:171], v240, s[24:27], s82 offen sc1\n"
"v_mfma_f32_16x16x32_bf16 v[8:11], a[76:79], a[188:191], v[8:11]\n"
"ds_read_b128 a[24:27], v242 offset:33792\n"
"v_mfma_f32_16x16x32_bf16 v[12:15], a[80:83], a[176:179], v[12:15]\n"
"ds_read_b128 a[28:31], v242 offset:38912\n"
"ds_read_b128 a[208:211], v242 offset:24576\n"
"v_mfma_f32_16x16x32_bf16 v[12:15], a[84:87], a[180:183], v[12:15]\n"
"s_add_u32 s82, s82, 8192\n"
"ds_read_b128 a[212:215], v242 offset:29696\n"
"buffer_load_dwordx4 a[172:175], v240, s[24:27], s82 offen sc1\n"
"v_mfma_f32_16x16x32_bf16 v[12:15], a[88:91], a[184:187], v[12:15]\n"
"ds_read_b128 a[216:219], v242 offset:34816\n"
"v_mfma_f32_16x16x32_bf16 v[12:15], a[92:95], a[188:191], v[12:15]\n"
"s_add_u32 s82, s82, 8192\n"
"ds_read_b128 a[220:223], v242 offset:39936\n"
"v_mfma_f32_16x16x32_bf16 v[16:19], a[96:99], a[176:179], v[16:19]\n"
"s_add_u32 m0, s43, 122880\n"
"v_mfma_f32_16x16x32_bf16 v[16:19], a[100:103], a[180:183], v[16:19]\n"
"buffer_load_dwordx4 v255, s[28:31], s83 offen offset:0 sc1 lds\n"
"v_mfma_f32_16x16x32_bf16 v[16:19], a[104:107], a[184:187], v[16:19]\n"
"v_mfma_f32_16x16x32_bf16 v[16:19], a[108:111], a[188:191], v[16:19]\n"
"s_waitcnt vmcnt(29) lgkmcnt(0)\n"
"buffer_load_dwordx4 v255, s[28:31], s83 offen offset:1024 sc1 lds\n"
"v_mfma_f32_16x16x32_bf16 v[0:3], a[112:115], a[192:195], v[0:3]\n"
"s_waitcnt vmcnt(25)\n"
"s_barrier\n"
"ds_read_b128 a[32:35], v242 offset:40960\n"
"ds_read_b128 a[36:39], v242 offset:46080\n"
"v_mfma_f32_16x16x32_bf16 v[0:3], a[116:119], a[196:199], v[0:3]\n"
"ds_read_b128 a[40:43], v242 offset:51200\n"
"buffer_load_dwordx4 v255, s[28:31], s83 offen offset:2048 sc1 lds\n"
"v_mfma_f32_16x16x32_bf16 v[0:3], a[120:123], a[200:203], v[0:3]\n"
"ds_read_b128 a[44:47], v242 offset:56320\n"
"v_mfma_f32_16x16x32_bf16 v[0:3], a[124:127], a[204:207], v[0:3]\n"
"ds_read_b128 a[48:51], v242 offset:41984\n"
"buffer_load_dwordx4 v255, s[28:31], s83 offen offset:3072 sc1 lds\n"
"v_mfma_f32_16x16x32_bf16 v[4:7], a[128:131], a[192:195], v[4:7]\n"
"ds_read_b128 a[52:55], v242 offset:47104\n"
"ds_read_b128 a[56:59], v242 offset:52224\n"
"v_mfma_f32_16x16x32_bf16 v[4:7], a[132:135], a[196:199], v[4:7]\n"
"s_add_u32 m0, s43, 126976\n"
"ds_read_b128 a[60:63], v242 offset:57344\n"
"v_mfma_f32_16x16x32_bf16 v[4:7], a[136:139], a[200:203], v[4:7]\n"
"ds_read_b128 a[64:67], v242 offset:43008\n"
"buffer_load_dwordx4 v236, s[28:31], s83 offen sc1 lds\n"
"v_mfma_f32_16x16x32_bf16 v[4:7], a[140:143], a[204:207], v[4:7]\n"
"ds_read_b128 a[68:71], v242 offset:48128\n"
"v_mfma_f32_16x16x32_bf16 v[8:11], a[0:3], a[192:195], v[8:11]\n"
"s_add_u32 s83, s83, 163840\n"
"ds_read_b128 a[72:75], v242 offset:53248\n"
"ds_read_b128 a[76:79], v242 offset:58368\n"
"buffer_load_dwordx4 a[176:179], v240, s[24:27], s82 offen sc1\n"
"v_mfma_f32_16x16x32_bf16 v[8:11], a[4:7], a[196:199], v[8:11]\n"
"ds_read_b128 a[80:83], v242 offset:44032\n"
"v_mfma_f32_16x16x32_bf16 v[8:11], a[8:11], a[200:203], v[8:11]\n"
"s_add_u32 s82, s82, 8192\n"
"ds_read_b128 a[84:87], v242 offset:49152\n"
"v_mfma_f32_16x16x32_bf16 v[8:11], a[12:15], a[204:207], v[8:11]\n"
"ds_read_b128 a[88:91], v242 offset:54272\n"
"buffer_load_dwordx4 a[180:183], v240, s[24:27], s82 offen sc1\n"
"v_mfma_f32_16x16x32_bf16 v[12:15], a[16:19], a[192:195], v[12:15]\n"
"ds_read_b128 a[92:95], v242 offset:59392\n"
"ds_read_b128 a[96:99], v242 offset:45056\n"
"v_mfma_f32_16x16x32_bf16 v[12:15], a[20:23], a[196:199], v[12:15]\n"
"s_add_u32 s82, s82, 8192\n"
"ds_read_b128 a[100:103], v242 offset:50176\n"
"buffer_load_dwordx4 a[184:187], v240, s[24:27], s82 offen sc1\n"
"v_mfma_f32_16x16x32_bf16 v[12:15], a[24:27], a[200:203], v[12:15]\n"
"ds_read_b128 a[104:107], v242 offset:55296\n"
"v_mfma_f32_16x16x32_bf16 v[12:15], a[28:31], a[204:207], v[12:15]\n"
"ds_read_b128 a[108:111], v242 offset:60416\n"
"v_mfma_f32_16x16x32_bf16 v[16:19], a[208:211], a[192:195], v[16:19]\n"
"s_add_u32 s82, s82, 8192\n"
"buffer_load_dwordx4 a[188:191], v240, s[24:27], s82 offen sc1\n"
"v_mfma_f32_16x16x32_bf16 v[16:19], a[212:215], a[196:199], v[16:19]\n"
"v_mfma_f32_16x16x32_bf16 v[16:19], a[216:219], a[200:203], v[16:19]\n"
"s_add_u32 s82, s82, 8192\n"
"v_mfma_f32_16x16x32_bf16 v[16:19], a[220:223], a[204:207], v[16:19]\n"
"s_waitcnt vmcnt(28) lgkmcnt(0)\n"
"s_add_u32 m0, s43, 0\n"
"v_mfma_f32_16x16x32_bf16 v[0:3], a[32:35], a[240:243], v[0:3]\n"
"s_waitcnt vmcnt(23)\n"
"s_barrier\n"
"ds_read_b128 a[112:115], v255 offset:20480\n"
"ds_read_b128 a[116:119], v255 offset:25600\n"
"buffer_load_dwordx4 v255, s[28:31], s83 offen offset:0 sc1 lds\n"
"v_mfma_f32_16x16x32_bf16 v[0:3], a[36:39], a[244:247], v[0:3]\n"
"ds_read_b128 a[120:123], v255 offset:30720\n"
"v_mfma_f32_16x16x32_bf16 v[0:3], a[40:43], a[248:251], v[0:3]\n"
"ds_read_b128 a[124:127], v255 offset:35840\n"
"buffer_load_dwordx4 v255, s[28:31], s83 offen offset:1024 sc1 lds\n"
"v_mfma_f32_16x16x32_bf16 v[0:3], a[44:47], a[252:255], v[0:3]\n"
"ds_read_b128 a[128:131], v255 offset:21504\n"
"v_mfma_f32_16x16x32_bf16 v[4:7], a[48:51], a[240:243], v[4:7]\n"
"ds_read_b128 a[132:135], v255 offset:26624\n"
"ds_read_b128 a[136:139], v255 offset:31744\n"
"buffer_load_dwordx4 v255, s[28:31], s83 offen offset:2048 sc1 lds\n"
"v_mfma_f32_16x16x32_bf16 v[4:7], a[52:55], a[244:247], v[4:7]\n"
"ds_read_b128 a[140:143], v255 offset:36864\n"
"v_mfma_f32_16x16x32_bf16 v[4:7], a[56:59], a[248:251], v[4:7]\n"
"ds_read_b128 a[0:3], v255 offset:22528\n"
"buffer_load_dwordx4 v255, s[28:31], s83 offen offset:3072 sc1 lds\n"
"v_mfma_f32_16x16x32_bf16 v[4:7], a[60:63], a[252:255], v[4:7]\n"
"ds_read_b128 a[4:7], v255 offset:27648\n"
"v_mfma_f32_16x16x32_bf16 v[8:11], a[64:67], a[240:243], v[8:11]\n"
"s_add_u32 m0, s43, 4096\n"
"ds_read_b128 a[8:11], v255 offset:32768\n"
"ds_read_b128 a[12:15], v255 offset:37888\n"
"v_mfma_f32_16x16x32_bf16 v[8:11], a[68:71], a[244:247], v[8:11]\n"
"ds_read_b128 a[16:19], v255 offset:23552\n"
"buffer_load_dwordx4 v236, s[28:31], s83 offen sc1 lds\n"
"v_mfma_f32_16x16x32_bf16 v[8:11], a[72:75], a[248:251], v[8:11]\n"
"ds_read_b128 a[20:23], v255 offset:28672\n"
"v_mfma_f32_16x16x32_bf16 v[8:11], a[76:79], a[252:255], v[8:11]\n"
"s_add_u32 s83, s83, 163840\n"
"ds_read_b128 a[24:27], v255 offset:33792\n"
"buffer_load_dwordx4 a[192:195], v240, s[24:27], s82 offen sc1\n"
"v_mfma_f32_16x16x32_bf16 v[12:15], a[80:83], a[240:243], v[12:15]\n"
"ds_read_b128 a[28:31], v255 offset:38912\n"
"ds_read_b128 a[208:211], v255 offset:24576\n"
"v_mfma_f32_16x16x32_bf16 v[12:15], a[84:87], a[244:247], v[12:15]\n"
"s_add_u32 s82, s82, 8192\n"
"ds_read_b128 a[212:215], v255 offset:29696\n"
"v_mfma_f32_16x16x32_bf16 v[12:15], a[88:91], a[248:251], v[12:15]\n"
"ds_read_b128 a[216:219], v255 offset:34816\n"
"buffer_load_dwordx4 a[196:199], v240, s[24:27], s82 offen sc1\n"
"v_mfma_f32_16x16x32_bf16 v[12:15], a[92:95], a[252:255], v[12:15]\n"
"ds_read_b128 a[220:223], v255 offset:39936\n"
"v_mfma_f32_16x16x32_bf16 v[16:19], a[96:99], a[240:243], v[16:19]\n"
"s_add_u32 s82, s82, 8192\n"
"buffer_load_dwordx4 a[200:203], v240, s[24:27], s82 offen sc1\n"
"v_mfma_f32_16x16x32_bf16 v[16:19], a[100:103], a[244:247], v[16:19]\n"
"v_mfma_f32_16x16x32_bf16 v[16:19], a[104:107], a[248:251], v[16:19]\n"
"v_mfma_f32_16x16x32_bf16 v[16:19], a[108:111], a[252:255], v[16:19]\n"
"s_waitcnt vmcnt(27) lgkmcnt(0)\n"
"s_add_u32 s82, s82, 8192\n"
"buffer_load_dwordx4 a[204:207], v240, s[24:27], s82 offen sc1\n"
"v_mfma_f32_16x16x32_bf16 v[0:3], a[112:115], a[144:147], v[0:3]\n"
"s_waitcnt vmcnt(22)\n"
"s_barrier\n"
"ds_read_b128 a[32:35], v255 offset:40960\n"
"ds_read_b128 a[36:39], v255 offset:46080\n"
"v_mfma_f32_16x16x32_bf16 v[0:3], a[116:119], a[148:151], v[0:3]\n"
"s_add_u32 s82, s82, 8192\n"
"ds_read_b128 a[40:43], v255 offset:51200\n"
"v_mfma_f32_16x16x32_bf16 v[0:3], a[120:123], a[152:155], v[0:3]\n"
"s_add_u32 m0, s43, 61440\n"
"ds_read_b128 a[44:47], v255 offset:56320\n"
"v_mfma_f32_16x16x32_bf16 v[0:3], a[124:127], a[156:159], v[0:3]\n"
"ds_read_b128 a[48:51], v255 offset:41984\n"
"buffer_load_dwordx4 v255, s[28:31], s83 offen offset:0 sc1 lds\n"
"v_mfma_f32_16x16x32_bf16 v[4:7], a[128:131], a[144:147], v[4:7]\n"
"ds_read_b128 a[52:55], v255 offset:47104\n"
"ds_read_b128 a[56:59], v255 offset:52224\n"
"v_mfma_f32_16x16x32_bf16 v[4:7], a[132:135], a[148:151], v[4:7]\n"
"ds_read_b128 a[60:63], v255 offset:57344\n"
"buffer_load_dwordx4 v255, s[28:31], s83 offen offset:1024 sc1 lds\n"
"v_mfma_f32_16x16x32_bf16 v[4:7], a[136:139], a[152:155], v[4:7]\n"
"ds_read_b128 a[64:67], v255 offset:43008\n"
"v_mfma_f32_16x16x32_bf16 v[4:7], a[140:143], a[156:159], v[4:7]\n"
"ds_read_b128 a[68:71], v255 offset:48128\n"
"buffer_load_dwordx4 v255, s[28:31], s83 offen offset:2048 sc1 lds\n"
"v_mfma_f32_16x16x32_bf16 v[8:11], a[0:3], a[144:147], v[8:11]\n"
"ds_read_b128 a[72:75], v255 offset:53248\n"
"ds_read_b128 a[76:79], v255 offset:58368\n"
"v_mfma_f32_16x16x32_bf16 v[8:11], a[4:7], a[148:151], v[8:11]\n"
"ds_read_b128 a[80:83], v255 offset:44032\n"
"buffer_load_dwordx4 v255, s[28:31], s83 offen offset:3072 sc1 lds\n"
"v_mfma_f32_16x16x32_bf16 v[8:11], a[8:11], a[152:155], v[8:11]\n"
"ds_read_b128 a[84:87], v255 offset:49152\n"
"v_mfma_f32_16x16x32_bf16 v[8:11], a[12:15], a[156:159], v[8:11]\n"
"s_add_u32 m0, s43, 65536\n"
"ds_read_b128 a[88:91], v255 offset:54272\n"
"v_mfma_f32_16x16x32_bf16 v[12:15], a[16:19], a[144:147], v[12:15]\n"
"ds_read_b128 a[92:95], v255 offset:59392\n"
"ds_read_b128 a[96:99], v255 offset:45056\n"
"buffer_load_dwordx4 v236, s[28:31], s83 offen sc1 lds\n"
"v_mfma_f32_16x16x32_bf16 v[12:15], a[20:23], a[148:151], v[12:15]\n"
"ds_read_b128 a[100:103], v255 offset:50176\n"
"v_mfma_f32_16x16x32_bf16 v[12:15], a[24:27], a[152:155], v[12:15]\n"
"s_add_u32 s83, s83, 163840\n"
"ds_read_b128 a[104:107], v255 offset:55296\n"
"buffer_load_dwordx4 a[240:243], v240, s[24:27], s82 offen sc1\n"
"v_mfma_f32_16x16x32_bf16 v[12:15], a[28:31], a[156:159], v[12:15]\n"
"ds_read_b128 a[108:111], v255 offset:60416\n"
"v_mfma_f32_16x16x32_bf16 v[16:19], a[208:211], a[144:147], v[16:19]\n"
"s_add_u32 s82, s82, 8192\n"
"v_mfma_f32_16x16x32_bf16 v[16:19], a[212:215], a[148:151], v[16:19]\n"
"buffer_load_dwordx4 a[244:247], v240, s[24:27], s82 offen sc1\n"
"v_mfma_f32_16x16x32_bf16 v[16:19], a[216:219], a[152:155], v[16:19]\n"
"v_mfma_f32_16x16x32_bf16 v[16:19], a[220:223], a[156:159], v[16:19]\n"
"s_waitcnt vmcnt(25) lgkmcnt(0)\n"
"s_add_u32 s82, s82, 8192\n"
"v_mfma_f32_16x16x32_bf16 v[0:3], a[32:35], a[160:163], v[0:3]\n"
"s_waitcnt vmcnt(20)\n"
"s_barrier\n"
"s_add_u32 m0, s76, 0\n"
"ds_read_b128 a[112:115], v243 offset:0\n"
"ds_read_b128 a[116:119], v243 offset:5120\n"
"v_mfma_f32_16x16x32_bf16 v[0:3], a[36:39], a[164:167], v[0:3]\n"
"ds_read_b128 a[120:123], v243 offset:10240\n"
"buffer_load_dword v220, v221, s[36:39], s95 offen sc1\n"
"buffer_load_dwordx4 v248, s[12:15], s84 offen lds\n"
"v_mfma_f32_16x16x32_bf16 v[0:3], a[40:43], a[168:171], v[0:3]\n"
"s_add_u32 m0, s76, 1024\n"
"ds_read_b128 a[124:127], v243 offset:15360\n"
"v_mfma_f32_16x16x32_bf16 v[0:3], a[44:47], a[172:175], v[0:3]\n"
"ds_read_b128 a[128:131], v243 offset:1024\n"
"buffer_load_dwordx4 v249, s[12:15], s84 offen lds\n"
"v_mfma_f32_16x16x32_bf16 v[4:7], a[48:51], a[160:163], v[4:7]\n"
"s_add_u32 m0, s76, 2048\n"
"ds_read_b128 a[132:135], v243 offset:6144\n"
"ds_read_b128 a[136:139], v243 offset:11264\n"
"v_mfma_f32_16x16x32_bf16 v[4:7], a[52:55], a[164:167], v[4:7]\n"
"ds_read_b128 a[140:143], v243 offset:16384\n"
"buffer_load_dwordx4 v250, s[12:15], s84 offen lds\n"
"v_mfma_f32_16x16x32_bf16 v[4:7], a[56:59], a[168:171], v[4:7]\n"
"ds_read_b128 a[0:3], v243 offset:2048\n"
"v_mfma_f32_16x16x32_bf16 v[4:7], a[60:63], a[172:175], v[4:7]\n"
"ds_read_b128 a[4:7], v243 offset:7168\n"
"v_mfma_f32_16x16x32_bf16 v[8:11], a[64:67], a[160:163], v[8:11]\n"
"ds_read_b128 a[8:11], v243 offset:12288\n"
"ds_read_b128 a[12:15], v243 offset:17408\n"
"v_mfma_f32_16x16x32_bf16 v[8:11], a[68:71], a[164:167], v[8:11]\n"
"ds_read_b128 a[16:19], v243 offset:3072\n"
"v_mfma_f32_16x16x32_bf16 v[8:11], a[72:75], a[168:171], v[8:11]\n"
"ds_read_b128 a[20:23], v243 offset:8192\n"
"v_mfma_f32_16x16x32_bf16 v[8:11], a[76:79], a[172:175], v[8:11]\n"
"ds_read_b128 a[24:27], v243 offset:13312\n"
"v_mfma_f32_16x16x32_bf16 v[12:15], a[80:83], a[160:163], v[12:15]\n"
"ds_read_b128 a[28:31], v243 offset:18432\n"
"ds_read_b128 a[208:211], v243 offset:4096\n"
"v_mfma_f32_16x16x32_bf16 v[12:15], a[84:87], a[164:167], v[12:15]\n"
"ds_read_b128 a[212:215], v243 offset:9216\n"
"v_mfma_f32_16x16x32_bf16 v[12:15], a[88:91], a[168:171], v[12:15]\n"
"ds_read_b128 a[216:219], v243 offset:14336\n"
"v_mfma_f32_16x16x32_bf16 v[12:15], a[92:95], a[172:175], v[12:15]\n"
"ds_read_b128 a[220:223], v243 offset:19456\n"
"v_mfma_f32_16x16x32_bf16 v[16:19], a[96:99], a[160:163], v[16:19]\n"
"v_mfma_f32_16x16x32_bf16 v[16:19], a[100:103], a[164:167], v[16:19]\n"
"v_mfma_f32_16x16x32_bf16 v[16:19], a[104:107], a[168:171], v[16:19]\n"
"v_mfma_f32_16x16x32_bf16 v[16:19], a[108:111], a[172:175], v[16:19]\n"
"s_waitcnt vmcnt(20) lgkmcnt(0)\n"
"v_mfma_f32_16x16x32_bf16 v[0:3], a[112:115], a[176:179], v[0:3]\n"
"s_waitcnt vmcnt(15)\n"
"s_barrier\n"
"ds_read_b128 a[32:35], v255 offset:0\n"
"ds_read_b128 a[36:39], v255 offset:5120\n"
"v_mfma_f32_16x16x32_bf16 v[0:3], a[116:119], a[180:183], v[0:3]\n"
"ds_read_b128 a[40:43], v255 offset:10240\n"
"v_mfma_f32_16x16x32_bf16 v[0:3], a[120:123], a[184:187], v[0:3]\n"
"ds_read_b128 a[44:47], v255 offset:15360\n"
"v_mfma_f32_16x16x32_bf16 v[0:3], a[124:127], a[188:191], v[0:3]\n"
"ds_read_b128 a[48:51], v255 offset:1024\n"
"v_mfma_f32_16x16x32_bf16 v[4:7], a[128:131], a[176:179], v[4:7]\n"
"ds_read_b128 a[52:55], v255 offset:6144\n"
"ds_read_b128 a[56:59], v255 offset:11264\n"
"v_mfma_f32_16x16x32_bf16 v[4:7], a[132:135], a[180:183], v[4:7]\n"
"ds_read_b128 a[60:63], v255 offset:16384\n"
"v_mfma_f32_16x16x32_bf16 v[4:7], a[136:139], a[184:187], v[4:7]\n"
"ds_read_b128 a[64:67], v255 offset:2048\n"
"v_mfma_f32_16x16x32_bf16 v[4:7], a[140:143], a[188:191], v[4:7]\n"
"ds_read_b128 a[68:71], v255 offset:7168\n"
"v_mfma_f32_16x16x32_bf16 v[8:11], a[0:3], a[176:179], v[8:11]\n"
"ds_read_b128 a[72:75], v255 offset:12288\n"
"ds_read_b128 a[76:79], v255 offset:17408\n"
"v_mfma_f32_16x16x32_bf16 v[8:11], a[4:7], a[180:183], v[8:11]\n"
"ds_read_b128 a[80:83], v255 offset:3072\n"
"v_mfma_f32_16x16x32_bf16 v[8:11], a[8:11], a[184:187], v[8:11]\n"
"ds_read_b128 a[84:87], v255 offset:8192\n"
"v_mfma_f32_16x16x32_bf16 v[8:11], a[12:15], a[188:191], v[8:11]\n"
"ds_read_b128 a[88:91], v255 offset:13312\n"
"v_mfma_f32_16x16x32_bf16 v[12:15], a[16:19], a[176:179], v[12:15]\n"
"ds_read_b128 a[92:95], v255 offset:18432\n"
"ds_read_b128 a[96:99], v255 offset:4096\n"
"v_mfma_f32_16x16x32_bf16 v[12:15], a[20:23], a[180:183], v[12:15]\n"
"ds_read_b128 a[100:103], v255 offset:9216\n"
"v_mfma_f32_16x16x32_bf16 v[12:15], a[24:27], a[184:187], v[12:15]\n"
"ds_read_b128 a[104:107], v255 offset:14336\n"
"v_mfma_f32_16x16x32_bf16 v[12:15], a[28:31], a[188:191], v[12:15]\n"
"ds_read_b128 a[108:111], v255 offset:19456\n"
"v_mfma_f32_16x16x32_bf16 v[16:19], a[208:211], a[176:179], v[16:19]\n"
"v_mfma_f32_16x16x32_bf16 v[16:19], a[212:215], a[180:183], v[16:19]\n"
"v_mfma_f32_16x16x32_bf16 v[16:19], a[216:219], a[184:187], v[16:19]\n"
"v_mfma_f32_16x16x32_bf16 v[16:19], a[220:223], a[188:191], v[16:19]\n"
"s_waitcnt vmcnt(11) lgkmcnt(0)\n"
"v_mfma_f32_16x16x32_bf16 v[0:3], a[32:35], a[192:195], v[0:3]\n"
"s_waitcnt vmcnt(6)\n"
"s_barrier\n"
"ds_read_b128 a[112:115], v242 offset:0\n"
"v_mfma_f32_16x16x32_bf16 v[0:3], a[36:39], a[196:199], v[0:3]\n"
"s_waitcnt vmcnt(3)\n"
"ds_read_b128 a[116:119], v242 offset:5120\n"
"v_cmp_ne_u32 vcc, s73, v220\n"
"v_mfma_f32_16x16x32_bf16 v[0:3], a[40:43], a[200:203], v[0:3]\n"
"s_cbranch_vccnz .L_ps_retry\n"
".L_ps_ok:\n"
"v_mfma_f32_16x16x32_bf16 v[0:3], a[44:47], a[204:207], v[0:3]\n"
"ds_read_b128 a[128:131], v242 offset:1024\n"
"buffer_load_dwordx4 v[80:83], v255, s[36:39], s86 offen sc1\n"
"buffer_load_dwordx4 v[84:87], v255, s[36:39], s86 offen offset:1024 sc1\n"
"buffer_load_dwordx2 v[88:89], v237, s[36:39], s86 offen offset:2048 sc1\n"
"v_mfma_f32_16x16x32_bf16 v[4:7], a[48:51], a[192:195], v[4:7]\n"
"s_add_u32 s86, s86, 10240\n"
"ds_read_b128 a[132:135], v242 offset:6144\n"
"buffer_load_dwordx4 v[90:93], v255, s[36:39], s86 offen sc1\n"
"buffer_load_dwordx4 v[94:97], v255, s[36:39], s86 offen offset:1024 sc1\n"
"buffer_load_dwordx2 v[98:99], v237, s[36:39], s86 offen offset:2048 sc1\n"
"v_mfma_f32_16x16x32_bf16 v[4:7], a[52:55], a[196:199], v[4:7]\n"
"s_add_u32 s86, s86, 10240\n"
"buffer_load_dwordx4 v[100:103], v255, s[36:39], s86 offen sc1\n"
"buffer_load_dwordx4 v[104:107], v255, s[36:39], s86 offen offset:1024 sc1\n"
"buffer_load_dwordx2 v[108:109], v237, s[36:39], s86 offen offset:2048 sc1\n"
"v_mfma_f32_16x16x32_bf16 v[4:7], a[56:59], a[200:203], v[4:7]\n"
"ds_read_b128 a[0:3], v242 offset:2048\n"
"v_mfma_f32_16x16x32_bf16 v[4:7], a[60:63], a[204:207], v[4:7]\n"
"v_mfma_f32_16x16x32_bf16 v[8:11], a[64:67], a[192:195], v[8:11]\n"
"ds_read_b128 a[4:7], v242 offset:7168\n"
"v_mfma_f32_16x16x32_bf16 v[8:11], a[68:71], a[196:199], v[8:11]\n"
"ds_read_b128 a[16:19], v242 offset:3072\n"
"v_mfma_f32_16x16x32_bf16 v[8:11], a[72:75], a[200:203], v[8:11]\n"
"v_mfma_f32_16x16x32_bf16 v[8:11], a[76:79], a[204:207], v[8:11]\n"
"ds_read_b128 a[20:23], v242 offset:8192\n"
"v_mfma_f32_16x16x32_bf16 v[12:15], a[80:83], a[192:195], v[12:15]\n"
"ds_read_b128 a[208:211], v242 offset:4096\n"
"v_mfma_f32_16x16x32_bf16 v[12:15], a[84:87], a[196:199], v[12:15]\n"
"v_mfma_f32_16x16x32_bf16 v[12:15], a[88:91], a[200:203], v[12:15]\n"
"ds_read_b128 a[212:215], v242 offset:9216\n"
"v_mfma_f32_16x16x32_bf16 v[12:15], a[92:95], a[204:207], v[12:15]\n"
"v_mfma_f32_16x16x32_bf16 v[16:19], a[96:99], a[192:195], v[16:19]\n"
"v_mfma_f32_16x16x32_bf16 v[16:19], a[100:103], a[196:199], v[16:19]\n"
"v_mfma_f32_16x16x32_bf16 v[16:19], a[104:107], a[200:203], v[16:19]\n"
"v_mfma_f32_16x16x32_bf16 v[16:19], a[108:111], a[204:207], v[16:19]\n"
"s_waitcnt lgkmcnt(0)\n"
"v_mfma_f32_16x16x32_bf16 v[0:3], a[112:115], a[240:243], v[0:3]\n"
"v_mfma_f32_16x16x32_bf16 v[0:3], a[116:119], a[244:247], v[0:3]\n"
"v_mfma_f32_16x16x32_bf16 v[4:7], a[128:131], a[240:243], v[4:7]\n"
".L_pad16:\n"
"s_cbranch_execz .L_padret1\n"
"v_mfma_f32_16x16x32_bf16 v[4:7], a[132:135], a[244:247], v[4:7]\n"
"v_mfma_f32_16x16x32_bf16 v[8:11], a[0:3], a[240:243], v[8:11]\n"
"v_mfma_f32_16x16x32_bf16 v[8:11], a[4:7], a[244:247], v[8:11]\n"
"v_mfma_f32_16x16x32_bf16 v[12:15], a[16:19], a[240:243], v[12:15]\n"
"v_mfma_f32_16x16x32_bf16 v[12:15], a[20:23], a[244:247], v[12:15]\n"
"v_mfma_f32_16x16x32_bf16 v[16:19], a[208:211], a[240:243], v[16:19]\n"
"v_mfma_f32_16x16x32_bf16 v[16:19], a[212:215], a[244:247], v[16:19]\n"
"s_waitcnt vmcnt(9)\n"
"ds_read_b64 v[64:65], v244 offset:0\n"
"ds_read_b64 v[66:67], v244 offset:32\n"
"ds_read_b64 v[68:69], v244 offset:64\n"
"ds_read_b64 v[70:71], v244 offset:96\n"
"ds_read_b64 v[72:73], v244 offset:128\n"
"s_mov_b32 s90, 0xbf317218\n"
"s_mov_b32 s91, 0xbf317218\n"
"s_mov_b32 s92, 0x40053526\n"
"s_mov_b32 s93, 0x40053526\n"
"v_pk_mul_f32 v[20:21], s[90:91], v[0:1]\n"
"v_pk_mul_f32 v[22:23], s[90:91], v[2:3]\n"
"v_pk_mul_f32 v[24:25], s[90:91], v[4:5]\n"
"v_pk_mul_f32 v[26:27], s[90:91], v[6:7]\n"
"v_pk_mul_f32 v[28:29], s[90:91], v[8:9]\n"
"v_pk_mul_f32 v[30:31], s[90:91], v[10:11]\n"
"v_pk_mul_f32 v[32:33], s[90:91], v[12:13]\n"
"v_pk_mul_f32 v[34:35], s[90:91], v[14:15]\n"
"v_pk_mul_f32 v[36:37], s[90:91], v[16:17]\n"
"v_pk_mul_f32 v[38:39], s[90:91], v[18:19]\n"
"v_exp_f32 v20, v20\n"
"v_exp_f32 v21, v21\n"
"v_exp_f32 v22, v22\n"
"v_exp_f32 v23, v23\n"
"v_exp_f32 v24, v24\n"
"v_exp_f32 v25, v25\n"
"v_exp_f32 v26, v26\n"
"v_exp_f32 v27, v27\n"
"v_exp_f32 v28, v28\n"
"v_exp_f32 v29, v29\n"
"v_exp_f32 v30, v30\n"
"v_exp_f32 v31, v31\n"
"v_exp_f32 v32, v32\n"
"v_exp_f32 v33, v33\n"
"v_exp_f32 v34, v34\n"
"v_exp_f32 v35, v35\n"
"v_exp_f32 v36, v36\n"
"v_exp_f32 v37, v37\n"
"v_exp_f32 v38, v38\n"
"v_exp_f32 v39, v39\n"
"v_pk_fma_f32 v[20:21], v[20:21], s[92:93], s[92:93]\n"
"v_pk_fma_f32 v[22:23], v[22:23], s[92:93], s[92:93]\n"
"v_pk_fma_f32 v[24:25], v[24:25], s[92:93], s[92:93]\n"
"v_pk_fma_f32 v[26:27], v[26:27], s[92:93], s[92:93]\n"
"v_pk_fma_f32 v[28:29], v[28:29], s[92:93], s[92:93]\n"
"v_pk_fma_f32 v[30:31], v[30:31], s[92:93], s[92:93]\n"
"v_pk_fma_f32 v[32:33], v[32:33], s[92:93], s[92:93]\n"
"v_pk_fma_f32 v[34:35], v[34:35], s[92:93], s[92:93]\n"
"v_pk_fma_f32 v[36:37], v[36:37], s[92:93], s[92:93]\n"
"v_pk_fma_f32 v[38:39], v[38:39], s[92:93], s[92:93]\n"
"v_rcp_f32 v20, v20\n"
"v_rcp_f32 v21, v21\n"
"v_rcp_f32 v22, v22\n"
"v_rcp_f32 v23, v23\n"
"v_rcp_f32 v24, v24\n"
"v_rcp_f32 v25, v25\n"
"v_rcp_f32 v26, v26\n"
"v_rcp_f32 v27, v27\n"
"v_rcp_f32 v28, v28\n"
"v_rcp_f32 v29, v29\n"
"v_rcp_f32 v30, v30\n"
"v_rcp_f32 v31, v31\n"
"v_rcp_f32 v32, v32\n"
"v_rcp_f32 v33, v33\n"
"v_rcp_f32 v34, v34\n"
"v_rcp_f32 v35, v35\n"
"v_rcp_f32 v36, v36\n"
"v_rcp_f32 v37, v37\n"
"v_rcp_f32 v38, v38\n"
"v_rcp_f32 v39, v39\n"
"s_waitcnt lgkmcnt(0)\n"
"s_nop 0\n"
"v_lshlrev_b32 v176, 16, v64\n"
"v_lshlrev_b32 v180, 16, v66\n"
"v_lshlrev_b32 v184, 16, v68\n"
"v_lshlrev_b32 v188, 16, v70\n"
"v_lshlrev_b32 v192, 16, v72\n"
"v_lshlrev_b32 v178, 16, v65\n"
"v_lshlrev_b32 v182, 16, v67\n"
"v_lshlrev_b32 v186, 16, v69\n"
"v_lshlrev_b32 v190, 16, v71\n"
"v_lshlrev_b32 v194, 16, v73\n"
"v_and_b32 v177, 0xffff0000, v64\n"
"v_and_b32 v181, 0xffff0000, v66\n"
"v_and_b32 v185, 0xffff0000, v68\n"
"v_and_b32 v189, 0xffff0000, v70\n"
"v_and_b32 v193, 0xffff0000, v72\n"
"v_and_b32 v179, 0xffff0000, v65\n"
"v_and_b32 v183, 0xffff0000, v67\n"
"v_and_b32 v187, 0xffff0000, v69\n"
"v_and_b32 v191, 0xffff0000, v71\n"
"v_and_b32 v195, 0xffff0000, v73\n"
"v_pk_mul_f32 v[0:1], v[0:1], v[20:21]\n"
"v_pk_mul_f32 v[2:3], v[2:3], v[22:23]\n"
"v_pk_mul_f32 v[4:5], v[4:5], v[24:25]\n"
"v_pk_mul_f32 v[6:7], v[6:7], v[26:27]\n"
"v_pk_mul_f32 v[8:9], v[8:9], v[28:29]\n"
"v_pk_mul_f32 v[10:11], v[10:11], v[30:31]\n"
"v_pk_mul_f32 v[12:13], v[12:13], v[32:33]\n"
"v_pk_mul_f32 v[14:15], v[14:15], v[34:35]\n"
"v_pk_mul_f32 v[16:17], v[16:17], v[36:37]\n"
"v_pk_mul_f32 v[18:19], v[18:19], v[38:39]\n"
"s_waitcnt vmcnt(6)\n"
"s_nop 0\n"
"v_lshlrev_b32 v40, 16, v80\n"
"v_lshlrev_b32 v42, 16, v81\n"
"v_lshlrev_b32 v44, 16, v82\n"
"v_lshlrev_b32 v46, 16, v83\n"
"v_lshlrev_b32 v48, 16, v84\n"
"v_lshlrev_b32 v50, 16, v85\n"
"v_lshlrev_b32 v52, 16, v86\n"
"v_lshlrev_b32 v54, 16, v87\n"
"v_lshlrev_b32 v56, 16, v88\n"
"v_lshlrev_b32 v58, 16, v89\n"
"v_and_b32 v41, 0xffff0000, v80\n"
"v_and_b32 v43, 0xffff0000, v81\n"
"v_and_b32 v45, 0xffff0000, v82\n"
"v_and_b32 v47, 0xffff0000, v83\n"
"v_and_b32 v49, 0xffff0000, v84\n"
"v_and_b32 v51, 0xffff0000, v85\n"
"v_and_b32 v53, 0xffff0000, v86\n"
"v_and_b32 v55, 0xffff0000, v87\n"
"v_and_b32 v57, 0xffff0000, v88\n"
"v_and_b32 v59, 0xffff0000, v89\n"
"v_pk_add_f32 v[128:129], v[128:129], v[40:41]\n"
"v_pk_add_f32 v[130:131], v[130:131], v[42:43]\n"
"v_pk_add_f32 v[136:137], v[136:137], v[44:45]\n"
"v_pk_add_f32 v[138:139], v[138:139], v[46:47]\n"
"v_pk_add_f32 v[144:145], v[144:145], v[48:49]\n"
"v_pk_add_f32 v[146:147], v[146:147], v[50:51]\n"
"v_pk_add_f32 v[152:153], v[152:153], v[52:53]\n"
"v_pk_add_f32 v[154:155], v[154:155], v[54:55]\n"
"v_pk_add_f32 v[160:161], v[160:161], v[56:57]\n"
"v_pk_add_f32 v[162:163], v[162:163], v[58:59]\n"
"s_waitcnt vmcnt(3)\n"
"s_nop 0\n"
"v_lshlrev_b32 v40, 16, v90\n"
"v_lshlrev_b32 v42, 16, v91\n"
"v_lshlrev_b32 v44, 16, v92\n"
"v_lshlrev_b32 v46, 16, v93\n"
"v_lshlrev_b32 v48, 16, v94\n"
"v_lshlrev_b32 v50, 16, v95\n"
"v_lshlrev_b32 v52, 16, v96\n"
"v_lshlrev_b32 v54, 16, v97\n"
"v_lshlrev_b32 v56, 16, v98\n"
"v_lshlrev_b32 v58, 16, v99\n"
"v_and_b32 v41, 0xffff0000, v90\n"
"v_and_b32 v43, 0xffff0000, v91\n"
"v_and_b32 v45, 0xffff0000, v92\n"
"v_and_b32 v47, 0xffff0000, v93\n"
"v_and_b32 v49, 0xffff0000, v94\n"
"v_and_b32 v51, 0xffff0000, v95\n"
"v_and_b32 v53, 0xffff0000, v96\n"
"v_and_b32 v55, 0xffff0000, v97\n"
"v_and_b32 v57, 0xffff0000, v98\n"
"v_and_b32 v59, 0xffff0000, v99\n"
"v_pk_add_f32 v[128:129], v[128:129], v[40:41]\n"
"v_pk_add_f32 v[130:131], v[130:131], v[42:43]\n"
"v_pk_add_f32 v[136:137], v[136:137], v[44:45]\n"
"v_pk_add_f32 v[138:139], v[138:139], v[46:47]\n"
"v_pk_add_f32 v[144:145], v[144:145], v[48:49]\n"
"v_pk_add_f32 v[146:147], v[146:147], v[50:51]\n"
"v_pk_add_f32 v[152:153], v[152:153], v[52:53]\n"
"v_pk_add_f32 v[154:155], v[154:155], v[54:55]\n"
"v_pk_add_f32 v[160:161], v[160:161], v[56:57]\n"
"v_pk_add_f32 v[162:163], v[162:163], v[58:59]\n"
"s_waitcnt vmcnt(0)\n"
"s_nop 0\n"
"v_lshlrev_b32 v40, 16, v100\n"
"v_lshlrev_b32 v42, 16, v101\n"
"v_lshlrev_b32 v44, 16, v102\n"
"v_lshlrev_b32 v46, 16, v103\n"
"v_lshlrev_b32 v48, 16, v104\n"
"v_lshlrev_b32 v50, 16, v105\n"
"v_lshlrev_b32 v52, 16, v106\n"
"v_lshlrev_b32 v54, 16, v107\n"
"v_lshlrev_b32 v56, 16, v108\n"
"v_lshlrev_b32 v58, 16, v109\n"
"v_and_b32 v41, 0xffff0000, v100\n"
"v_and_b32 v43, 0xffff0000, v101\n"
"v_and_b32 v45, 0xffff0000, v102\n"
"v_and_b32 v47, 0xffff0000, v103\n"
"v_and_b32 v49, 0xffff0000, v104\n"
"v_and_b32 v51, 0xffff0000, v105\n"
"v_and_b32 v53, 0xffff0000, v106\n"
"v_and_b32 v55, 0xffff0000, v107\n"
"v_and_b32 v57, 0xffff0000, v108\n"
"v_and_b32 v59, 0xffff0000, v109\n"
"v_pk_add_f32 v[128:129], v[128:129], v[40:41]\n"
"v_pk_add_f32 v[130:131], v[130:131], v[42:43]\n"
"v_pk_add_f32 v[136:137], v[136:137], v[44:45]\n"
"v_pk_add_f32 v[138:139], v[138:139], v[46:47]\n"
"v_pk_add_f32 v[144:145], v[144:145], v[48:49]\n"
"v_pk_add_f32 v[146:147], v[146:147], v[50:51]\n"
"v_pk_add_f32 v[152:153], v[152:153], v[52:53]\n"
"v_pk_add_f32 v[154:155], v[154:155], v[54:55]\n"
"v_pk_add_f32 v[160:161], v[160:161], v[56:57]\n"
"v_pk_add_f32 v[162:163], v[162:163], v[58:59]\n"
"v_pk_fma_f32 v[176:177], v[0:1], v[128:129], v[176:177]\n"
"v_pk_fma_f32 v[178:179], v[2:3], v[130:131], v[178:179]\n"
"v_cvt_pk_bf16_f32 v176, v176, v177\n"
"v_cvt_pk_bf16_f32 v177, v178, v179\n"
"ds_write_b64 v244, v[176:177] offset:0\n"
"v_pk_fma_f32 v[180:181], v[4:5], v[136:137], v[180:181]\n"
"v_pk_fma_f32 v[182:183], v[6:7], v[138:139], v[182:183]\n"
"v_cvt_pk_bf16_f32 v180, v180, v181\n"
"v_cvt_pk_bf16_f32 v181, v182, v183\n"
"ds_write_b64 v244, v[180:181] offset:32\n"
"v_pk_fma_f32 v[184:185], v[8:9], v[144:145], v[184:185]\n"
"v_pk_fma_f32 v[186:187], v[10:11], v[146:147], v[186:187]\n"
"v_cvt_pk_bf16_f32 v184, v184, v185\n"
"v_cvt_pk_bf16_f32 v185, v186, v187\n"
"ds_write_b64 v244, v[184:185] offset:64\n"
"v_pk_fma_f32 v[188:189], v[12:13], v[152:153], v[188:189]\n"
"v_pk_fma_f32 v[190:191], v[14:15], v[154:155], v[190:191]\n"
"v_cvt_pk_bf16_f32 v188, v188, v189\n"
"v_cvt_pk_bf16_f32 v189, v190, v191\n"
"ds_write_b64 v244, v[188:189] offset:96\n"
"v_pk_fma_f32 v[192:193], v[16:17], v[160:161], v[192:193]\n"
"v_pk_fma_f32 v[194:195], v[18:19], v[162:163], v[194:195]\n"
"v_cvt_pk_bf16_f32 v192, v192, v193\n"
"v_cvt_pk_bf16_f32 v193, v194, v195\n"
"ds_write_b64 v244, v[192:193] offset:128\n"
"s_waitcnt lgkmcnt(0)\n"
"ds_read_b128 v[0:3], v245 offset:0\n"
"ds_read_b128 v[4:7], v245 offset:1024\n"
"ds_read_b128 v[8:11], v245 offset:2048\n"
"s_waitcnt lgkmcnt(2)\n"
"buffer_store_dwordx4 v[0:3], v248, s[60:63], s84 offen\n"
"s_waitcnt lgkmcnt(1)\n"
"buffer_store_dwordx4 v[4:7], v249, s[60:63], s84 offen\n"
"s_waitcnt lgkmcnt(0)\n"
"buffer_store_dwordx4 v[8:11], v250, s[60:63], s84 offen\n"
"s_or_b32 s90, s75, s68\n"
"s_cbranch_scc1 .L_fin\n"
"s_add_u32 s90, s74, 1\n"
"v_mov_b32 v1, s90\n"
"buffer_store_dword v1, v225, s[36:39], 0 offen sc1\n"
".L_fin:\n"
"s_endpgm\n"
".L_stats_retry:\n"
"s_mov_b32 s34, 0\n"
".L_stats_spin:\n"
"s_sleep 4\n"
"buffer_load_dwordx2 v[136:137], v222, s[36:39], s94 offen offset:0 sc1\n"
"buffer_load_dwordx2 v[138:139], v222, s[36:39], s94 offen offset:128 sc1\n"
"buffer_load_dwordx2 v[140:141], v222, s[36:39], s94 offen offset:256 sc1\n"
"buffer_load_dwordx2 v[142:143], v222, s[36:39], s94 offen offset:384 sc1\n"
"s_waitcnt vmcnt(0)\n"
"v_xor_b32 v137, s73, v137\n"
"v_xor_b32 v139, s73, v139\n"
"v_xor_b32 v141, s73, v141\n"
"v_xor_b32 v143, s73, v143\n"
"v_or3_b32 v137, v137, v139, v141\n"
"v_or_b32 v137, v137, v143\n"
"v_cmp_ne_u32 vcc, 0, v137\n"
"s_nop 1\n"
"s_cbranch_vccz .L_stats_ok\n"
"s_add_u32 s34, s34, 1\n"
"s_cmp_lt_u32 s34, 100000\n"
"s_cbranch_scc1 .L_stats_spin\n"
"s_branch .L_stats_ok\n"
".L_fx_retry:\n"
"s_mov_b32 s34, 0\n"
".L_fx_spin:\n"
"s_sleep 4\n"
"buffer_load_dword v220, v221, s[36:39], s95 offen sc1\n"
"s_waitcnt vmcnt(0)\n"
"v_cmp_ne_u32 vcc, s73, v220\n"
"s_nop 1\n"
"s_cbranch_vccz .L_fx_ok\n"
"s_add_u32 s34, s34, 1\n"
"s_cmp_lt_u32 s34, 100000\n"
"s_cbranch_scc1 .L_fx_spin\n"
"s_branch .L_fx_ok\n"
".L_f1_retry:\n"
"s_mov_b32 s34, 0\n"
".L_f1_spin:\n"
"s_sleep 4\n"
"buffer_load_dword v220, v255, s[36:39], s95 offen sc1\n"
"s_waitcnt vmcnt(0)\n"
"v_cmp_ne_u32 vcc, s73, v220\n"
"s_nop 1\n"
"s_cbranch_vccz .L_f1_ok\n"
"s_add_u32 s34, s34, 1\n"
"s_cmp_lt_u32 s34, 100000\n"
"s_cbranch_scc1 .L_f1_spin\n"
"s_branch .L_f1_ok\n"
".L_fo_retry:\n"
"s_mov_b32 s34, 0\n"
".L_fo_spin:\n"
"s_sleep 4\n"
"buffer_load_dword v220, v221, s[36:39], s95 offen sc1\n"
"s_waitcnt vmcnt(0)\n"
"v_cmp_ne_u32 vcc, s73, v220\n"
"s_nop 1\n"
"s_cbranch_vccz .L_fo_ok\n"
"s_add_u32 s34, s34, 1\n"
"s_cmp_lt_u32 s34, 100000\n"
"s_cbranch_scc1 .L_fo_spin\n"
"s_branch .L_fo_ok\n"
".L_nogu:\n"
"s_nop 7\n"
"s_lshl_b32 s95, s68, 10\n"
"s_add_u32 s95, s95, 4096\n"
"buffer_load_dword v220, v255, s[36:39], s95 offen sc1\n"
"v_accvgpr_read_b32 v16, a8\n"
"v_accvgpr_read_b32 v17, a9\n"
"v_accvgpr_read_b32 v18, a10\n"
"v_accvgpr_read_b32 v19, a11\n"
"v_accvgpr_read_b32 v20, a24\n"
"v_accvgpr_read_b32 v21, a25\n"
"v_accvgpr_read_b32 v22, a26\n"
"v_accvgpr_read_b32 v23, a27\n"
"v_mul_f32 v16, v214, v16\n"
"v_mul_f32 v17, v214, v17\n"
"v_mul_f32 v18, v214, v18\n"
"v_mul_f32 v19, v214, v19\n"
"v_mul_f32 v20, v214, v20\n"
"v_mul_f32 v21, v214, v21\n"
"v_mul_f32 v22, v214, v22\n"
"v_mul_f32 v23, v214, v23\n"
"v_cvt_pk_bf16_f32 v72, v16, v17\n"
"v_cvt_pk_bf16_f32 v73, v18, v19\n"
"v_cvt_pk_bf16_f32 v74, v20, v21\n"
"v_cvt_pk_bf16_f32 v75, v22, v23\n"
"buffer_store_dwordx4 v[72:75], v255, s[36:39], s44 offen offset:2048 sc1\n"
"s_sleep 5\n"
"v_accvgpr_read_b32 v24, a12\n"
"v_accvgpr_read_b32 v25, a13\n"
"v_accvgpr_read_b32 v26, a14\n"
"v_accvgpr_read_b32 v27, a15\n"
"v_accvgpr_read_b32 v28, a28\n"
"v_accvgpr_read_b32 v29, a29\n"
"v_accvgpr_read_b32 v30, a30\n"
"v_accvgpr_read_b32 v31, a31\n"
"v_mul_f32 v24, v215, v24\n"
"v_mul_f32 v25, v215, v25\n"
"v_mul_f32 v26, v215, v26\n"
"v_mul_f32 v27, v215, v27\n"
"v_mul_f32 v28, v215, v28\n"
"v_mul_f32 v29, v215, v29\n"
"v_mul_f32 v30, v215, v30\n"
"v_mul_f32 v31, v215, v31\n"
"v_cvt_pk_bf16_f32 v76, v24, v25\n"
"v_cvt_pk_bf16_f32 v77, v26, v27\n"
"v_cvt_pk_bf16_f32 v78, v28, v29\n"
"v_cvt_pk_bf16_f32 v79, v30, v31\n"
"buffer_store_dwordx4 v[76:79], v255, s[36:39], s44 offen offset:3072 sc1\n"
"s_sleep 5\n"
"s_add_u32 s44, s44, 8192\n"
"v_accvgpr_read_b32 v32, a32\n"
"v_accvgpr_read_b32 v33, a33\n"
"v_accvgpr_read_b32 v34, a34\n"
"v_accvgpr_read_b32 v35, a35\n"
"v_accvgpr_read_b32 v36, a48\n"
"v_accvgpr_read_b32 v37, a49\n"
"v_accvgpr_read_b32 v38, a50\n"
"v_accvgpr_read_b32 v39, a51\n"
"v_mul_f32 v32, v212, v32\n"
"v_mul_f32 v33, v212, v33\n"
"v_mul_f32 v34, v212, v34\n"
"v_mul_f32 v35, v212, v35\n"
"v_mul_f32 v36, v212, v36\n"
"v_mul_f32 v37, v212, v37\n"
"v_mul_f32 v38, v212, v38\n"
"v_mul_f32 v39, v212, v39\n"
"v_cvt_pk_bf16_f32 v80, v32, v33\n"
"v_cvt_pk_bf16_f32 v81, v34, v35\n"
"v_cvt_pk_bf16_f32 v82, v36, v37\n"
"v_cvt_pk_bf16_f32 v83, v38, v39\n"
"buffer_store_dwordx4 v[80:83], v255, s[36:39], s44 offen offset:0 sc1\n"
"s_sleep 5\n"
"v_accvgpr_read_b32 v40, a36\n"
"v_accvgpr_read_b32 v41, a37\n"
"v_accvgpr_read_b32 v42, a38\n"
"v_accvgpr_read_b32 v43, a39\n"
"v_accvgpr_read_b32 v44, a52\n"
"v_accvgpr_read_b32 v45, a53\n"
"v_accvgpr_read_b32 v46, a54\n"
"v_accvgpr_read_b32 v47, a55\n"
"v_mul_f32 v40, v213, v40\n"
"v_mul_f32 v41, v213, v41\n"
"v_mul_f32 v42, v213, v42\n"
"v_mul_f32 v43, v213, v43\n"
"v_mul_f32 v44, v213, v44\n"
"v_mul_f32 v45, v213, v45\n"
"v_mul_f32 v46, v213, v46\n"
"v_mul_f32 v47, v213, v47\n"
"v_cvt_pk_bf16_f32 v84, v40, v41\n"
"v_cvt_pk_bf16_f32 v85, v42, v43\n"
"v_cvt_pk_bf16_f32 v86, v44, v45\n"
"v_cvt_pk_bf16_f32 v87, v46, v47\n"
"buffer_store_dwordx4 v[84:87], v255, s[36:39], s44 offen offset:1024 sc1\n"
"s_sleep 5\n"
"v_accvgpr_read_b32 v48, a40\n"
"v_accvgpr_read_b32 v49, a41\n"
"v_accvgpr_read_b32 v50, a42\n"
"v_accvgpr_read_b32 v51, a43\n"
"v_accvgpr_read_b32 v52, a56\n"
"v_accvgpr_read_b32 v53, a57\n"
"v_accvgpr_read_b32 v54, a58\n"
"v_accvgpr_read_b32 v55, a59\n"
"v_mul_f32 v48, v214, v48\n"
"v_mul_f32 v49, v214, v49\n"
"v_mul_f32 v50, v214, v50\n"
"v_mul_f32 v51, v214, v51\n"
"v_mul_f32 v52, v214, v52\n"
"v_mul_f32 v53, v214, v53\n"
"v_mul_f32 v54, v214, v54\n"
"v_mul_f32 v55, v214, v55\n"
"v_cvt_pk_bf16_f32 v88, v48, v49\n"
"v_cvt_pk_bf16_f32 v89, v50, v51\n"
"v_cvt_pk_bf16_f32 v90, v52, v53\n"
"v_cvt_pk_bf16_f32 v91, v54, v55\n"
"buffer_store_dwordx4 v[88:91], v255, s[36:39], s44 offen offset:2048 sc1\n"
"s_sleep 5\n"
"v_accvgpr_read_b32 v56, a44\n"
"v_accvgpr_read_b32 v57, a45\n"
"v_accvgpr_read_b32 v58, a46\n"
"v_accvgpr_read_b32 v59, a47\n"
"v_accvgpr_read_b32 v60, a60\n"
"v_accvgpr_read_b32 v61, a61\n"
"v_accvgpr_read_b32 v62, a62\n"
"v_accvgpr_read_b32 v63, a63\n"
"v_mul_f32 v56, v215, v56\n"
"v_mul_f32 v57, v215, v57\n"
"v_mul_f32 v58, v215, v58\n"
"v_mul_f32 v59, v215, v59\n"
"v_mul_f32 v60, v215, v60\n"
"v_mul_f32 v61, v215, v61\n"
"v_mul_f32 v62, v215, v62\n"
"v_mul_f32 v63, v215, v63\n"
"v_cvt_pk_bf16_f32 v92, v56, v57\n"
"v_cvt_pk_bf16_f32 v93, v58, v59\n"
"v_cvt_pk_bf16_f32 v94, v60, v61\n"
"v_cvt_pk_bf16_f32 v95, v62, v63\n"
"buffer_store_dwordx4 v[92:95], v255, s[36:39], s44 offen offset:3072 sc1\n"
"s_sleep 5\n"
"s_add_u32 s44, s44, 8192\n"
"v_accvgpr_read_b32 v0, a64\n"
"v_accvgpr_read_b32 v1, a65\n"
"v_accvgpr_read_b32 v2, a66\n"
"v_accvgpr_read_b32 v3, a67\n"
"v_accvgpr_read_b32 v4, a80\n"
"v_accvgpr_read_b32 v5, a81\n"
"v_accvgpr_read_b32 v6, a82\n"
"v_accvgpr_read_b32 v7, a83\n"
"v_mul_f32 v0, v212, v0\n"
"v_mul_f32 v1, v212, v1\n"
"v_mul_f32 v2, v212, v2\n"
"v_mul_f32 v3, v212, v3\n"
"v_mul_f32 v4, v212, v4\n"
"v_mul_f32 v5, v212, v5\n"
"v_mul_f32 v6, v212, v6\n"
"v_mul_f32 v7, v212, v7\n"
"v_cvt_pk_bf16_f32 v96, v0, v1\n"
"v_cvt_pk_bf16_f32 v97, v2, v3\n"
"v_cvt_pk_bf16_f32 v98, v4, v5\n"
"v_cvt_pk_bf16_f32 v99, v6, v7\n"
"buffer_store_dwordx4 v[96:99], v255, s[36:39], s44 offen offset:0 sc1\n"
"s_sleep 5\n"
"v_accvgpr_read_b32 v8, a68\n"
"v_accvgpr_read_b32 v9, a69\n"
"v_accvgpr_read_b32 v10, a70\n"
"v_accvgpr_read_b32 v11, a71\n"
"v_accvgpr_read_b32 v12, a84\n"
"v_accvgpr_read_b32 v13, a85\n"
"v_accvgpr_read_b32 v14, a86\n"
"v_accvgpr_read_b32 v15, a87\n"
"v_mul_f32 v8, v213, v8\n"
"v_mul_f32 v9, v213, v9\n"
"v_mul_f32 v10, v213, v10\n"
"v_mul_f32 v11, v213, v11\n"
"v_mul_f32 v12, v213, v12\n"
"v_mul_f32 v13, v213, v13\n"
"v_mul_f32 v14, v213, v14\n"
"v_mul_f32 v15, v213, v15\n"
"v_cvt_pk_bf16_f32 v100, v8, v9\n"
"v_cvt_pk_bf16_f32 v101, v10, v11\n"
"v_cvt_pk_bf16_f32 v102, v12, v13\n"
"v_cvt_pk_bf16_f32 v103, v14, v15\n"
"buffer_store_dwordx4 v[100:103], v255, s[36:39], s44 offen offset:1024 sc1\n"
"s_sleep 5\n"
"v_accvgpr_read_b32 v16, a72\n"
"v_accvgpr_read_b32 v17, a73\n"
"v_accvgpr_read_b32 v18, a74\n"
"v_accvgpr_read_b32 v19, a75\n"
"v_accvgpr_read_b32 v20, a88\n"
"v_accvgpr_read_b32 v21, a89\n"
"v_accvgpr_read_b32 v22, a90\n"
"v_accvgpr_read_b32 v23, a91\n"
"v_mul_f32 v16, v214, v16\n"
"v_mul_f32 v17, v214, v17\n"
"v_mul_f32 v18, v214, v18\n"
"v_mul_f32 v19, v214, v19\n"
"v_mul_f32 v20, v214, v20\n"
"v_mul_f32 v21, v214, v21\n"
"v_mul_f32 v22, v214, v22\n"
"v_mul_f32 v23, v214, v23\n"
"v_cvt_pk_bf16_f32 v104, v16, v17\n"
"v_cvt_pk_bf16_f32 v105, v18, v19\n"
"v_cvt_pk_bf16_f32 v106, v20, v21\n"
"v_cvt_pk_bf16_f32 v107, v22, v23\n"
"buffer_store_dwordx4 v[104:107], v255, s[36:39], s44 offen offset:2048 sc1\n"
"s_sleep 5\n"
"v_accvgpr_read_b32 v24, a76\n"
"v_accvgpr_read_b32 v25, a77\n"
"v_accvgpr_read_b32 v26, a78\n"
"v_accvgpr_read_b32 v27, a79\n"
"v_accvgpr_read_b32 v28, a92\n"
"v_accvgpr_read_b32 v29, a93\n"
"v_accvgpr_read_b32 v30, a94\n"
"v_accvgpr_read_b32 v31, a95\n"
"v_mul_f32 v24, v215, v24\n"
"v_mul_f32 v25, v215, v25\n"
"v_mul_f32 v26, v215, v26\n"
"v_mul_f32 v27, v215, v27\n"
"v_mul_f32 v28, v215, v28\n"
"v_mul_f32 v29, v215, v29\n"
"v_mul_f32 v30, v215, v30\n"
"v_mul_f32 v31, v215, v31\n"
"v_cvt_pk_bf16_f32 v108, v24, v25\n"
"v_cvt_pk_bf16_f32 v109, v26, v27\n"
"v_cvt_pk_bf16_f32 v110, v28, v29\n"
"v_cvt_pk_bf16_f32 v111, v30, v31\n"
"buffer_store_dwordx4 v[108:111], v255, s[36:39], s44 offen offset:3072 sc1\n"
"s_sleep 5\n"
"s_add_u32 s44, s44, 8192\n"
"v_accvgpr_read_b32 v32, a96\n"
"v_accvgpr_read_b32 v33, a97\n"
"v_accvgpr_read_b32 v34, a98\n"
"v_accvgpr_read_b32 v35, a99\n"
"v_accvgpr_read_b32 v36, a112\n"
"v_accvgpr_read_b32 v37, a113\n"
"v_accvgpr_read_b32 v38, a114\n"
"v_accvgpr_read_b32 v39, a115\n"
"v_mul_f32 v32, v212, v32\n"
"v_mul_f32 v33, v212, v33\n"
"v_mul_f32 v34, v212, v34\n"
"v_mul_f32 v35, v212, v35\n"
"v_mul_f32 v36, v212, v36\n"
"v_mul_f32 v37, v212, v37\n"
"v_mul_f32 v38, v212, v38\n"
"v_mul_f32 v39, v212, v39\n"
"v_cvt_pk_bf16_f32 v112, v32, v33\n"
"v_cvt_pk_bf16_f32 v113, v34, v35\n"
"v_cvt_pk_bf16_f32 v114, v36, v37\n"
"v_cvt_pk_bf16_f32 v115, v38, v39\n"
"buffer_store_dwordx4 v[112:115], v255, s[36:39], s44 offen offset:0 sc1\n"
"s_sleep 5\n"
"v_accvgpr_read_b32 v40, a100\n"
"v_accvgpr_read_b32 v41, a101\n"
"v_accvgpr_read_b32 v42, a102\n"
"v_accvgpr_read_b32 v43, a103\n"
"v_accvgpr_read_b32 v44, a116\n"
"v_accvgpr_read_b32 v45, a117\n"
"v_accvgpr_read_b32 v46, a118\n"
"v_accvgpr_read_b32 v47, a119\n"
"v_mul_f32 v40, v213, v40\n"
"v_mul_f32 v41, v213, v41\n"
"v_mul_f32 v42, v213, v42\n"
"v_mul_f32 v43, v213, v43\n"
"v_mul_f32 v44, v213, v44\n"
"v_mul_f32 v45, v213, v45\n"
"v_mul_f32 v46, v213, v46\n"
"v_mul_f32 v47, v213, v47\n"
"v_cvt_pk_bf16_f32 v116, v40, v41\n"
"v_cvt_pk_bf16_f32 v117, v42, v43\n"
"v_cvt_pk_bf16_f32 v118, v44, v45\n"
"v_cvt_pk_bf16_f32 v119, v46, v47\n"
"buffer_store_dwordx4 v[116:119], v255, s[36:39], s44 offen offset:1024 sc1\n"
"s_sleep 5\n"
"v_accvgpr_read_b32 v48, a104\n"
"v_accvgpr_read_b32 v49, a105\n"
"v_accvgpr_read_b32 v50, a106\n"
"v_accvgpr_read_b32 v51, a107\n"
"v_accvgpr_read_b32 v52, a120\n"
"v_accvgpr_read_b32 v53, a121\n"
"v_accvgpr_read_b32 v54, a122\n"
"v_accvgpr_read_b32 v55, a123\n"
"v_mul_f32 v48, v214, v48\n"
"v_mul_f32 v49, v214, v49\n"
"v_mul_f32 v50, v214, v50\n"
"v_mul_f32 v51, v214, v51\n"
"v_mul_f32 v52, v214, v52\n"
"v_mul_f32 v53, v214, v53\n"
"v_mul_f32 v54, v214, v54\n"
"v_mul_f32 v55, v214, v55\n"
"v_cvt_pk_bf16_f32 v120, v48, v49\n"
"v_cvt_pk_bf16_f32 v121, v50, v51\n"
"v_cvt_pk_bf16_f32 v122, v52, v53\n"
"v_cvt_pk_bf16_f32 v123, v54, v55\n"
"buffer_store_dwordx4 v[120:123], v255, s[36:39], s44 offen offset:2048 sc1\n"
"s_sleep 5\n"
"v_accvgpr_read_b32 v56, a108\n"
"v_accvgpr_read_b32 v57, a109\n"
"v_accvgpr_read_b32 v58, a110\n"
"v_accvgpr_read_b32 v59, a111\n"
"v_accvgpr_read_b32 v60, a124\n"
"v_accvgpr_read_b32 v61, a125\n"
"v_accvgpr_read_b32 v62, a126\n"
"v_accvgpr_read_b32 v63, a127\n"
"v_mul_f32 v56, v215, v56\n"
"v_mul_f32 v57, v215, v57\n"
"v_mul_f32 v58, v215, v58\n"
"v_mul_f32 v59, v215, v59\n"
"v_mul_f32 v60, v215, v60\n"
"v_mul_f32 v61, v215, v61\n"
"v_mul_f32 v62, v215, v62\n"
"v_mul_f32 v63, v215, v63\n"
"v_cvt_pk_bf16_f32 v124, v56, v57\n"
"v_cvt_pk_bf16_f32 v125, v58, v59\n"
"v_cvt_pk_bf16_f32 v126, v60, v61\n"
"v_cvt_pk_bf16_f32 v127, v62, v63\n"
"buffer_store_dwordx4 v[124:127], v255, s[36:39], s44 offen offset:3072 sc1\n"
"s_sleep 5\n"
"s_add_u32 s44, s44, 8192\n"
"s_waitcnt vmcnt(14)\n"
"v_cmp_ne_u32 vcc, s73, v220\n"
"s_nop 0\n"
"s_cbranch_vccnz .L_f1b_retry\n"
".L_f1b_ok:\n"
"s_waitcnt vmcnt(0) lgkmcnt(0)\n"
"s_barrier\n"
"s_lshl_b32 s90, s75, 6\n"
"s_add_u32 s90, s90, 2686976\n"
"v_mov_b32 v218, s73\n"
"buffer_store_dword v218, v225, s[36:39], s90 offen sc1\n"
"s_lshl1_add_u32 s90, s69, s45\n"
"s_lshl_b32 s90, s90, 20\n"
"s_lshl_b32 s91, s71, 18\n"
"s_add_u32 s90, s90, s91\n"
"s_add_u32 s40, s90, 41943040\n"
"s_mov_b32 s82, 0\n"
"s_lshr_b32 s91, s71, 1\n"
"s_lshl2_add_u32 s91, s91, s68\n"
"s_lshl_b32 s91, s91, 10\n"
"v_add_u32 v240, s91, v255\n"
"v_xor_b32 v241, 4096, v240\n"
"s_and_b32 s91, s71, 1\n"
"s_lshl1_add_u32 s91, s46, s91\n"
"s_lshl3_add_u32 s91, s68, s91\n"
"s_mul_i32 s83, s91, 5120\n"
"s_lshr_b32 s90, s68, 1\n"
"s_mul_i32 s91, s90, 40960\n"
"s_xor_b32 s92, s68, s71\n"
"s_and_b32 s92, s92, 1\n"
"s_lshl1_add_u32 s93, s46, s92\n"
"s_mul_i32 s93, s93, 5120\n"
"s_add_u32 s91, s91, s93\n"
"s_mul_i32 s93, s71, 1310720\n"
"s_add_u32 s93, s93, s91\n"
"s_add_u32 s41, s93, 3145728\n"
"s_mul_i32 s43, s68, 5120\n"
"v_add_u32 v236, 4096, v255\n"
"v_lshrrev_b32 v237, 1, v255\n"
"v_add_u32 v242, 61440, v255\n"
"v_add_u32 v243, 122880, v255\n"
"v_and_b32 v221, 7, v229\n"
"v_lshlrev_b32 v221, 6, v221\n"
"s_and_b32 s95, s75, 0xf8\n"
"s_lshl_b32 s95, s95, 6\n"
"s_add_u32 s95, s95, 2686976\n"
"s_add_u32 m0, s43, 0\n"
"s_nop 0\n"
"buffer_load_dwordx4 v255, s[36:39], s41 offen offset:0 sc1 lds\n"
"buffer_load_dwordx4 v255, s[36:39], s41 offen offset:1024 sc1 lds\n"
"buffer_load_dwordx4 v255, s[36:39], s41 offen offset:2048 sc1 lds\n"
"buffer_load_dwordx4 v255, s[36:39], s41 offen offset:3072 sc1 lds\n"
"s_add_u32 m0, s43, 4096\n"
"s_nop 0\n"
"buffer_load_dwordx4 v236, s[36:39], s41 offen sc1 lds\n"
"s_add_u32 s41, s41, 81920\n"
"s_add_u32 m0, s43, 61440\n"
"s_nop 0\n"
"buffer_load_dwordx4 v255, s[36:39], s41 offen offset:0 sc1 lds\n"
"buffer_load_dwordx4 v255, s[36:39], s41 offen offset:1024 sc1 lds\n"
"buffer_load_dwordx4 v255, s[36:39], s41 offen offset:2048 sc1 lds\n"
"buffer_load_dwordx4 v255, s[36:39], s41 offen offset:3072 sc1 lds\n"
"s_add_u32 m0, s43, 65536\n"
"s_nop 0\n"
"buffer_load_dwordx4 v236, s[36:39], s41 offen sc1 lds\n"
"s_add_u32 s41, s41, 81920\n"
"s_add_u32 m0, s43, 81920\n"
"s_nop 0\n"
"buffer_load_dwordx4 v255, s[36:39], s41 offen offset:0 sc1 lds\n"
"buffer_load_dwordx4 v255, s[36:39], s41 offen offset:1024 sc1 lds\n"
"buffer_load_dwordx4 v255, s[36:39], s41 offen offset:2048 sc1 lds\n"
"buffer_load_dwordx4 v255, s[36:39], s41 offen offset:3072 sc1 lds\n"
"s_add_u32 m0, s43, 86016\n"
"s_nop 0\n"
"buffer_load_dwordx4 v236, s[36:39], s41 offen sc1 lds\n"
"s_add_u32 s41, s41, 81920\n"
"s_add_u32 m0, s43, 102400\n"
"s_nop 0\n"
"buffer_load_dwordx4 v255, s[36:39], s41 offen offset:0 sc1 lds\n"
"buffer_load_dwordx4 v255, s[36:39], s41 offen offset:1024 sc1 lds\n"
"buffer_load_dwordx4 v255, s[36:39], s41 offen offset:2048 sc1 lds\n"
"buffer_load_dwordx4 v255, s[36:39], s41 offen offset:3072 sc1 lds\n"
"s_add_u32 m0, s43, 106496\n"
"s_nop 0\n"
"buffer_load_dwordx4 v236, s[36:39], s41 offen sc1 lds\n"
"s_add_u32 s41, s41, 81920\n"
"s_add_u32 m0, s43, 20480\n"
"s_nop 0\n"
"buffer_load_dwordx4 v255, s[36:39], s41 offen offset:0 sc1 lds\n"
"buffer_load_dwordx4 v255, s[36:39], s41 offen offset:1024 sc1 lds\n"
"buffer_load_dwordx4 v255, s[36:39], s41 offen offset:2048 sc1 lds\n"
"buffer_load_dwordx4 v255, s[36:39], s41 offen offset:3072 sc1 lds\n"
"s_add_u32 m0, s43, 24576\n"
"s_nop 0\n"
"buffer_load_dwordx4 v236, s[36:39], s41 offen sc1 lds\n"
"s_add_u32 s41, s41, 81920\n"
"s_add_u32 m0, s43, 40960\n"
"s_nop 0\n"
"buffer_load_dwordx4 v255, s[36:39], s41 offen offset:0 sc1 lds\n"
"buffer_load_dwordx4 v255, s[36:39], s41 offen offset:1024 sc1 lds\n"
"buffer_load_dwordx4 v255, s[36:39], s41 offen offset:2048 sc1 lds\n"
"buffer_load_dwordx4 v255, s[36:39], s41 offen offset:3072 sc1 lds\n"
"s_add_u32 m0, s43, 45056\n"
"s_nop 0\n"
"buffer_load_dwordx4 v236, s[36:39], s41 offen sc1 lds\n"
"s_add_u32 s41, s41, 81920\n"
"s_sleep 16\n"
"buffer_load_dword v220, v221, s[36:39], s95 offen sc1\n"
"s_mov_b32 s85, 67108864\n"
"s_waitcnt vmcnt(0)\n"
"v_cmp_ne_u32 vcc, s73, v220\n"
"s_nop 0\n"
"s_cbranch_vccnz .L_fo2_retry\n"
".L_fo2_ok:\n"
"s_add_u32 s42, s40, 8192\n"
"buffer_load_dwordx4 a[144:147], v240, s[36:39], s40 offen sc1\n"
"buffer_load_dwordx4 a[148:151], v241, s[36:39], s40 offen sc1\n"
"buffer_load_dwordx4 a[152:155], v240, s[36:39], s42 offen sc1\n"
"buffer_load_dwordx4 a[156:159], v241, s[36:39], s42 offen sc1\n"
"s_add_u32 s40, s40, 16384\n"
"s_add_u32 s42, s40, 8192\n"
"buffer_load_dwordx4 a[160:163], v240, s[36:39], s40 offen sc1\n"
"buffer_load_dwordx4 a[164:167], v241, s[36:39], s40 offen sc1\n"
"buffer_load_dwordx4 a[168:171], v240, s[36:39], s42 offen sc1\n"
"buffer_load_dwordx4 a[172:175], v241, s[36:39], s42 offen sc1\n"
"s_add_u32 s40, s40, 16384\n"
"s_add_u32 s42, s40, 8192\n"
"buffer_load_dwordx4 a[176:179], v240, s[36:39], s40 offen sc1\n"
"buffer_load_dwordx4 a[180:183], v241, s[36:39], s40 offen sc1\n"
"buffer_load_dwordx4 a[184:187], v240, s[36:39], s42 offen sc1\n"
"buffer_load_dwordx4 a[188:191], v241, s[36:39], s42 offen sc1\n"
"s_add_u32 s40, s40, 16384\n"
"s_nop 0\n"
"s_barrier\n"
"ds_read_b128 a[32:35], v255 offset:0\n"
"ds_read_b128 a[36:39], v255 offset:10240\n"
"ds_read_b128 a[40:43], v255 offset:1024\n"
"ds_read_b128 a[44:47], v255 offset:11264\n"
"ds_read_b128 a[48:51], v255 offset:2048\n"
"ds_read_b128 a[52:55], v255 offset:12288\n"
"ds_read_b128 a[56:59], v255 offset:3072\n"
"ds_read_b128 a[60:63], v255 offset:13312\n"
"ds_read_b128 a[64:67], v255 offset:4096\n"
"ds_read_b128 a[68:71], v255 offset:14336\n"
"ds_read_b128 a[72:75], v255 offset:5120\n"
"ds_read_b128 a[76:79], v255 offset:15360\n"
"ds_read_b128 a[80:83], v255 offset:6144\n"
"ds_read_b128 a[84:87], v255 offset:16384\n"
"ds_read_b128 a[88:91], v255 offset:7168\n"
"ds_read_b128 a[92:95], v255 offset:17408\n"
"ds_read_b128 a[96:99], v255 offset:8192\n"
"ds_read_b128 a[100:103], v255 offset:18432\n"
"ds_read_b128 a[104:107], v255 offset:9216\n"
"ds_read_b128 a[108:111], v255 offset:19456\n"
"s_branch .L_e3\n"
".L_f1b_retry:\n"
"s_mov_b32 s34, 0\n"
".L_f1b_spin:\n"
"s_sleep 24\n"
"buffer_load_dword v220, v255, s[36:39], s95 offen sc1\n"
"s_waitcnt vmcnt(0)\n"
"v_cmp_ne_u32 vcc, s73, v220\n"
"s_nop 1\n"
"s_cbranch_vccz .L_f1b_ok\n"
"s_add_u32 s34, s34, 1\n"
"s_cmp_lt_u32 s34, 100000\n"
"s_cbranch_scc1 .L_f1b_spin\n"
"s_branch .L_f1b_ok\n"
".L_fo2_retry:\n"
"s_mov_b32 s34, 0\n"
".L_fo2_spin:\n"
"s_sleep 10\n"
"buffer_load_dword v220, v221, s[36:39], s95 offen sc1\n"
"s_waitcnt vmcnt(0)\n"
"v_cmp_ne_u32 vcc, s73, v220\n"
"s_nop 1\n"
"s_cbranch_vccz .L_fo2_ok\n"
"s_add_u32 s34, s34, 1\n"
"s_cmp_lt_u32 s34, 100000\n"
"s_cbranch_scc1 .L_fo2_spin\n"
"s_branch .L_fo2_ok\n"
".L_fh_retry:\n"
"s_mov_b32 s34, 0\n"
".L_fh_spin:\n"
"s_sleep 4\n"
"buffer_load_dword v220, v221, s[36:39], s95 offen sc1\n"
"s_waitcnt vmcnt(0)\n"
"v_cmp_ne_u32 vcc, s73, v220\n"
"s_nop 1\n"
"s_cbranch_vccz .L_fh_ok\n"
"s_add_u32 s34, s34, 1\n"
"s_cmp_lt_u32 s34, 100000\n"
"s_cbranch_scc1 .L_fh_spin\n"
"s_branch .L_fh_ok\n"
".L_ps_retry:\n"
"s_mov_b32 s34, 0\n"
".L_ps_spin:\n"
"s_sleep 4\n"
"buffer_load_dword v220, v221, s[36:39], s95 offen sc1\n"
"s_waitcnt vmcnt(0)\n"
"v_cmp_ne_u32 vcc, s73, v220\n"
"s_nop 1\n"
"s_cbranch_vccz .L_ps_ok\n"
"s_add_u32 s34, s34, 1\n"
"s_cmp_lt_u32 s34, 100000\n"
"s_cbranch_scc1 .L_ps_spin\n"
"s_branch .L_ps_ok\n"
".L_fallback:\n"
"s_waitcnt vmcnt(0) lgkmcnt(0)\n"
"s_barrier\n"
"s_mov_b32 s89, -1\n"
"s_load_dwordx16 s[40:55], %1, 0\n"
"s_branch .L_redo\n"
".L_maxpass:\n"
"s_waitcnt vmcnt(0) lgkmcnt(0)\n"
"s_barrier\n"
"v_mov_b32 v212, 0xff800000\n"
"v_mov_b32 v213, 0xff800000\n"
"v_mov_b32 v214, 0xff800000\n"
"v_mov_b32 v215, 0xff800000\n"
"s_lshl_b32 s82, s68, 12\n"
"s_mov_b32 s86, 8\n"
".L_fbloop:\n"
"s_add_u32 m0, s84, 0\n"
"s_nop 0\n"
"buffer_load_dwordx4 v236, s[24:27], s82 offen offset:0 lds\n"
"buffer_load_dwordx4 v237, s[24:27], s82 offen offset:1024 lds\n"
"buffer_load_dwordx4 v238, s[24:27], s82 offen offset:2048 lds\n"
"buffer_load_dwordx4 v239, s[24:27], s82 offen offset:3072 lds\n"
"s_add_u32 s82, s82, 16384\n"
"s_and_b32 s82, s82, 0x1ffff\n"
"s_waitcnt vmcnt(0)\n"
"s_barrier\n"
"ds_read_b128 a[208:211], v240 offset:0\n"
"ds_read_b128 a[212:215], v241 offset:0\n"
"ds_read_b128 a[216:219], v242 offset:0\n"
"ds_read_b128 a[220:223], v243 offset:0\n"
"s_waitcnt lgkmcnt(0)\n"
"v_mfma_f32_16x16x32_bf16 v[0:3], a[208:211], a[144:147], v[192:195]\n"
"v_mfma_f32_16x16x32_bf16 v[0:3], a[212:215], a[148:151], v[0:3]\n"
"v_mfma_f32_16x16x32_bf16 v[4:7], a[212:215], a[164:167], v[196:199]\n"
"v_mfma_f32_16x16x32_bf16 v[4:7], a[208:211], a[160:163], v[4:7]\n"
"v_mfma_f32_16x16x32_bf16 v[8:11], a[208:211], a[176:179], v[200:203]\n"
"v_mfma_f32_16x16x32_bf16 v[8:11], a[212:215], a[180:183], v[8:11]\n"
"v_mfma_f32_16x16x32_bf16 v[12:15], a[212:215], a[196:199], v[204:207]\n"
"v_mfma_f32_16x16x32_bf16 v[12:15], a[208:211], a[192:195], v[12:15]\n"
"v_mfma_f32_16x16x32_bf16 v[0:3], a[216:219], a[152:155], v[0:3]\n"
"v_mfma_f32_16x16x32_bf16 v[0:3], a[220:223], a[156:159], v[0:3]\n"
"v_mfma_f32_16x16x32_bf16 v[4:7], a[220:223], a[172:175], v[4:7]\n"
"v_mfma_f32_16x16x32_bf16 v[4:7], a[216:219], a[168:171], v[4:7]\n"
"v_mfma_f32_16x16x32_bf16 v[8:11], a[216:219], a[184:187], v[8:11]\n"
"v_mfma_f32_16x16x32_bf16 v[8:11], a[220:223], a[188:191], v[8:11]\n"
"v_mfma_f32_16x16x32_bf16 v[12:15], a[220:223], a[204:207], v[12:15]\n"
"v_mfma_f32_16x16x32_bf16 v[12:15], a[216:219], a[200:203], v[12:15]\n"
"ds_read_b128 a[208:211], v240 offset:4096\n"
"ds_read_b128 a[212:215], v241 offset:4096\n"
"ds_read_b128 a[216:219], v242 offset:4096\n"
"ds_read_b128 a[220:223], v243 offset:4096\n"
"s_waitcnt lgkmcnt(0)\n"
"v_mfma_f32_16x16x32_bf16 v[16:19], a[208:211], a[144:147], v[192:195]\n"
"v_mfma_f32_16x16x32_bf16 v[16:19], a[212:215], a[148:151], v[16:19]\n"
"v_mfma_f32_16x16x32_bf16 v[20:23], a[212:215], a[164:167], v[196:199]\n"
"v_mfma_f32_16x16x32_bf16 v[20:23], a[208:211], a[160:163], v[20:23]\n"
"v_mfma_f32_16x16x32_bf16 v[24:27], a[208:211], a[176:179], v[200:203]\n"
"v_mfma_f32_16x16x32_bf16 v[24:27], a[212:215], a[180:183], v[24:27]\n"
"v_mfma_f32_16x16x32_bf16 v[28:31], a[212:215], a[196:199], v[204:207]\n"
"v_mfma_f32_16x16x32_bf16 v[28:31], a[208:211], a[192:195], v[28:31]\n"
"v_mfma_f32_16x16x32_bf16 v[16:19], a[216:219], a[152:155], v[16:19]\n"
"v_mfma_f32_16x16x32_bf16 v[16:19], a[220:223], a[156:159], v[16:19]\n"
"v_mfma_f32_16x16x32_bf16 v[20:23], a[220:223], a[172:175], v[20:23]\n"
"v_mfma_f32_16x16x32_bf16 v[20:23], a[216:219], a[168:171], v[20:23]\n"
"v_mfma_f32_16x16x32_bf16 v[24:27], a[216:219], a[184:187], v[24:27]\n"
"v_mfma_f32_16x16x32_bf16 v[24:27], a[220:223], a[188:191], v[24:27]\n"
"v_mfma_f32_16x16x32_bf16 v[28:31], a[220:223], a[204:207], v[28:31]\n"
"v_mfma_f32_16x16x32_bf16 v[28:31], a[216:219], a[200:203], v[28:31]\n"
"ds_read_b128 a[208:211], v240 offset:8192\n"
"ds_read_b128 a[212:215], v241 offset:8192\n"
"ds_read_b128 a[216:219], v242 offset:8192\n"
"ds_read_b128 a[220:223], v243 offset:8192\n"
"s_waitcnt lgkmcnt(0)\n"
"v_mfma_f32_16x16x32_bf16 v[32:35], a[208:211], a[144:147], v[192:195]\n"
"v_mfma_f32_16x16x32_bf16 v[32:35], a[212:215], a[148:151], v[32:35]\n"
"v_mfma_f32_16x16x32_bf16 v[36:39], a[212:215], a[164:167], v[196:199]\n"
"v_mfma_f32_16x16x32_bf16 v[36:39], a[208:211], a[160:163], v[36:39]\n"
"v_mfma_f32_16x16x32_bf16 v[40:43], a[208:211], a[176:179], v[200:203]\n"
"v_mfma_f32_16x16x32_bf16 v[40:43], a[212:215], a[180:183], v[40:43]\n"
"v_mfma_f32_16x16x32_bf16 v[44:47], a[212:215], a[196:199], v[204:207]\n"
"v_mfma_f32_16x16x32_bf16 v[44:47], a[208:211], a[192:195], v[44:47]\n"
"v_mfma_f32_16x16x32_bf16 v[32:35], a[216:219], a[152:155], v[32:35]\n"
"v_mfma_f32_16x16x32_bf16 v[32:35], a[220:223], a[156:159], v[32:35]\n"
"v_mfma_f32_16x16x32_bf16 v[36:39], a[220:223], a[172:175], v[36:39]\n"
"v_mfma_f32_16x16x32_bf16 v[36:39], a[216:219], a[168:171], v[36:39]\n"
"v_mfma_f32_16x16x32_bf16 v[40:43], a[216:219], a[184:187], v[40:43]\n"
"v_mfma_f32_16x16x32_bf16 v[40:43], a[220:223], a[188:191], v[40:43]\n"
"v_mfma_f32_16x16x32_bf16 v[44:47], a[220:223], a[204:207], v[44:47]\n"
"v_mfma_f32_16x16x32_bf16 v[44:47], a[216:219], a[200:203], v[44:47]\n"
"ds_read_b128 a[208:211], v240 offset:12288\n"
"ds_read_b128 a[212:215], v241 offset:12288\n"
"ds_read_b128 a[216:219], v242 offset:12288\n"
"ds_read_b128 a[220:223], v243 offset:12288\n"
"s_waitcnt lgkmcnt(0)\n"
"v_mfma_f32_16x16x32_bf16 v[48:51], a[208:211], a[144:147], v[192:195]\n"
"v_mfma_f32_16x16x32_bf16 v[48:51], a[212:215], a[148:151], v[48:51]\n"
"v_mfma_f32_16x16x32_bf16 v[52:55], a[212:215], a[164:167], v[196:199]\n"
"v_mfma_f32_16x16x32_bf16 v[52:55], a[208:211], a[160:163], v[52:55]\n"
"v_mfma_f32_16x16x32_bf16 v[56:59], a[208:211], a[176:179], v[200:203]\n"
"v_mfma_f32_16x16x32_bf16 v[56:59], a[212:215], a[180:183], v[56:59]\n"
"v_mfma_f32_16x16x32_bf16 v[60:63], a[212:215], a[196:199], v[204:207]\n"
"v_mfma_f32_16x16x32_bf16 v[60:63], a[208:211], a[192:195], v[60:63]\n"
"v_mfma_f32_16x16x32_bf16 v[48:51], a[216:219], a[152:155], v[48:51]\n"
"v_mfma_f32_16x16x32_bf16 v[48:51], a[220:223], a[156:159], v[48:51]\n"
"v_mfma_f32_16x16x32_bf16 v[52:55], a[220:223], a[172:175], v[52:55]\n"
"v_mfma_f32_16x16x32_bf16 v[52:55], a[216:219], a[168:171], v[52:55]\n"
"v_mfma_f32_16x16x32_bf16 v[56:59], a[216:219], a[184:187], v[56:59]\n"
"v_mfma_f32_16x16x32_bf16 v[56:59], a[220:223], a[188:191], v[56:59]\n"
"v_mfma_f32_16x16x32_bf16 v[60:63], a[220:223], a[204:207], v[60:63]\n"
"v_mfma_f32_16x16x32_bf16 v[60:63], a[216:219], a[200:203], v[60:63]\n"
"s_barrier\n"
"v_max3_f32 v212, v212, v0, v1\n"
"v_max3_f32 v212, v212, v2, v3\n"
"v_max3_f32 v213, v213, v4, v5\n"
"v_max3_f32 v213, v213, v6, v7\n"
"v_max3_f32 v214, v214, v8, v9\n"
"v_max3_f32 v214, v214, v10, v11\n"
"v_max3_f32 v215, v215, v12, v13\n"
"v_max3_f32 v215, v215, v14, v15\n"
"v_max3_f32 v212, v212, v16, v17\n"
"v_max3_f32 v212, v212, v18, v19\n"
"v_max3_f32 v213, v213, v20, v21\n"
"v_max3_f32 v213, v213, v22, v23\n"
"v_max3_f32 v214, v214, v24, v25\n"
"v_max3_f32 v214, v214, v26, v27\n"
"v_max3_f32 v215, v215, v28, v29\n"
"v_max3_f32 v215, v215, v30, v31\n"
"v_max3_f32 v212, v212, v32, v33\n"
"v_max3_f32 v212, v212, v34, v35\n"
"v_max3_f32 v213, v213, v36, v37\n"
"v_max3_f32 v213, v213, v38, v39\n"
"v_max3_f32 v214, v214, v40, v41\n"
"v_max3_f32 v214, v214, v42, v43\n"
"v_max3_f32 v215, v215, v44, v45\n"
"v_max3_f32 v215, v215, v46, v47\n"
"v_max3_f32 v212, v212, v48, v49\n"
"v_max3_f32 v212, v212, v50, v51\n"
"v_max3_f32 v213, v213, v52, v53\n"
"v_max3_f32 v213, v213, v54, v55\n"
"v_max3_f32 v214, v214, v56, v57\n"
"v_max3_f32 v214, v214, v58, v59\n"
"v_max3_f32 v215, v215, v60, v61\n"
"v_max3_f32 v215, v215, v62, v63\n"
"s_sub_u32 s86, s86, 1\n"
"s_cmp_lg_u32 s86, 0\n"
"s_cbranch_scc1 .L_fbloop\n"
"v_mov_b32 v216, v212\n"
"s_nop 1\n"
"v_permlane16_swap_b32 v216, v212\n"
"v_max_f32 v212, v212, v216\n"
"v_mov_b32 v216, v213\n"
"s_nop 1\n"
"v_permlane16_swap_b32 v216, v213\n"
"v_max_f32 v213, v213, v216\n"
"v_mov_b32 v216, v214\n"
"s_nop 1\n"
"v_permlane16_swap_b32 v216, v214\n"
"v_max_f32 v214, v214, v216\n"
"v_mov_b32 v216, v215\n"
"s_nop 1\n"
"v_permlane16_swap_b32 v216, v215\n"
"v_max_f32 v215, v215, v216\n"
"v_mov_b32 v216, v212\n"
"s_nop 1\n"
"v_permlane32_swap_b32 v216, v212\n"
"v_max_f32 v212, v212, v216\n"
"v_mov_b32 v216, v213\n"
"s_nop 1\n"
"v_permlane32_swap_b32 v216, v213\n"
"v_max_f32 v213, v213, v216\n"
"v_mov_b32 v216, v214\n"
"s_nop 1\n"
"v_permlane32_swap_b32 v216, v214\n"
"v_max_f32 v214, v214, v216\n"
"v_mov_b32 v216, v215\n"
"s_nop 1\n"
"v_permlane32_swap_b32 v216, v215\n"
"v_max_f32 v215, v215, v216\n"
"v_sub_f32 v192, 0, v212\n"
"v_sub_f32 v193, 0, v212\n"
"v_sub_f32 v194, 0, v212\n"
"v_sub_f32 v195, 0, v212\n"
"v_sub_f32 v196, 0, v213\n"
"v_sub_f32 v197, 0, v213\n"
"v_sub_f32 v198, 0, v213\n"
"v_sub_f32 v199, 0, v213\n"
"v_sub_f32 v200, 0, v214\n"
"v_sub_f32 v201, 0, v214\n"
"v_sub_f32 v202, 0, v214\n"
"v_sub_f32 v203, 0, v214\n"
"v_sub_f32 v204, 0, v215\n"
"v_sub_f32 v205, 0, v215\n"
"v_sub_f32 v206, 0, v215\n"
"v_sub_f32 v207, 0, v215\n"
"s_lshl_b32 s82, s68, 12\n"
"s_mov_b32 s83, s82\n"
"s_add_u32 m0, s84, 32768\n"
"s_nop 0\n"
"buffer_load_dwordx4 v236, s[24:27], s82 offen offset:0 lds\n"
"buffer_load_dwordx4 v237, s[24:27], s82 offen offset:1024 lds\n"
"buffer_load_dwordx4 v238, s[24:27], s82 offen offset:2048 lds\n"
"buffer_load_dwordx4 v239, s[24:27], s82 offen offset:3072 lds\n"
"s_add_u32 s82, s82, 16384\n"
"s_and_b32 s82, s82, 0x1ffff\n"
"s_add_u32 m0, s84, 16384\n"
"s_nop 0\n"
"buffer_load_dwordx4 v236, s[24:27], s82 offen offset:0 lds\n"
"buffer_load_dwordx4 v237, s[24:27], s82 offen offset:1024 lds\n"
"buffer_load_dwordx4 v238, s[24:27], s82 offen offset:2048 lds\n"
"buffer_load_dwordx4 v239, s[24:27], s82 offen offset:3072 lds\n"
"s_add_u32 s82, s82, 16384\n"
"s_and_b32 s82, s82, 0x1ffff\n"
"s_add_u32 m0, s84, 0\n"
"s_nop 0\n"
"buffer_load_dwordx4 v236, s[24:27], s82 offen offset:0 lds\n"
"buffer_load_dwordx4 v237, s[24:27], s82 offen offset:1024 lds\n"
"buffer_load_dwordx4 v238, s[24:27], s82 offen offset:2048 lds\n"
"buffer_load_dwordx4 v239, s[24:27], s82 offen offset:3072 lds\n"
"s_add_u32 s82, s82, 16384\n"
"s_and_b32 s82, s82, 0x1ffff\n"
"s_add_u32 m0, s85, 0\n"
"s_nop 0\n"
"buffer_load_dwordx4 v230, s[28:31], s83 offen offset:0 lds\n"
"buffer_load_dwordx4 v231, s[28:31], s83 offen offset:1024 lds\n"
"buffer_load_dwordx4 v230, s[28:31], s83 offen offset:2048 lds\n"
"buffer_load_dwordx4 v231, s[28:31], s83 offen offset:3072 lds\n"
"s_add_u32 s83, s83, 16384\n"
"s_and_b32 s83, s83, 0x1ffff\n"
"s_add_u32 m0, s85, 16384\n"
"s_nop 0\n"
"buffer_load_dwordx4 v230, s[28:31], s83 offen offset:0 lds\n"
"buffer_load_dwordx4 v231, s[28:31], s83 offen offset:1024 lds\n"
"buffer_load_dwordx4 v230, s[28:31], s83 offen offset:2048 lds\n"
"buffer_load_dwordx4 v231, s[28:31], s83 offen offset:3072 lds\n"
"s_add_u32 s83, s83, 16384\n"
"s_and_b32 s83, s83, 0x1ffff\n"
"s_waitcnt vmcnt(0)\n"
"s_barrier\n"
"ds_read_b128 a[208:211], v240 offset:32768\n"
"ds_read_b128 a[212:215], v241 offset:32768\n"
"ds_read_b128 a[216:219], v242 offset:32768\n"
"ds_read_b128 a[220:223], v243 offset:32768\n"
"s_branch .L_attn\n"
".L_end:\n"
    : "+v"(tid)
    : "s"(__builtin_amdgcn_kernarg_segment_ptr()), "s"(blockIdx.x)
    : "memory", "vcc", "scc", "v1","v2","v3","v4","v5","v6","v7","v8","v9","v10","v11","v12","v13","v14","v15","v16","v17","v18","v19","v20","v21","v22","v23","v24","v25","v26","v27","v28","v29","v30","v31","v32","v33","v34","v35","v36","v37","v38","v39","v40","v41","v42","v43","v44","v45","v46","v47","v48","v49","v50","v51","v52","v53","v54","v55","v56","v57","v58","v59","v60","v61","v62","v63","v64","v65","v66","v67","v68","v69","v70","v71","v72","v73","v74","v75","v76","v77","v78","v79","v80","v81","v82","v83","v84","v85","v86","v87","v88","v89","v90","v91","v92","v93","v94","v95","v96","v97","v98","v99","v100","v101","v102","v103","v104","v105","v106","v107","v108","v109","v110","v111","v112","v113","v114","v115","v116","v117","v118","v119","v120","v121","v122","v123","v124","v125","v126","v127","v128","v129","v130","v131","v132","v133","v134","v135","v136","v137","v138","v139","v140","v141","v142","v143","v144","v145","v146","v147","v148","v149","v150","v151","v152","v153","v154","v155","v156","v157","v158","v159","v160","v161","v162","v163","v164","v165","v166","v167","v168","v169","v170","v171","v172","v173","v174","v175","v176","v177","v178","v179","v180","v181","v182","v183","v184","v185","v186","v187","v188","v189","v190","v191","v192","v193","v194","v195","v196","v197","v198","v199","v200","v201","v202","v203","v204","v205","v206","v207","v208","v209","v210","v211","v212","v213","v214","v215","v216","v217","v218","v219","v220","v221","v222","v223","v224","v225","v226","v227","v228","v229","v230","v231","v232","v233","v234","v235","v236","v237","v238","v239","v240","v241","v242","v243","v244","v245","v246","v247","v248","v249","v250","v251","v252","v253","v254","v255","a0","a1","a2","a3","a4","a5","a6","a7","a8","a9","a10","a11","a12","a13","a14","a15","a16","a17","a18","a19","a20","a21","a22","a23","a24","a25","a26","a27","a28","a29","a30","a31","a32","a33","a34","a35","a36","a37","a38","a39","a40","a41","a42","a43","a44","a45","a46","a47","a48","a49","a50","a51","a52","a53","a54","a55","a56","a57","a58","a59","a60","a61","a62","a63","a64","a65","a66","a67","a68","a69","a70","a71","a72","a73","a74","a75","a76","a77","a78","a79","a80","a81","a82","a83","a84","a85","a86","a87","a88","a89","a90","a91","a92","a93","a94","a95","a96","a97","a98","a99","a100","a101","a102","a103","a104","a105","a106","a107","a108","a109","a110","a111","a112","a113","a114","a115","a116","a117","a118","a119","a120","a121","a122","a123","a124","a125","a126","a127","a128","a129","a130","a131","a132","a133","a134","a135","a136","a137","a138","a139","a140","a141","a142","a143","a144","a145","a146","a147","a148","a149","a150","a151","a152","a153","a154","a155","a156","a157","a158","a159","a160","a161","a162","a163","a164","a165","a166","a167","a168","a169","a170","a171","a172","a173","a174","a175","a176","a177","a178","a179","a180","a181","a182","a183","a184","a185","a186","a187","a188","a189","a190","a191","a192","a193","a194","a195","a196","a197","a198","a199","a200","a201","a202","a203","a204","a205","a206","a207","a208","a209","a210","a211","a212","a213","a214","a215","a216","a217","a218","a219","a220","a221","a222","a223","a224","a225","a226","a227","a228","a229","a230","a231","a232","a233","a234","a235","a236","a237","a238","a239","a240","a241","a242","a243","a244","a245","a246","a247","a248","a249","a250","a251","a252","a253","a254","a255","s8","s9","s10","s11","s12","s13","s14","s15","s16","s17","s18","s19","s20","s21","s22","s23","s24","s25","s26","s27","s28","s29","s30","s31","s33","s34","s35","s36","s37","s38","s39","s40","s41","s42","s43","s44","s45","s46","s47","s48","s49","s50","s51","s52","s53","s54","s55","s56","s57","s58","s59","s60","s61","s62","s63","s64","s65","s66","s67","s68","s69","s70","s71","s72","s73","s74","s75","s76","s77","s78","s79","s80","s81","s82","s83","s84","s85","s86","s87","s88","s89","s90","s91","s92","s93","s94","s95","s96","s97","s98","s99");
}


extern "C" void kernel_launch(void* const* d_in, const int* in_sizes, int n_in, void* d_out, int out_size, void* d_ws, size_t ws_size,
                              hipStream_t stream) {
  (void)hipFuncSetAttribute((const void*)fused_block_kernel, hipFuncAttributeMaxDynamicSharedMemorySize, 163840);

  hipLaunchKernelGGL(fused_block_kernel, dim3(256), dim3(256), 163840, stream, d_in[0], d_in[1], d_in[2], d_in[3], d_in[4], d_in[6], d_in[7],
                     d_ws, d_in[8], d_out);
}
```
